```python
import torch

def _softmax(x, axis=-1):
    return torch.softmax(x, dim=axis)
def _gelu(x):
    return torch.nn.functional.gelu(x, approximate="tanh")
def _silu(x):
    return torch.nn.functional.silu(x)
def _sigmoid(x):
    return torch.sigmoid(x)
def _layernorm(x, axis=-1, eps=1e-5):
    m = x.mean(dim=axis, keepdim=True)
    v = x.var(dim=axis, keepdim=True, unbiased=False)
    return (x - m) / torch.sqrt(v + eps)
def _scatter_add(tgt, idx, src, axis=0):
    return tgt.clone().index_add_(axis, idx, src)
def _segment_sum(src, seg, n):
    out = torch.zeros((n,) + src.shape[1:], dtype=src.dtype)
    return out.index_add_(0, seg, src)
def _pad(x, pw):
    flat = tuple(v for pair in reversed(pw) for v in pair)
    return torch.nn.functional.pad(x, flat)

kernel_name = 'concat_heads__gqa_block_cached__gqa_block_cached__near_ridge'

def setup_inputs():

    _w = (lambda a: (lambda t: t.to(torch.bfloat16) if t.is_floating_point() else t)(torch.as_tensor(a)))
    def _A_setup():
        B, Sq, D = 16, 128, 448
        Hq, Hkv, Sk, Dh = 16, 8, 1024, 256
        assert Hq % Hkv == 0, (Hq, Hkv)
        return {
            "x": _w((lambda *s: torch.randn(*s, dtype=torch.bfloat16) * 0.8)(B, Sq, D)),
            "k_cache": _w((lambda *s: torch.randn(*s, dtype=torch.bfloat16) * 0.8)(B, Hkv, Sk, Dh)),
            "v_cache": _w((lambda *s: torch.randn(*s, dtype=torch.bfloat16) * 0.05)(B, Hkv, Sk, Dh)),
            "wq": _w((lambda *s: torch.randn(*s, dtype=torch.bfloat16) * 0.8)(D, Hq * Dh) * (D ** -0.5)),
            "wo": _w((lambda *s: torch.randn(*s, dtype=torch.bfloat16) * 0.05)(Hq * Dh, D) * ((Hq * Dh) ** -0.5)),
        }
    _A_in = _A_setup()

    def _B_setup():
        B, Sq, D = 16, 128, 448
        Hq, Hkv, Sk, Dh = 16, 8, 1024, 256
        assert Hq % Hkv == 0, (Hq, Hkv)
        return {
            "x": _w((lambda *s: torch.randn(*s, dtype=torch.bfloat16) * 0.8)(B, Sq, D)),
            "k_cache": _w((lambda *s: torch.randn(*s, dtype=torch.bfloat16) * 0.8)(B, Hkv, Sk, Dh)),
            "v_cache": _w((lambda *s: torch.randn(*s, dtype=torch.bfloat16) * 0.05)(B, Hkv, Sk, Dh)),
            "wq": _w((lambda *s: torch.randn(*s, dtype=torch.bfloat16) * 0.8)(D, Hq * Dh) * (D ** -0.5)),
            "wo": _w((lambda *s: torch.randn(*s, dtype=torch.bfloat16) * 0.05)(Hq * Dh, D) * ((Hq * Dh) ** -0.5)),
        }
    _B_in = _B_setup()

    return {
        "A_x": _A_in["x"],
        "A_k_cache": _A_in["k_cache"],
        "A_v_cache": _A_in["v_cache"],
        "A_wq": _A_in["wq"],
        "A_wo": _A_in["wo"],
        "B_x": _B_in["x"],
        "B_k_cache": _B_in["k_cache"],
        "B_v_cache": _B_in["v_cache"],
        "B_wq": _B_in["wq"],
        "B_wo": _B_in["wo"],
    }

def reference(A_x, A_k_cache, A_v_cache, A_wq, A_wo, B_x, B_k_cache, B_v_cache, B_wq, B_wo):
    def _A_ref(x, k_cache, v_cache, wq, wo):
        B, Sq, D = x.shape
        Hq, Dh = 16, 256
        Hkv = k_cache.shape[1]
        rep = Hq // Hkv
        q = (lambda x, *d: x.permute(*d))((x @ wq).reshape(B, Sq, Hq, Dh), 0, 2, 1, 3)
        k = (lambda x, n, axis: torch.repeat_interleave(x, n, dim=axis))(k_cache, rep, axis=1)
        v = (lambda x, n, axis: torch.repeat_interleave(x, n, dim=axis))(v_cache, rep, axis=1)
        scores = torch.einsum("bhqd,bhkd->bhqk", q, k) * (Dh ** -0.5)
        p = _softmax(scores, axis=-1)
        o = torch.einsum("bhqk,bhkd->bhqd", p, v)
        return (lambda x, *d: x.permute(*d))(o, 0, 2, 1, 3).reshape(B, Sq, Hq * Dh) @ wo

    def _B_ref(x, k_cache, v_cache, wq, wo):
        B, Sq, D = x.shape
        Hq, Dh = 16, 256
        Hkv = k_cache.shape[1]
        rep = Hq // Hkv
        q = (lambda x, *d: x.permute(*d))((x @ wq).reshape(B, Sq, Hq, Dh), 0, 2, 1, 3)
        k = (lambda x, n, axis: torch.repeat_interleave(x, n, dim=axis))(k_cache, rep, axis=1)
        v = (lambda x, n, axis: torch.repeat_interleave(x, n, dim=axis))(v_cache, rep, axis=1)
        scores = torch.einsum("bhqd,bhkd->bhqk", q, k) * (Dh ** -0.5)
        p = _softmax(scores, axis=-1)
        o = torch.einsum("bhqk,bhkd->bhqd", p, v)
        return (lambda x, *d: x.permute(*d))(o, 0, 2, 1, 3).reshape(B, Sq, Hq * Dh) @ wo

    a = _A_ref(A_x, A_k_cache, A_v_cache, A_wq, A_wo)
    b = _B_ref(B_x, B_k_cache, B_v_cache, B_wq, B_wo)
    return torch.concatenate([a, b], axis=-1)
```

```cpp
#include <hip/hip_runtime.h>
#include <hip/hip_bf16.h>
typedef unsigned short u16;
__device__ inline float b2f(u16 x){ return __uint_as_float(((unsigned)x)<<16); }
__device__ inline u16 f2b(float f){ unsigned u=__float_as_uint(f); u += 0x7fffu + ((u>>16)&1u); return (u16)(u>>16); }
__global__ void qproj(const u16* x, const u16* wq, u16* q){
  int n = blockIdx.x*256+threadIdx.x; int row = blockIdx.y;
  float a=0; for(int c=0;c<448;c++) a += b2f(x[row*448+c])*b2f(wq[c*4096+n]);
  q[row*4096+n]=f2b(a);
}
__global__ void attn(const u16* q, const u16* k, const u16* v, u16* o){
  int s = blockIdx.x, h = blockIdx.y, b = blockIdx.z; int t = threadIdx.x;
  __shared__ float qs[256]; __shared__ float p[1024]; __shared__ float red[256];
  qs[t] = b2f(q[(b*128+s)*4096 + h*256 + t]); __syncthreads();
  const u16* kb = k + (size_t)(b*8 + h/2)*1024*256; const u16* vb = v + (size_t)(b*8 + h/2)*1024*256;
  float mx=-1e30f;
  for(int j=t;j<1024;j+=256){ float a=0; for(int d=0;d<256;d++) a+=qs[d]*b2f(kb[j*256+d]); a*=0.0625f; p[j]=a; mx=fmaxf(mx,a);}
  red[t]=mx; __syncthreads(); for(int o2=128;o2>0;o2>>=1){ if(t<o2) red[t]=fmaxf(red[t],red[t+o2]); __syncthreads(); }
  mx=red[0]; __syncthreads(); float sm=0;
  for(int j=t;j<1024;j+=256){ float e=__expf(p[j]-mx); p[j]=e; sm+=e; }
  red[t]=sm; __syncthreads(); for(int o2=128;o2>0;o2>>=1){ if(t<o2) red[t]+=red[t+o2]; __syncthreads(); }
  sm=red[0];
  float a=0; for(int j=0;j<1024;j++) a+=p[j]*b2f(vb[j*256+t]);
  o[(b*128+s)*4096+h*256+t]=f2b(a/sm);
}
__global__ void oproj(const u16* o, const u16* wo, u16* out, int coff){
  int n = threadIdx.x; int row = blockIdx.x; if(n>=448) return;
  float a=0; for(int c=0;c<4096;c++) a += b2f(o[row*4096+c])*b2f(wo[c*448+n]);
  out[row*896+coff+n]=f2b(a);
}
extern "C" void kernel_launch(void* const* d_in, const int* in_sizes, int n_in, void* d_out, int out_size, void* d_ws, size_t ws_size, hipStream_t stream){
  for(int br=0;br<2;br++){
    const u16* x=(const u16*)d_in[br*5+0]; const u16* k=(const u16*)d_in[br*5+1]; const u16* v=(const u16*)d_in[br*5+2];
    const u16* wq=(const u16*)d_in[br*5+3]; const u16* wo=(const u16*)d_in[br*5+4];
    u16* q=(u16*)d_ws; u16* o=q+2048*4096;
    qproj<<<dim3(16,2048),256,0,stream>>>(x,wq,q);
    attn<<<dim3(128,16,16),256,0,stream>>>(q,k,v,o);
    oproj<<<2048,448,0,stream>>>(o,wo,(u16*)d_out,br*448);
  }
}
```

```cpp
#include <hip/hip_runtime.h>
#include <hip/hip_bf16.h>

__global__ __launch_bounds__(256, 1) void gqa_fused_kernel(
    const char* xA, const char* kA, const char* vA, const char* wqA, const char* woA,
    const char* xB, const char* kB, const char* vB, const char* wqB, const char* woB, char* ws, char* out) {
  __shared__ __attribute__((aligned(16))) char smem[163840];
  const unsigned wg = blockIdx.x;
  const unsigned wave = __builtin_amdgcn_readfirstlane(threadIdx.x >> 6);
  const auto ka = __builtin_amdgcn_kernarg_segment_ptr();
  const unsigned ldsb = (unsigned)(size_t)smem;
  asm volatile(
"s_bfe_u32 s16, %[wg], 0x10002\n"
"s_mul_i32 s87, s16, 40\n"
"s_load_dwordx2 s[36:37], %[ka], s87\n"
"s_add_u32 s86, s87, 24\n"
"s_load_dwordx2 s[40:41], %[ka], s86\n"
"s_add_u32 s86, s87, 8\n"
"s_load_dwordx2 s[44:45], %[ka], s86\n"
"s_add_u32 s86, s87, 16\n"
"s_load_dwordx2 s[48:49], %[ka], s86\n"
"s_add_u32 s86, s87, 32\n"
"s_load_dwordx2 s[52:53], %[ka], s86\n"
"s_load_dwordx2 s[56:57], %[ka], 80\n"
"s_load_dwordx2 s[60:61], %[ka], 88\n"
"s_and_b32 s34, %[wg], 1\n"
"s_lshl_b32 s34, s34, 2\n"
"s_bfe_u32 s17, %[wg], 0x20003\n"
"s_or_b32 s34, s34, s17\n"
"s_bfe_u32 s17, %[wg], 0x10001\n"
"s_lshl_b32 s17, s17, 3\n"
"s_lshr_b32 s35, %[wg], 5\n"
"s_add_u32 s17, s17, s35\n"
"s_lshr_b32 s18, %[wg], 3\n"
"s_mul_i32 s19, s17, 114688\n"
"s_lshl_b32 s20, s34, 10\n"
"s_mov_b32 s80, %[wave]\n"
"v_mbcnt_lo_u32_b32 v227, -1, 0\n"
"v_mbcnt_hi_u32_b32 v227, -1, v227\n"
"s_getpc_b64 s[78:79]\n"
"Lcodepc%=:\n"
"s_lshl_b32 s87, s18, 2\n"
"s_add_u32 s87, s87, s80\n"
"v_min_u32 v225, 4, v227\n"
"v_lshl_add_u32 v225, v225, 7, s87\n"
"v_lshlrev_b32 v225, 7, v225\n"
"v_min_u32 v245, Lcodeend%=-Lcodepc%=-4, v225\n"
"global_load_dword v249, v245, s[78:79]\n"
"v_lshrrev_b32 v224, 4, v227\n"
"v_lshlrev_b32 v224, 1, v224\n"
"v_and_b32 v225, 7, v227\n"
"v_xor_b32 v224, v224, v225\n"
"v_lshlrev_b32 v224, 4, v224\n"
"v_lshrrev_b32 v226, 3, v227\n"
"v_lshl_add_u32 v225, s80, 5, v226\n"
"v_mul_u32_u24 v225, 896, v225\n"
"v_add_u32 v229, v224, v225\n"
"s_lshl_b32 s95, s80, 10\n"
"s_mov_b32 s92, 7168\n"
"s_mov_b32 s93, 14336\n"
"s_mov_b32 s86, 21504\n"
"v_lshrrev_b32 v224, 2, v227\n"
"v_and_b32 v224, 4, v224\n"
"s_and_b32 s87, s80, 1\n"
"v_or_b32 v224, s87, v224\n"
"v_and_b32 v225, 7, v227\n"
"v_xor_b32 v224, v224, v225\n"
"v_lshlrev_b32 v224, 4, v224\n"
"v_lshl_add_u32 v228, v226, 13, v224\n"
"s_lshl_b32 s87, s80, 16\n"
"v_add_u32 v228, s87, v228\n"
"s_mov_b32 s64, 0\n"
"s_mov_b32 s65, 262144\n"
"s_mov_b32 s66, 524288\n"
"s_mov_b32 s67, 786432\n"
"s_mov_b32 s68, 1048576\n"
"s_mov_b32 s69, 1310720\n"
"s_mov_b32 s70, 1572864\n"
"s_mov_b32 s71, 1835008\n"
"s_mov_b32 s72, 2097152\n"
"s_mov_b32 s73, 2359296\n"
"s_mov_b32 s74, 2621440\n"
"s_mov_b32 s75, 2883584\n"
"s_mov_b32 s76, 3145728\n"
"s_mov_b32 s77, 3407872\n"
"s_waitcnt lgkmcnt(0)\n"
"s_add_u32 s36, s36, s19\n"
"s_addc_u32 s37, s37, 0\n"
"s_and_b32 s37, s37, 0xffff\n"
"s_mov_b32 s38, 114688\n"
"s_mov_b32 s39, 0x20000\n"
"s_add_u32 m0, s95, 0\n"
"s_nop 0\n"
"buffer_load_dwordx4 v229, s[36:39], 0 offen offset:0 lds\n"
"s_add_u32 m0, s95, 4096\n"
"s_nop 0\n"
"buffer_load_dwordx4 v229, s[36:39], s92 offen offset:0 lds\n"
"s_add_u32 m0, s95, 8192\n"
"s_nop 0\n"
"buffer_load_dwordx4 v229, s[36:39], s93 offen offset:0 lds\n"
"s_add_u32 m0, s95, 12288\n"
"s_nop 0\n"
"buffer_load_dwordx4 v229, s[36:39], s86 offen offset:0 lds\n"
"s_add_u32 m0, s95, 16256\n"
"s_nop 0\n"
"buffer_load_dwordx4 v229, s[36:39], 0 offen offset:128 lds\n"
"s_add_u32 m0, s95, 20352\n"
"s_nop 0\n"
"buffer_load_dwordx4 v229, s[36:39], s92 offen offset:128 lds\n"
"s_add_u32 m0, s95, 24448\n"
"s_nop 0\n"
"buffer_load_dwordx4 v229, s[36:39], s93 offen offset:128 lds\n"
"s_add_u32 m0, s95, 28544\n"
"s_nop 0\n"
"buffer_load_dwordx4 v229, s[36:39], s86 offen offset:128 lds\n"
"s_add_u32 s40, s40, s20\n"
"s_addc_u32 s41, s41, 0\n"
"s_and_b32 s41, s41, 0xffff\n"
"s_mov_b32 s42, 3662848\n"
"s_mov_b32 s43, 0x20000\n"
"s_add_u32 m0, s95, 77824\n"
"s_nop 0\n"
"buffer_load_dwordx4 v228, s[40:43], s64 offen offset:0 lds\n"
"s_add_u32 m0, s95, 81920\n"
"s_nop 0\n"
"buffer_load_dwordx4 v228, s[40:43], s65 offen offset:0 lds\n"
"s_add_u32 m0, s95, 86016\n"
"s_nop 0\n"
"buffer_load_dwordx4 v228, s[40:43], s66 offen offset:0 lds\n"
"s_add_u32 m0, s95, 90112\n"
"s_nop 0\n"
"buffer_load_dwordx4 v228, s[40:43], s67 offen offset:0 lds\n"
"s_add_u32 m0, s95, 94208\n"
"s_nop 0\n"
"buffer_load_dwordx4 v228, s[40:43], s68 offen offset:0 lds\n"
"s_add_u32 m0, s95, 98304\n"
"s_nop 0\n"
"buffer_load_dwordx4 v228, s[40:43], s69 offen offset:0 lds\n"
"s_add_u32 m0, s95, 102400\n"
"s_nop 0\n"
"buffer_load_dwordx4 v228, s[40:43], s70 offen offset:0 lds\n"
"s_lshl_b32 s21, s17, 3\n"
"s_or_b32 s21, s21, s34\n"
"s_lshl_b32 s21, s21, 19\n"
"s_mul_i32 s22, s34, 458752\n"
"s_lshl_b32 s23, s16, 4\n"
"s_add_u32 s23, s23, s17\n"
"s_lshl_b32 s23, s23, 20\n"
"s_mul_i32 s24, s17, 229376\n"
"s_mul_i32 s25, s16, 896\n"
"s_add_u32 s24, s24, s25\n"
"s_lshl_b32 s25, s17, 8\n"
"s_or_b32 s25, s25, s16\n"
"s_add_u32 s56, s56, s23\n"
"s_addc_u32 s57, s57, 0\n"
"s_and_b32 s57, s57, 0xffff\n"
"s_mov_b32 s58, 950400\n"
"s_mov_b32 s59, 0x20000\n"
"s_mov_b32 s96, 950272\n"
"s_add_u32 s44, s44, s21\n"
"s_addc_u32 s45, s45, 0\n"
"s_and_b32 s45, s45, 0xffff\n"
"s_mov_b32 s46, 524288\n"
"s_mov_b32 s47, 0x20000\n"
"s_add_u32 s48, s48, s21\n"
"s_addc_u32 s49, s49, 0\n"
"s_and_b32 s49, s49, 0xffff\n"
"s_mov_b32 s50, 524288\n"
"s_mov_b32 s51, 0x20000\n"
"s_add_u32 s52, s52, s22\n"
"s_addc_u32 s53, s53, 0\n"
"s_and_b32 s53, s53, 0xffff\n"
"s_mov_b32 s54, 458752\n"
"s_mov_b32 s55, 0x20000\n"
"s_add_u32 s60, s60, s24\n"
"s_addc_u32 s61, s61, 0\n"
"s_and_b32 s61, s61, 0xffff\n"
"s_mov_b32 s62, 228480\n"
"s_mov_b32 s63, 0x20000\n"
"s_lshl_b32 s87, s34, 2\n"
"s_add_u32 s87, s87, s80\n"
"s_lshl_b32 s87, s87, 10\n"
"s_add_u32 s87, s87, 917504\n"
"v_and_b32 v225, 7, v227\n"
"v_lshl_add_u32 v246, v225, 7, s87\n"
"s_lshl_b32 s87, s25, 8\n"
"s_or_b32 s35, s35, s87\n"
"s_lshl_b32 s87, s18, 24\n"
"s_or_b32 s35, s35, s87\n"
"v_and_b32 v224, 15, v227\n"
"v_lshrrev_b32 v226, 4, v227\n"
"v_mul_u32_u24 v225, 1056, v224\n"
"v_lshl_add_u32 v252, v226, 4, v225\n"
"v_and_b32 v225, 6, v224\n"
"v_xor_b32 v225, v225, v226\n"
"v_lshlrev_b32 v225, 4, v225\n"
"v_and_b32 v226, 7, v224\n"
"v_lshl_add_u32 v225, v226, 7, v225\n"
"v_lshrrev_b32 v226, 3, v224\n"
"v_lshl_add_u32 v225, v226, 12, v225\n"
"v_add_u32 v230, s95, v225\n"
"v_xor_b32 v231, 64, v230\n"
"v_lshrrev_b32 v224, 5, v227\n"
"v_and_b32 v225, 31, v227\n"
"v_lshlrev_b32 v225, 4, v225\n"
"v_lshl_add_u32 v255, v224, 13, v225\n"
"s_lshl_b32 s87, s80, 11\n"
"v_add_u32 v255, s87, v255\n"
"s_mul_i32 s82, s80, 4224\n"
"s_add_u32 s91, s82, 152064\n"
"s_mov_b32 s84, 0\n"
"s_mov_b32 s85, 0\n"
"s_mov_b32 s46, 0x80000\n"
"s_mov_b32 s50, 0x80000\n"
"s_add_u32 s83, s82, 135168\n"
"v_lshrrev_b32 v224, 2, v227\n"
"v_mul_u32_u24 v224, 1056, v224\n"
"v_and_b32 v225, 3, v227\n"
"v_lshl_add_u32 v224, v225, 3, v224\n"
"v_add_u32 v253, 101376, v224\n"
"v_add_u32 v254, 118272, v224\n"
"s_mov_b32 s89, 33792\n"
"s_mov_b32 s90, 50688\n"
"s_mov_b32 s88, 0x3db8aa3b\n"
"s_mov_b32 s94, 0\n"
"v_lshrrev_b32 v224, 4, v227\n"
"v_lshlrev_b32 v225, 10, v224\n"
"v_and_b32 v224, 1, v224\n"
"v_lshl_add_u32 v225, v224, 4, v225\n"
"v_bfe_u32 v224, v227, 2, 2\n"
"v_lshl_add_u32 v225, v224, 7, v225\n"
"v_lshrrev_b32 v224, 1, v224\n"
"v_lshl_add_u32 v225, v224, 6, v225\n"
"v_bfe_u32 v224, v227, 1, 1\n"
"v_lshl_add_u32 v225, v224, 5, v225\n"
"v_and_b32 v224, 1, v227\n"
"v_lshl_add_u32 v225, v224, 3, v225\n"
"v_xor_b32 v224, 0, v225\n"
"v_add_u32 v232, 20480, v224\n"
"v_add_u32 v236, 77824, v224\n"
"v_add_u32 v240, 135168, v224\n"
"v_xor_b32 v224, 16, v225\n"
"v_add_u32 v233, 20480, v224\n"
"v_add_u32 v237, 77824, v224\n"
"v_add_u32 v241, 135168, v224\n"
"v_xor_b32 v224, 64, v225\n"
"v_add_u32 v234, 20480, v224\n"
"v_add_u32 v238, 77824, v224\n"
"v_add_u32 v242, 135168, v224\n"
"v_xor_b32 v224, 80, v225\n"
"v_add_u32 v235, 20480, v224\n"
"v_add_u32 v239, 77824, v224\n"
"v_add_u32 v243, 135168, v224\n"
"s_add_u32 m0, s95, 32512\n"
"s_nop 0\n"
"buffer_load_dwordx4 v229, s[36:39], 0 offen offset:256 lds\n"
"s_add_u32 m0, s95, 36608\n"
"s_nop 0\n"
"buffer_load_dwordx4 v229, s[36:39], s92 offen offset:256 lds\n"
"s_add_u32 m0, s95, 40704\n"
"s_nop 0\n"
"buffer_load_dwordx4 v229, s[36:39], s93 offen offset:256 lds\n"
"s_add_u32 m0, s95, 44800\n"
"s_nop 0\n"
"buffer_load_dwordx4 v229, s[36:39], s86 offen offset:256 lds\n"
"s_add_u32 m0, s95, 48768\n"
"s_nop 0\n"
"buffer_load_dwordx4 v229, s[36:39], 0 offen offset:384 lds\n"
"s_add_u32 m0, s95, 52864\n"
"s_nop 0\n"
"buffer_load_dwordx4 v229, s[36:39], s92 offen offset:384 lds\n"
"s_add_u32 m0, s95, 56960\n"
"s_nop 0\n"
"buffer_load_dwordx4 v229, s[36:39], s93 offen offset:384 lds\n"
"s_add_u32 m0, s95, 61056\n"
"s_nop 0\n"
"buffer_load_dwordx4 v229, s[36:39], s86 offen offset:384 lds\n"
"v_add_u32 v229, 512, v229\n"
"s_waitcnt vmcnt(15)\n"
"ds_read_b128 a[0:3], v230 offset:0\n"
"ds_read_b128 a[56:59], v230 offset:8192\n"
"ds_read_b128 a[4:7], v231 offset:0\n"
"ds_read_b128 a[60:63], v231 offset:8192\n"
"ds_read_b128 a[8:11], v230 offset:16384\n"
"ds_read_b128 a[64:67], v230 offset:24576\n"
"ds_read_b128 a[12:15], v231 offset:16384\n"
"ds_read_b128 a[68:71], v231 offset:24576\n"
"s_waitcnt vmcnt(8)\n"
"s_barrier\n"
"ds_read_b64_tr_b16 a[112:113], v236 offset:0\n"
"ds_read_b64_tr_b16 a[114:115], v236 offset:512\n"
"ds_read_b64_tr_b16 a[116:117], v236 offset:4096\n"
"ds_read_b64_tr_b16 a[118:119], v236 offset:4608\n"
"ds_read_b64_tr_b16 a[120:121], v236 offset:8192\n"
"ds_read_b64_tr_b16 a[122:123], v236 offset:8704\n"
"ds_read_b64_tr_b16 a[124:125], v236 offset:12288\n"
"ds_read_b64_tr_b16 a[126:127], v236 offset:12800\n"
"ds_read_b64_tr_b16 a[128:129], v236 offset:16384\n"
"ds_read_b64_tr_b16 a[130:131], v236 offset:16896\n"
"ds_read_b64_tr_b16 a[132:133], v236 offset:20480\n"
"ds_read_b64_tr_b16 a[134:135], v236 offset:20992\n"
"ds_read_b64_tr_b16 a[136:137], v236 offset:24576\n"
"ds_read_b64_tr_b16 a[138:139], v236 offset:25088\n"
"ds_read_b64_tr_b16 a[140:141], v237 offset:0\n"
"ds_read_b64_tr_b16 a[142:143], v237 offset:512\n"
"ds_read_b64_tr_b16 a[144:145], v237 offset:4096\n"
"ds_read_b64_tr_b16 a[146:147], v237 offset:4608\n"
"ds_read_b64_tr_b16 a[148:149], v237 offset:8192\n"
"ds_read_b64_tr_b16 a[150:151], v237 offset:8704\n"
"ds_read_b64_tr_b16 a[152:153], v237 offset:12288\n"
"s_add_u32 m0, s95, 106368\n"
"s_nop 0\n"
"buffer_load_dwordx4 v228, s[40:43], s64 offen offset:128 lds\n"
"s_add_u32 m0, s95, 110464\n"
"s_nop 0\n"
"buffer_load_dwordx4 v228, s[40:43], s65 offen offset:128 lds\n"
"s_add_u32 m0, s95, 114560\n"
"s_nop 0\n"
"buffer_load_dwordx4 v228, s[40:43], s66 offen offset:128 lds\n"
"s_add_u32 m0, s95, 118656\n"
"s_nop 0\n"
"buffer_load_dwordx4 v228, s[40:43], s67 offen offset:128 lds\n"
"s_add_u32 m0, s95, 122752\n"
"s_nop 0\n"
"buffer_load_dwordx4 v228, s[40:43], s68 offen offset:128 lds\n"
"s_add_u32 m0, s95, 126848\n"
"s_nop 0\n"
"buffer_load_dwordx4 v228, s[40:43], s69 offen offset:128 lds\n"
"s_add_u32 m0, s95, 130944\n"
"s_nop 0\n"
"buffer_load_dwordx4 v228, s[40:43], s70 offen offset:128 lds\n"
"s_waitcnt vmcnt(7)\n"
"s_memrealtime s[18:19]\n"
"ds_read_b128 a[16:19], v230 offset:32768\n"
"ds_read_b128 a[20:23], v231 offset:32768\n"
"ds_read_b128 a[24:27], v230 offset:49152\n"
"ds_read_b128 a[28:31], v231 offset:49152\n"
"ds_read_b128 a[84:87], v231 offset:57344\n"
"ds_read_b128 a[72:75], v230 offset:40960\n"
"ds_read_b128 a[76:79], v231 offset:40960\n"
"ds_read_b128 a[80:83], v230 offset:57344\n"
"s_waitcnt lgkmcnt(14)\n"
"v_mfma_f32_16x16x32_bf16 v[128:131], a[112:115], a[0:3], 0\n"
"s_add_u32 m0, s95, 134912\n"
"ds_read_b64_tr_b16 a[154:155], v237 offset:12800\n"
"v_mfma_f32_16x16x32_bf16 v[128:131], a[116:119], a[4:7], v[128:131]\n"
"buffer_load_dwordx4 v228, s[40:43], s64 offen offset:256 lds\n"
"v_mfma_f32_16x16x32_bf16 v[128:131], a[120:123], a[8:11], v[128:131]\n"
"s_add_u32 m0, s95, 139008\n"
"ds_read_b64_tr_b16 a[156:157], v237 offset:16384\n"
"ds_read_b64_tr_b16 a[158:159], v237 offset:16896\n"
"v_mfma_f32_16x16x32_bf16 v[128:131], a[124:127], a[12:15], v[128:131]\n"
"s_waitcnt lgkmcnt(10)\n"
"buffer_load_dwordx4 v228, s[40:43], s65 offen offset:256 lds\n"
"v_mfma_f32_16x16x32_bf16 v[128:131], a[128:131], a[16:19], v[128:131]\n"
"s_waitcnt lgkmcnt(9)\n"
"s_add_u32 m0, s95, 143104\n"
"ds_read_b64_tr_b16 a[160:161], v237 offset:20480\n"
"ds_read_b64_tr_b16 a[162:163], v237 offset:20992\n"
"v_mfma_f32_16x16x32_bf16 v[128:131], a[132:135], a[20:23], v[128:131]\n"
"s_waitcnt lgkmcnt(10)\n"
"buffer_load_dwordx4 v228, s[40:43], s66 offen offset:256 lds\n"
"v_mfma_f32_16x16x32_bf16 v[128:131], a[136:139], a[24:27], v[128:131]\n"
"s_add_u32 m0, s95, 147200\n"
"ds_read_b64_tr_b16 a[164:165], v237 offset:24576\n"
"ds_read_b64_tr_b16 a[166:167], v237 offset:25088\n"
"v_mfma_f32_16x16x32_bf16 v[132:135], a[112:115], a[56:59], 0\n"
"buffer_load_dwordx4 v228, s[40:43], s67 offen offset:256 lds\n"
"v_mfma_f32_16x16x32_bf16 v[132:135], a[116:119], a[60:63], v[132:135]\n"
"s_add_u32 m0, s95, 151296\n"
"ds_read_b64_tr_b16 a[168:169], v238 offset:0\n"
"ds_read_b64_tr_b16 a[170:171], v238 offset:512\n"
"v_mfma_f32_16x16x32_bf16 v[132:135], a[120:123], a[64:67], v[132:135]\n"
"buffer_load_dwordx4 v228, s[40:43], s68 offen offset:256 lds\n"
"v_mfma_f32_16x16x32_bf16 v[132:135], a[124:127], a[68:71], v[132:135]\n"
"s_waitcnt lgkmcnt(8)\n"
"s_add_u32 m0, s95, 155392\n"
"ds_read_b64_tr_b16 a[172:173], v238 offset:4096\n"
"ds_read_b64_tr_b16 a[174:175], v238 offset:4608\n"
"v_mfma_f32_16x16x32_bf16 v[132:135], a[128:131], a[72:75], v[132:135]\n"
"buffer_load_dwordx4 v228, s[40:43], s69 offen offset:256 lds\n"
"v_mfma_f32_16x16x32_bf16 v[132:135], a[132:135], a[76:79], v[132:135]\n"
"s_add_u32 m0, s95, 159488\n"
"ds_read_b64_tr_b16 a[176:177], v238 offset:8192\n"
"ds_read_b64_tr_b16 a[178:179], v238 offset:8704\n"
"v_mfma_f32_16x16x32_bf16 v[132:135], a[136:139], a[80:83], v[132:135]\n"
"buffer_load_dwordx4 v228, s[40:43], s70 offen offset:256 lds\n"
"v_mfma_f32_16x16x32_bf16 v[136:139], a[140:143], a[0:3], 0\n"
"ds_read_b64_tr_b16 a[180:181], v238 offset:12288\n"
"ds_read_b64_tr_b16 a[182:183], v238 offset:12800\n"
"v_mfma_f32_16x16x32_bf16 v[136:139], a[144:147], a[4:7], v[136:139]\n"
"s_add_u32 m0, s95, 0\n"
"ds_read_b64_tr_b16 a[184:185], v238 offset:16384\n"
"v_mfma_f32_16x16x32_bf16 v[136:139], a[148:151], a[8:11], v[136:139]\n"
"buffer_load_dwordx4 v229, s[36:39], 0 offen offset:0 lds\n"
"v_mfma_f32_16x16x32_bf16 v[136:139], a[152:155], a[12:15], v[136:139]\n"
"s_waitcnt lgkmcnt(7)\n"
"ds_read_b64_tr_b16 a[186:187], v238 offset:16896\n"
"ds_read_b64_tr_b16 a[188:189], v238 offset:20480\n"
"v_mfma_f32_16x16x32_bf16 v[136:139], a[156:159], a[16:19], v[136:139]\n"
"s_add_u32 m0, s95, 4096\n"
"ds_read_b64_tr_b16 a[190:191], v238 offset:20992\n"
"v_mfma_f32_16x16x32_bf16 v[136:139], a[160:163], a[20:23], v[136:139]\n"
"buffer_load_dwordx4 v229, s[36:39], s92 offen offset:0 lds\n"
"v_mfma_f32_16x16x32_bf16 v[136:139], a[164:167], a[24:27], v[136:139]\n"
"ds_read_b64_tr_b16 a[192:193], v238 offset:24576\n"
"ds_read_b64_tr_b16 a[194:195], v238 offset:25088\n"
"v_mfma_f32_16x16x32_bf16 v[140:143], a[140:143], a[56:59], 0\n"
"s_add_u32 m0, s95, 8192\n"
"ds_read_b64_tr_b16 a[196:197], v239 offset:0\n"
"v_mfma_f32_16x16x32_bf16 v[140:143], a[144:147], a[60:63], v[140:143]\n"
"buffer_load_dwordx4 v229, s[36:39], s93 offen offset:0 lds\n"
"v_mfma_f32_16x16x32_bf16 v[140:143], a[148:151], a[64:67], v[140:143]\n"
"ds_read_b64_tr_b16 a[198:199], v239 offset:512\n"
"ds_read_b64_tr_b16 a[200:201], v239 offset:4096\n"
"v_mfma_f32_16x16x32_bf16 v[140:143], a[152:155], a[68:71], v[140:143]\n"
"s_add_u32 m0, s95, 12288\n"
"ds_read_b64_tr_b16 a[202:203], v239 offset:4608\n"
"v_mfma_f32_16x16x32_bf16 v[140:143], a[156:159], a[72:75], v[140:143]\n"
"buffer_load_dwordx4 v229, s[36:39], s86 offen offset:0 lds\n"
"v_mfma_f32_16x16x32_bf16 v[140:143], a[160:163], a[76:79], v[140:143]\n"
"ds_read_b64_tr_b16 a[204:205], v239 offset:8192\n"
"ds_read_b64_tr_b16 a[206:207], v239 offset:8704\n"
"v_mfma_f32_16x16x32_bf16 v[140:143], a[164:167], a[80:83], v[140:143]\n"
"s_add_u32 m0, s95, 16256\n"
"ds_read_b64_tr_b16 a[208:209], v239 offset:12288\n"
"v_mfma_f32_16x16x32_bf16 v[144:147], a[168:171], a[0:3], 0\n"
"s_waitcnt lgkmcnt(9)\n"
"buffer_load_dwordx4 v229, s[36:39], 0 offen offset:128 lds\n"
"v_mfma_f32_16x16x32_bf16 v[144:147], a[172:175], a[4:7], v[144:147]\n"
"ds_read_b64_tr_b16 a[210:211], v239 offset:12800\n"
"ds_read_b64_tr_b16 a[212:213], v239 offset:16384\n"
"v_mfma_f32_16x16x32_bf16 v[144:147], a[176:179], a[8:11], v[144:147]\n"
"s_add_u32 m0, s95, 20352\n"
"ds_read_b64_tr_b16 a[214:215], v239 offset:16896\n"
"v_mfma_f32_16x16x32_bf16 v[144:147], a[180:183], a[12:15], v[144:147]\n"
"buffer_load_dwordx4 v229, s[36:39], s92 offen offset:128 lds\n"
"v_mfma_f32_16x16x32_bf16 v[144:147], a[184:187], a[16:19], v[144:147]\n"
"ds_read_b64_tr_b16 a[216:217], v239 offset:20480\n"
"ds_read_b64_tr_b16 a[218:219], v239 offset:20992\n"
"v_mfma_f32_16x16x32_bf16 v[144:147], a[188:191], a[20:23], v[144:147]\n"
"s_waitcnt vmcnt(13) lgkmcnt(8)\n"
"ds_read_b64_tr_b16 a[220:221], v239 offset:24576\n"
"v_mfma_f32_16x16x32_bf16 v[144:147], a[192:195], a[24:27], v[144:147]\n"
"s_barrier\n"
"ds_read_b64_tr_b16 a[222:223], v239 offset:25088\n"
"v_mfma_f32_16x16x32_bf16 v[148:151], a[168:171], a[56:59], 0\n"
"s_add_u32 m0, s95, 24448\n"
"ds_read_b64_tr_b16 a[112:113], v236 offset:28672\n"
"v_mfma_f32_16x16x32_bf16 v[148:151], a[172:175], a[60:63], v[148:151]\n"
"buffer_load_dwordx4 v229, s[36:39], s93 offen offset:128 lds\n"
"v_mfma_f32_16x16x32_bf16 v[148:151], a[176:179], a[64:67], v[148:151]\n"
"ds_read_b64_tr_b16 a[114:115], v236 offset:29184\n"
"ds_read_b64_tr_b16 a[116:117], v236 offset:32768\n"
"v_mfma_f32_16x16x32_bf16 v[148:151], a[180:183], a[68:71], v[148:151]\n"
"s_add_u32 m0, s95, 28544\n"
"ds_read_b64_tr_b16 a[118:119], v236 offset:33280\n"
"v_mfma_f32_16x16x32_bf16 v[148:151], a[184:187], a[72:75], v[148:151]\n"
"buffer_load_dwordx4 v229, s[36:39], s86 offen offset:128 lds\n"
"v_mfma_f32_16x16x32_bf16 v[148:151], a[188:191], a[76:79], v[148:151]\n"
"ds_read_b64_tr_b16 a[120:121], v236 offset:36864\n"
"ds_read_b64_tr_b16 a[122:123], v236 offset:37376\n"
"v_mfma_f32_16x16x32_bf16 v[148:151], a[192:195], a[80:83], v[148:151]\n"
"s_add_u32 m0, s95, 32512\n"
"ds_read_b64_tr_b16 a[124:125], v236 offset:40960\n"
"v_mfma_f32_16x16x32_bf16 v[152:155], a[196:199], a[0:3], 0\n"
"buffer_load_dwordx4 v229, s[36:39], 0 offen offset:256 lds\n"
"v_mfma_f32_16x16x32_bf16 v[152:155], a[200:203], a[4:7], v[152:155]\n"
"s_waitcnt lgkmcnt(7)\n"
"ds_read_b64_tr_b16 a[126:127], v236 offset:41472\n"
"ds_read_b64_tr_b16 a[128:129], v236 offset:45056\n"
"v_mfma_f32_16x16x32_bf16 v[152:155], a[204:207], a[8:11], v[152:155]\n"
"s_add_u32 m0, s95, 36608\n"
"ds_read_b64_tr_b16 a[130:131], v236 offset:45568\n"
"v_mfma_f32_16x16x32_bf16 v[152:155], a[208:211], a[12:15], v[152:155]\n"
"buffer_load_dwordx4 v229, s[36:39], s92 offen offset:256 lds\n"
"v_mfma_f32_16x16x32_bf16 v[152:155], a[212:215], a[16:19], v[152:155]\n"
"ds_read_b64_tr_b16 a[132:133], v236 offset:49152\n"
"ds_read_b64_tr_b16 a[134:135], v236 offset:49664\n"
"v_mfma_f32_16x16x32_bf16 v[152:155], a[216:219], a[20:23], v[152:155]\n"
"s_add_u32 m0, s95, 40704\n"
"ds_read_b64_tr_b16 a[136:137], v236 offset:53248\n"
"v_mfma_f32_16x16x32_bf16 v[152:155], a[220:223], a[24:27], v[152:155]\n"
"buffer_load_dwordx4 v229, s[36:39], s93 offen offset:256 lds\n"
"v_mfma_f32_16x16x32_bf16 v[156:159], a[196:199], a[56:59], 0\n"
"ds_read_b64_tr_b16 a[138:139], v236 offset:53760\n"
"ds_read_b64_tr_b16 a[140:141], v237 offset:28672\n"
"v_mfma_f32_16x16x32_bf16 v[156:159], a[200:203], a[60:63], v[156:159]\n"
"s_add_u32 m0, s95, 44800\n"
"ds_read_b64_tr_b16 a[142:143], v237 offset:29184\n"
"v_mfma_f32_16x16x32_bf16 v[156:159], a[204:207], a[64:67], v[156:159]\n"
"buffer_load_dwordx4 v229, s[36:39], s86 offen offset:256 lds\n"
"v_mfma_f32_16x16x32_bf16 v[156:159], a[208:211], a[68:71], v[156:159]\n"
"ds_read_b64_tr_b16 a[144:145], v237 offset:32768\n"
"ds_read_b64_tr_b16 a[146:147], v237 offset:33280\n"
"v_mfma_f32_16x16x32_bf16 v[156:159], a[212:215], a[72:75], v[156:159]\n"
"s_add_u32 m0, s95, 49152\n"
"ds_read_b64_tr_b16 a[148:149], v237 offset:36864\n"
"v_mfma_f32_16x16x32_bf16 v[156:159], a[216:219], a[76:79], v[156:159]\n"
"buffer_load_dwordx4 v228, s[40:43], s71 offen offset:0 lds\n"
"v_mfma_f32_16x16x32_bf16 v[156:159], a[220:223], a[80:83], v[156:159]\n"
"s_waitcnt lgkmcnt(7)\n"
"ds_read_b64_tr_b16 a[150:151], v237 offset:37376\n"
"ds_read_b64_tr_b16 a[152:153], v237 offset:40960\n"
"v_mfma_f32_16x16x32_bf16 v[160:163], a[112:115], a[0:3], 0\n"
"ds_read_b64_tr_b16 a[154:155], v237 offset:41472\n"
"v_mfma_f32_16x16x32_bf16 v[160:163], a[116:119], a[4:7], v[160:163]\n"
"s_add_u32 m0, s95, 53248\n"
"ds_read_b64_tr_b16 a[156:157], v237 offset:45056\n"
"v_mfma_f32_16x16x32_bf16 v[160:163], a[120:123], a[8:11], v[160:163]\n"
"buffer_load_dwordx4 v228, s[40:43], s72 offen offset:0 lds\n"
"v_mfma_f32_16x16x32_bf16 v[160:163], a[124:127], a[12:15], v[160:163]\n"
"ds_read_b64_tr_b16 a[158:159], v237 offset:45568\n"
"ds_read_b64_tr_b16 a[160:161], v237 offset:49152\n"
"v_mfma_f32_16x16x32_bf16 v[160:163], a[128:131], a[16:19], v[160:163]\n"
"ds_read_b64_tr_b16 a[162:163], v237 offset:49664\n"
"v_mfma_f32_16x16x32_bf16 v[160:163], a[132:135], a[20:23], v[160:163]\n"
"s_waitcnt lgkmcnt(8)\n"
"s_add_u32 m0, s95, 57344\n"
"ds_read_b64_tr_b16 a[164:165], v237 offset:53248\n"
"v_mfma_f32_16x16x32_bf16 v[160:163], a[136:139], a[24:27], v[160:163]\n"
"buffer_load_dwordx4 v228, s[40:43], s73 offen offset:0 lds\n"
"v_mfma_f32_16x16x32_bf16 v[164:167], a[112:115], a[56:59], 0\n"
"ds_read_b64_tr_b16 a[166:167], v237 offset:53760\n"
"ds_read_b64_tr_b16 a[168:169], v238 offset:28672\n"
"v_mfma_f32_16x16x32_bf16 v[164:167], a[116:119], a[60:63], v[164:167]\n"
"ds_read_b64_tr_b16 a[170:171], v238 offset:29184\n"
"v_mfma_f32_16x16x32_bf16 v[164:167], a[120:123], a[64:67], v[164:167]\n"
"s_add_u32 m0, s95, 61440\n"
"ds_read_b64_tr_b16 a[172:173], v238 offset:32768\n"
"v_mfma_f32_16x16x32_bf16 v[164:167], a[124:127], a[68:71], v[164:167]\n"
"buffer_load_dwordx4 v228, s[40:43], s74 offen offset:0 lds\n"
"v_mfma_f32_16x16x32_bf16 v[164:167], a[128:131], a[72:75], v[164:167]\n"
"ds_read_b64_tr_b16 a[174:175], v238 offset:33280\n"
"ds_read_b64_tr_b16 a[176:177], v238 offset:36864\n"
"v_mfma_f32_16x16x32_bf16 v[164:167], a[132:135], a[76:79], v[164:167]\n"
"ds_read_b64_tr_b16 a[178:179], v238 offset:37376\n"
"v_mfma_f32_16x16x32_bf16 v[164:167], a[136:139], a[80:83], v[164:167]\n"
"s_add_u32 m0, s95, 65536\n"
"ds_read_b64_tr_b16 a[180:181], v238 offset:40960\n"
"v_mfma_f32_16x16x32_bf16 v[168:171], a[140:143], a[0:3], 0\n"
"buffer_load_dwordx4 v228, s[40:43], s75 offen offset:0 lds\n"
"v_mfma_f32_16x16x32_bf16 v[168:171], a[144:147], a[4:7], v[168:171]\n"
"s_waitcnt lgkmcnt(8)\n"
"ds_read_b64_tr_b16 a[182:183], v238 offset:41472\n"
"ds_read_b64_tr_b16 a[184:185], v238 offset:45056\n"
"v_mfma_f32_16x16x32_bf16 v[168:171], a[148:151], a[8:11], v[168:171]\n"
"ds_read_b64_tr_b16 a[186:187], v238 offset:45568\n"
"v_mfma_f32_16x16x32_bf16 v[168:171], a[152:155], a[12:15], v[168:171]\n"
"s_add_u32 m0, s95, 69632\n"
"ds_read_b64_tr_b16 a[188:189], v238 offset:49152\n"
"v_mfma_f32_16x16x32_bf16 v[168:171], a[156:159], a[16:19], v[168:171]\n"
"buffer_load_dwordx4 v228, s[40:43], s76 offen offset:0 lds\n"
"v_mfma_f32_16x16x32_bf16 v[168:171], a[160:163], a[20:23], v[168:171]\n"
"s_waitcnt lgkmcnt(8)\n"
"ds_read_b64_tr_b16 a[190:191], v238 offset:49664\n"
"ds_read_b64_tr_b16 a[192:193], v238 offset:53248\n"
"v_mfma_f32_16x16x32_bf16 v[168:171], a[164:167], a[24:27], v[168:171]\n"
"ds_read_b64_tr_b16 a[194:195], v238 offset:53760\n"
"v_mfma_f32_16x16x32_bf16 v[172:175], a[140:143], a[56:59], 0\n"
"s_add_u32 m0, s95, 73728\n"
"ds_read_b64_tr_b16 a[196:197], v239 offset:28672\n"
"v_mfma_f32_16x16x32_bf16 v[172:175], a[144:147], a[60:63], v[172:175]\n"
"buffer_load_dwordx4 v228, s[40:43], s77 offen offset:0 lds\n"
"v_mfma_f32_16x16x32_bf16 v[172:175], a[148:151], a[64:67], v[172:175]\n"
"ds_read_b64_tr_b16 a[198:199], v239 offset:29184\n"
"ds_read_b64_tr_b16 a[200:201], v239 offset:32768\n"
"v_mfma_f32_16x16x32_bf16 v[172:175], a[152:155], a[68:71], v[172:175]\n"
"ds_read_b64_tr_b16 a[202:203], v239 offset:33280\n"
"v_mfma_f32_16x16x32_bf16 v[172:175], a[156:159], a[72:75], v[172:175]\n"
"ds_read_b64_tr_b16 a[204:205], v239 offset:36864\n"
"v_mfma_f32_16x16x32_bf16 v[172:175], a[160:163], a[76:79], v[172:175]\n"
"ds_read_b64_tr_b16 a[206:207], v239 offset:37376\n"
"v_mfma_f32_16x16x32_bf16 v[172:175], a[164:167], a[80:83], v[172:175]\n"
"ds_read_b64_tr_b16 a[208:209], v239 offset:40960\n"
"v_mfma_f32_16x16x32_bf16 v[176:179], a[168:171], a[0:3], 0\n"
"s_waitcnt lgkmcnt(8)\n"
"ds_read_b64_tr_b16 a[210:211], v239 offset:41472\n"
"v_mfma_f32_16x16x32_bf16 v[176:179], a[172:175], a[4:7], v[176:179]\n"
"ds_read_b64_tr_b16 a[212:213], v239 offset:45056\n"
"v_mfma_f32_16x16x32_bf16 v[176:179], a[176:179], a[8:11], v[176:179]\n"
"ds_read_b64_tr_b16 a[214:215], v239 offset:45568\n"
"v_mfma_f32_16x16x32_bf16 v[176:179], a[180:183], a[12:15], v[176:179]\n"
"ds_read_b64_tr_b16 a[216:217], v239 offset:49152\n"
"v_mfma_f32_16x16x32_bf16 v[176:179], a[184:187], a[16:19], v[176:179]\n"
"ds_read_b64_tr_b16 a[218:219], v239 offset:49664\n"
"v_mfma_f32_16x16x32_bf16 v[176:179], a[188:191], a[20:23], v[176:179]\n"
"s_waitcnt vmcnt(19) lgkmcnt(8)\n"
"ds_read_b64_tr_b16 a[220:221], v239 offset:53248\n"
"v_mfma_f32_16x16x32_bf16 v[176:179], a[192:195], a[24:27], v[176:179]\n"
"s_barrier\n"
"ds_read_b64_tr_b16 a[222:223], v239 offset:53760\n"
"v_mfma_f32_16x16x32_bf16 v[180:183], a[168:171], a[56:59], 0\n"
"s_add_u32 m0, s95, 77696\n"
"ds_read_b64_tr_b16 a[112:113], v240 offset:0\n"
"v_mfma_f32_16x16x32_bf16 v[180:183], a[172:175], a[60:63], v[180:183]\n"
"buffer_load_dwordx4 v228, s[40:43], s71 offen offset:128 lds\n"
"v_mfma_f32_16x16x32_bf16 v[180:183], a[176:179], a[64:67], v[180:183]\n"
"ds_read_b64_tr_b16 a[114:115], v240 offset:512\n"
"ds_read_b64_tr_b16 a[116:117], v240 offset:4096\n"
"v_mfma_f32_16x16x32_bf16 v[180:183], a[180:183], a[68:71], v[180:183]\n"
"ds_read_b64_tr_b16 a[118:119], v240 offset:4608\n"
"v_mfma_f32_16x16x32_bf16 v[180:183], a[184:187], a[72:75], v[180:183]\n"
"ds_read_b64_tr_b16 a[120:121], v240 offset:8192\n"
"v_mfma_f32_16x16x32_bf16 v[180:183], a[188:191], a[76:79], v[180:183]\n"
"ds_read_b64_tr_b16 a[122:123], v240 offset:8704\n"
"v_mfma_f32_16x16x32_bf16 v[180:183], a[192:195], a[80:83], v[180:183]\n"
"s_add_u32 m0, s95, 81792\n"
"ds_read_b64_tr_b16 a[124:125], v240 offset:12288\n"
"v_mfma_f32_16x16x32_bf16 v[184:187], a[196:199], a[0:3], 0\n"
"buffer_load_dwordx4 v228, s[40:43], s72 offen offset:128 lds\n"
"v_mfma_f32_16x16x32_bf16 v[184:187], a[200:203], a[4:7], v[184:187]\n"
"s_waitcnt lgkmcnt(7)\n"
"ds_read_b64_tr_b16 a[126:127], v240 offset:12800\n"
"ds_read_b64_tr_b16 a[128:129], v240 offset:16384\n"
"v_mfma_f32_16x16x32_bf16 v[184:187], a[204:207], a[8:11], v[184:187]\n"
"s_memtime s[36:37]\n"
"ds_read_b64_tr_b16 a[130:131], v240 offset:16896\n"
"v_mfma_f32_16x16x32_bf16 v[184:187], a[208:211], a[12:15], v[184:187]\n"
"s_memrealtime s[38:39]\n"
"ds_read_b64_tr_b16 a[132:133], v240 offset:20480\n"
"v_mfma_f32_16x16x32_bf16 v[184:187], a[212:215], a[16:19], v[184:187]\n"
"ds_read_b64_tr_b16 a[134:135], v240 offset:20992\n"
"v_mfma_f32_16x16x32_bf16 v[184:187], a[216:219], a[20:23], v[184:187]\n"
"s_add_u32 m0, s95, 85888\n"
"ds_read_b64_tr_b16 a[136:137], v240 offset:24576\n"
"v_mfma_f32_16x16x32_bf16 v[184:187], a[220:223], a[24:27], v[184:187]\n"
"buffer_load_dwordx4 v228, s[40:43], s73 offen offset:128 lds\n"
"v_mfma_f32_16x16x32_bf16 v[188:191], a[196:199], a[56:59], 0\n"
"ds_read_b64_tr_b16 a[138:139], v240 offset:25088\n"
"ds_read_b64_tr_b16 a[140:141], v241 offset:0\n"
"v_mfma_f32_16x16x32_bf16 v[188:191], a[200:203], a[60:63], v[188:191]\n"
"ds_read_b64_tr_b16 a[142:143], v241 offset:512\n"
"v_mfma_f32_16x16x32_bf16 v[188:191], a[204:207], a[64:67], v[188:191]\n"
"ds_read_b64_tr_b16 a[144:145], v241 offset:4096\n"
"v_mfma_f32_16x16x32_bf16 v[188:191], a[208:211], a[68:71], v[188:191]\n"
"ds_read_b64_tr_b16 a[146:147], v241 offset:4608\n"
"v_mfma_f32_16x16x32_bf16 v[188:191], a[212:215], a[72:75], v[188:191]\n"
"s_add_u32 m0, s95, 89984\n"
"ds_read_b64_tr_b16 a[148:149], v241 offset:8192\n"
"v_mfma_f32_16x16x32_bf16 v[188:191], a[216:219], a[76:79], v[188:191]\n"
"buffer_load_dwordx4 v228, s[40:43], s74 offen offset:128 lds\n"
"v_mfma_f32_16x16x32_bf16 v[188:191], a[220:223], a[80:83], v[188:191]\n"
"s_waitcnt lgkmcnt(7)\n"
"ds_read_b64_tr_b16 a[150:151], v241 offset:8704\n"
"ds_read_b64_tr_b16 a[152:153], v241 offset:12288\n"
"v_mfma_f32_16x16x32_bf16 v[192:195], a[112:115], a[0:3], 0\n"
"ds_read_b64_tr_b16 a[154:155], v241 offset:12800\n"
"v_mfma_f32_16x16x32_bf16 v[192:195], a[116:119], a[4:7], v[192:195]\n"
"ds_read_b64_tr_b16 a[156:157], v241 offset:16384\n"
"v_mfma_f32_16x16x32_bf16 v[192:195], a[120:123], a[8:11], v[192:195]\n"
"ds_read_b64_tr_b16 a[158:159], v241 offset:16896\n"
"v_mfma_f32_16x16x32_bf16 v[192:195], a[124:127], a[12:15], v[192:195]\n"
"s_add_u32 m0, s95, 94080\n"
"ds_read_b64_tr_b16 a[160:161], v241 offset:20480\n"
"v_mfma_f32_16x16x32_bf16 v[192:195], a[128:131], a[16:19], v[192:195]\n"
"s_waitcnt vmcnt(11)\n"
"buffer_load_dwordx4 v228, s[40:43], s75 offen offset:128 lds\n"
"v_mfma_f32_16x16x32_bf16 v[192:195], a[132:135], a[20:23], v[192:195]\n"
"s_waitcnt lgkmcnt(7)\n"
"ds_read_b64_tr_b16 a[162:163], v241 offset:20992\n"
"ds_read_b64_tr_b16 a[164:165], v241 offset:24576\n"
"v_mfma_f32_16x16x32_bf16 v[192:195], a[136:139], a[24:27], v[192:195]\n"
"ds_read_b64_tr_b16 a[166:167], v241 offset:25088\n"
"v_mfma_f32_16x16x32_bf16 v[196:199], a[112:115], a[56:59], 0\n"
"ds_read_b64_tr_b16 a[168:169], v242 offset:0\n"
"v_mfma_f32_16x16x32_bf16 v[196:199], a[116:119], a[60:63], v[196:199]\n"
"ds_read_b64_tr_b16 a[170:171], v242 offset:512\n"
"ds_read_b128 a[32:35], v230 offset:0\n"
"v_mfma_f32_16x16x32_bf16 v[196:199], a[120:123], a[64:67], v[196:199]\n"
"ds_read_b64_tr_b16 a[172:173], v242 offset:4096\n"
"ds_read_b128 a[88:91], v230 offset:8192\n"
"v_mfma_f32_16x16x32_bf16 v[196:199], a[124:127], a[68:71], v[196:199]\n"
"ds_read_b64_tr_b16 a[174:175], v242 offset:4608\n"
"ds_read_b128 a[36:39], v231 offset:0\n"
"v_mfma_f32_16x16x32_bf16 v[196:199], a[128:131], a[72:75], v[196:199]\n"
"ds_read_b64_tr_b16 a[176:177], v242 offset:8192\n"
"ds_read_b128 a[92:95], v231 offset:8192\n"
"v_mfma_f32_16x16x32_bf16 v[196:199], a[132:135], a[76:79], v[196:199]\n"
"ds_read_b64_tr_b16 a[178:179], v242 offset:8704\n"
"ds_read_b128 a[40:43], v230 offset:16384\n"
"v_mfma_f32_16x16x32_bf16 v[196:199], a[136:139], a[80:83], v[196:199]\n"
"ds_read_b64_tr_b16 a[180:181], v242 offset:12288\n"
"ds_read_b128 a[96:99], v230 offset:24576\n"
"v_mfma_f32_16x16x32_bf16 v[200:203], a[140:143], a[0:3], 0\n"
"ds_read_b64_tr_b16 a[182:183], v242 offset:12800\n"
"ds_read_b128 a[44:47], v231 offset:16384\n"
"v_mfma_f32_16x16x32_bf16 v[200:203], a[144:147], a[4:7], v[200:203]\n"
"s_waitcnt lgkmcnt(14)\n"
"ds_read_b64_tr_b16 a[184:185], v242 offset:16384\n"
"ds_read_b128 a[100:103], v231 offset:24576\n"
"v_mfma_f32_16x16x32_bf16 v[200:203], a[148:151], a[8:11], v[200:203]\n"
"ds_read_b64_tr_b16 a[186:187], v242 offset:16896\n"
"ds_read_b128 a[48:51], v230 offset:32768\n"
"v_mfma_f32_16x16x32_bf16 v[200:203], a[152:155], a[12:15], v[200:203]\n"
"ds_read_b64_tr_b16 a[188:189], v242 offset:20480\n"
"ds_read_b128 a[104:107], v230 offset:40960\n"
"v_mfma_f32_16x16x32_bf16 v[200:203], a[156:159], a[16:19], v[200:203]\n"
"ds_read_b64_tr_b16 a[190:191], v242 offset:20992\n"
"ds_read_b128 a[52:55], v231 offset:32768\n"
"v_mfma_f32_16x16x32_bf16 v[200:203], a[160:163], a[20:23], v[200:203]\n"
"s_add_u32 m0, s95, 98176\n"
"ds_read_b64_tr_b16 a[192:193], v242 offset:24576\n"
"ds_read_b128 a[108:111], v231 offset:40960\n"
"v_mfma_f32_16x16x32_bf16 v[200:203], a[164:167], a[24:27], v[200:203]\n"
"buffer_load_dwordx4 v228, s[40:43], s76 offen offset:128 lds\n"
"v_mfma_f32_16x16x32_bf16 v[204:207], a[140:143], a[56:59], 0\n"
"ds_read_b64_tr_b16 a[194:195], v242 offset:25088\n"
"ds_read_b64_tr_b16 a[196:197], v243 offset:0\n"
"v_mfma_f32_16x16x32_bf16 v[204:207], a[144:147], a[60:63], v[204:207]\n"
"ds_read_b64_tr_b16 a[198:199], v243 offset:512\n"
"v_mfma_f32_16x16x32_bf16 v[204:207], a[148:151], a[64:67], v[204:207]\n"
"ds_read_b64_tr_b16 a[200:201], v243 offset:4096\n"
"v_mfma_f32_16x16x32_bf16 v[204:207], a[152:155], a[68:71], v[204:207]\n"
"ds_read_b64_tr_b16 a[202:203], v243 offset:4608\n"
"v_mfma_f32_16x16x32_bf16 v[204:207], a[156:159], a[72:75], v[204:207]\n"
"s_add_u32 m0, s95, 102272\n"
"ds_read_b64_tr_b16 a[204:205], v243 offset:8192\n"
"v_mfma_f32_16x16x32_bf16 v[204:207], a[160:163], a[76:79], v[204:207]\n"
"buffer_load_dwordx4 v228, s[40:43], s77 offen offset:128 lds\n"
"v_mfma_f32_16x16x32_bf16 v[204:207], a[164:167], a[80:83], v[204:207]\n"
"s_waitcnt lgkmcnt(8)\n"
"ds_read_b64_tr_b16 a[206:207], v243 offset:8704\n"
"ds_read_b64_tr_b16 a[208:209], v243 offset:12288\n"
"v_mfma_f32_16x16x32_bf16 v[208:211], a[168:171], a[0:3], 0\n"
"ds_read_b64_tr_b16 a[210:211], v243 offset:12800\n"
"v_mfma_f32_16x16x32_bf16 v[208:211], a[172:175], a[4:7], v[208:211]\n"
"ds_read_b64_tr_b16 a[212:213], v243 offset:16384\n"
"v_mfma_f32_16x16x32_bf16 v[208:211], a[176:179], a[8:11], v[208:211]\n"
"ds_read_b64_tr_b16 a[214:215], v243 offset:16896\n"
"v_mfma_f32_16x16x32_bf16 v[208:211], a[180:183], a[12:15], v[208:211]\n"
"ds_read_b64_tr_b16 a[216:217], v243 offset:20480\n"
"v_mfma_f32_16x16x32_bf16 v[208:211], a[184:187], a[16:19], v[208:211]\n"
"ds_read_b64_tr_b16 a[218:219], v243 offset:20992\n"
"v_mfma_f32_16x16x32_bf16 v[208:211], a[188:191], a[20:23], v[208:211]\n"
"s_waitcnt vmcnt(7) lgkmcnt(8)\n"
"ds_read_b64_tr_b16 a[220:221], v243 offset:24576\n"
"v_mfma_f32_16x16x32_bf16 v[208:211], a[192:195], a[24:27], v[208:211]\n"
"s_barrier\n"
"ds_read_b64_tr_b16 a[222:223], v243 offset:25088\n"
"v_mfma_f32_16x16x32_bf16 v[212:215], a[168:171], a[56:59], 0\n"
"s_add_u32 m0, s95, 106240\n"
"ds_read_b64_tr_b16 a[112:113], v232 offset:28672\n"
"v_mfma_f32_16x16x32_bf16 v[212:215], a[172:175], a[60:63], v[212:215]\n"
"buffer_load_dwordx4 v228, s[40:43], s71 offen offset:256 lds\n"
"v_mfma_f32_16x16x32_bf16 v[212:215], a[176:179], a[64:67], v[212:215]\n"
"ds_read_b64_tr_b16 a[114:115], v232 offset:29184\n"
"ds_read_b64_tr_b16 a[116:117], v232 offset:32768\n"
"v_mfma_f32_16x16x32_bf16 v[212:215], a[180:183], a[68:71], v[212:215]\n"
"ds_read_b64_tr_b16 a[118:119], v232 offset:33280\n"
"v_mfma_f32_16x16x32_bf16 v[212:215], a[184:187], a[72:75], v[212:215]\n"
"ds_read_b64_tr_b16 a[120:121], v232 offset:36864\n"
"v_mfma_f32_16x16x32_bf16 v[212:215], a[188:191], a[76:79], v[212:215]\n"
"ds_read_b64_tr_b16 a[122:123], v232 offset:37376\n"
"v_mfma_f32_16x16x32_bf16 v[212:215], a[192:195], a[80:83], v[212:215]\n"
"s_add_u32 m0, s95, 110336\n"
"ds_read_b64_tr_b16 a[124:125], v232 offset:40960\n"
"v_mfma_f32_16x16x32_bf16 v[216:219], a[196:199], a[0:3], 0\n"
"buffer_load_dwordx4 v228, s[40:43], s72 offen offset:256 lds\n"
"v_mfma_f32_16x16x32_bf16 v[216:219], a[200:203], a[4:7], v[216:219]\n"
"s_waitcnt lgkmcnt(7)\n"
"ds_read_b64_tr_b16 a[126:127], v232 offset:41472\n"
"ds_read_b64_tr_b16 a[128:129], v232 offset:45056\n"
"v_mfma_f32_16x16x32_bf16 v[216:219], a[204:207], a[8:11], v[216:219]\n"
"ds_read_b64_tr_b16 a[130:131], v232 offset:45568\n"
"v_mfma_f32_16x16x32_bf16 v[216:219], a[208:211], a[12:15], v[216:219]\n"
"ds_read_b64_tr_b16 a[132:133], v232 offset:49152\n"
"v_mfma_f32_16x16x32_bf16 v[216:219], a[212:215], a[16:19], v[216:219]\n"
"ds_read_b64_tr_b16 a[134:135], v232 offset:49664\n"
"v_mfma_f32_16x16x32_bf16 v[216:219], a[216:219], a[20:23], v[216:219]\n"
"s_add_u32 m0, s95, 114432\n"
"ds_read_b64_tr_b16 a[136:137], v232 offset:53248\n"
"v_mfma_f32_16x16x32_bf16 v[216:219], a[220:223], a[24:27], v[216:219]\n"
"buffer_load_dwordx4 v228, s[40:43], s73 offen offset:256 lds\n"
"v_mfma_f32_16x16x32_bf16 v[220:223], a[196:199], a[56:59], 0\n"
"ds_read_b64_tr_b16 a[138:139], v232 offset:53760\n"
"ds_read_b64_tr_b16 a[140:141], v233 offset:28672\n"
"v_mfma_f32_16x16x32_bf16 v[220:223], a[200:203], a[60:63], v[220:223]\n"
"ds_read_b64_tr_b16 a[142:143], v233 offset:29184\n"
"v_mfma_f32_16x16x32_bf16 v[220:223], a[204:207], a[64:67], v[220:223]\n"
"ds_read_b64_tr_b16 a[144:145], v233 offset:32768\n"
"v_mfma_f32_16x16x32_bf16 v[220:223], a[208:211], a[68:71], v[220:223]\n"
"ds_read_b64_tr_b16 a[146:147], v233 offset:33280\n"
"v_mfma_f32_16x16x32_bf16 v[220:223], a[212:215], a[72:75], v[220:223]\n"
"s_add_u32 m0, s95, 118528\n"
"ds_read_b64_tr_b16 a[148:149], v233 offset:36864\n"
"v_mfma_f32_16x16x32_bf16 v[220:223], a[216:219], a[76:79], v[220:223]\n"
"buffer_load_dwordx4 v228, s[40:43], s74 offen offset:256 lds\n"
"v_mfma_f32_16x16x32_bf16 v[220:223], a[220:223], a[80:83], v[220:223]\n"
"s_waitcnt lgkmcnt(7)\n"
"ds_read_b64_tr_b16 a[150:151], v233 offset:37376\n"
"ds_read_b64_tr_b16 a[152:153], v233 offset:40960\n"
"v_mfma_f32_16x16x32_bf16 v[128:131], a[112:115], a[28:31], v[128:131]\n"
"ds_read_b64_tr_b16 a[154:155], v233 offset:41472\n"
"v_mfma_f32_16x16x32_bf16 v[128:131], a[116:119], a[32:35], v[128:131]\n"
"ds_read_b64_tr_b16 a[156:157], v233 offset:45056\n"
"v_mfma_f32_16x16x32_bf16 v[128:131], a[120:123], a[36:39], v[128:131]\n"
"ds_read_b64_tr_b16 a[158:159], v233 offset:45568\n"
"v_mfma_f32_16x16x32_bf16 v[128:131], a[124:127], a[40:43], v[128:131]\n"
"s_add_u32 m0, s95, 122624\n"
"ds_read_b64_tr_b16 a[160:161], v233 offset:49152\n"
"v_mfma_f32_16x16x32_bf16 v[128:131], a[128:131], a[44:47], v[128:131]\n"
"buffer_load_dwordx4 v228, s[40:43], s75 offen offset:256 lds\n"
"v_mfma_f32_16x16x32_bf16 v[128:131], a[132:135], a[48:51], v[128:131]\n"
"s_waitcnt lgkmcnt(7)\n"
"ds_read_b64_tr_b16 a[162:163], v233 offset:49664\n"
"ds_read_b64_tr_b16 a[164:165], v233 offset:53248\n"
"v_mfma_f32_16x16x32_bf16 v[128:131], a[136:139], a[52:55], v[128:131]\n"
"ds_read_b64_tr_b16 a[166:167], v233 offset:53760\n"
"v_mfma_f32_16x16x32_bf16 v[132:135], a[112:115], a[84:87], v[132:135]\n"
"ds_read_b64_tr_b16 a[168:169], v234 offset:28672\n"
"v_mfma_f32_16x16x32_bf16 v[132:135], a[116:119], a[88:91], v[132:135]\n"
"ds_read_b64_tr_b16 a[170:171], v234 offset:29184\n"
"v_mfma_f32_16x16x32_bf16 v[132:135], a[120:123], a[92:95], v[132:135]\n"
"s_add_u32 m0, s95, 126720\n"
"ds_read_b64_tr_b16 a[172:173], v234 offset:32768\n"
"v_mul_f32 v128, s88, v128\n"
"v_mfma_f32_16x16x32_bf16 v[132:135], a[124:127], a[96:99], v[132:135]\n"
"buffer_load_dwordx4 v228, s[40:43], s76 offen offset:256 lds\n"
"v_mul_f32 v129, s88, v129\n"
"v_mfma_f32_16x16x32_bf16 v[132:135], a[128:131], a[100:103], v[132:135]\n"
"ds_read_b64_tr_b16 a[174:175], v234 offset:33280\n"
"ds_read_b64_tr_b16 a[176:177], v234 offset:36864\n"
"v_mfma_f32_16x16x32_bf16 v[132:135], a[132:135], a[104:107], v[132:135]\n"
"ds_read_b64_tr_b16 a[178:179], v234 offset:37376\n"
"v_mul_f32 v130, s88, v130\n"
"v_mfma_f32_16x16x32_bf16 v[132:135], a[136:139], a[108:111], v[132:135]\n"
"ds_read_b64_tr_b16 a[180:181], v234 offset:40960\n"
"v_mul_f32 v131, s88, v131\n"
"v_mfma_f32_16x16x32_bf16 v[136:139], a[140:143], a[28:31], v[136:139]\n"
"ds_read_b64_tr_b16 a[182:183], v234 offset:41472\n"
"v_cvt_pk_bf16_f32 v0, v128, v129\n"
"v_mfma_f32_16x16x32_bf16 v[136:139], a[144:147], a[32:35], v[136:139]\n"
"s_waitcnt lgkmcnt(8)\n"
"s_add_u32 m0, s95, 130816\n"
"ds_read_b64_tr_b16 a[184:185], v234 offset:45056\n"
"v_cvt_pk_bf16_f32 v1, v130, v131\n"
"v_mfma_f32_16x16x32_bf16 v[136:139], a[148:151], a[36:39], v[136:139]\n"
"buffer_load_dwordx4 v228, s[40:43], s77 offen offset:256 lds\n"
"v_mul_f32 v132, s88, v132\n"
"v_mfma_f32_16x16x32_bf16 v[136:139], a[152:155], a[40:43], v[136:139]\n"
"ds_read_b64_tr_b16 a[186:187], v234 offset:45568\n"
"ds_read_b64_tr_b16 a[188:189], v234 offset:49152\n"
"v_mfma_f32_16x16x32_bf16 v[136:139], a[156:159], a[44:47], v[136:139]\n"
"ds_read_b64_tr_b16 a[190:191], v234 offset:49664\n"
"v_mul_f32 v133, s88, v133\n"
"v_mfma_f32_16x16x32_bf16 v[136:139], a[160:163], a[48:51], v[136:139]\n"
"ds_read_b64_tr_b16 a[192:193], v234 offset:53248\n"
"v_mul_f32 v134, s88, v134\n"
"v_mfma_f32_16x16x32_bf16 v[136:139], a[164:167], a[52:55], v[136:139]\n"
"ds_read_b64_tr_b16 a[194:195], v234 offset:53760\n"
"v_mul_f32 v135, s88, v135\n"
"v_mfma_f32_16x16x32_bf16 v[140:143], a[140:143], a[84:87], v[140:143]\n"
"ds_read_b64_tr_b16 a[196:197], v235 offset:28672\n"
"v_cvt_pk_bf16_f32 v32, v132, v133\n"
"v_mfma_f32_16x16x32_bf16 v[140:143], a[144:147], a[88:91], v[140:143]\n"
"ds_read_b64_tr_b16 a[198:199], v235 offset:29184\n"
"v_cvt_pk_bf16_f32 v33, v134, v135\n"
"v_mfma_f32_16x16x32_bf16 v[140:143], a[148:151], a[92:95], v[140:143]\n"
"ds_read_b64_tr_b16 a[200:201], v235 offset:32768\n"
"v_mul_f32 v136, s88, v136\n"
"v_mfma_f32_16x16x32_bf16 v[140:143], a[152:155], a[96:99], v[140:143]\n"
"ds_read_b64_tr_b16 a[202:203], v235 offset:33280\n"
"v_mul_f32 v137, s88, v137\n"
"v_mfma_f32_16x16x32_bf16 v[140:143], a[156:159], a[100:103], v[140:143]\n"
"ds_read_b64_tr_b16 a[204:205], v235 offset:36864\n"
"v_mul_f32 v138, s88, v138\n"
"v_mfma_f32_16x16x32_bf16 v[140:143], a[160:163], a[104:107], v[140:143]\n"
"ds_read_b64_tr_b16 a[206:207], v235 offset:37376\n"
"v_mul_f32 v139, s88, v139\n"
"v_mfma_f32_16x16x32_bf16 v[140:143], a[164:167], a[108:111], v[140:143]\n"
"s_waitcnt lgkmcnt(8)\n"
"ds_read_b64_tr_b16 a[208:209], v235 offset:40960\n"
"v_cvt_pk_bf16_f32 v2, v136, v137\n"
"v_mfma_f32_16x16x32_bf16 v[144:147], a[168:171], a[28:31], v[144:147]\n"
"ds_read_b64_tr_b16 a[210:211], v235 offset:41472\n"
"v_cvt_pk_bf16_f32 v3, v138, v139\n"
"v_mfma_f32_16x16x32_bf16 v[144:147], a[172:175], a[32:35], v[144:147]\n"
"ds_read_b64_tr_b16 a[212:213], v235 offset:45056\n"
"v_mfma_f32_16x16x32_bf16 v[144:147], a[176:179], a[36:39], v[144:147]\n"
"ds_read_b64_tr_b16 a[214:215], v235 offset:45568\n"
"v_permlane16_swap_b32 v0, v2\n"
"v_mfma_f32_16x16x32_bf16 v[144:147], a[180:183], a[40:43], v[144:147]\n"
"ds_read_b64_tr_b16 a[216:217], v235 offset:49152\n"
"v_permlane16_swap_b32 v1, v3\n"
"v_mfma_f32_16x16x32_bf16 v[144:147], a[184:187], a[44:47], v[144:147]\n"
"ds_read_b64_tr_b16 a[218:219], v235 offset:49664\n"
"v_mul_f32 v140, s88, v140\n"
"v_mfma_f32_16x16x32_bf16 v[144:147], a[188:191], a[48:51], v[144:147]\n"
"s_waitcnt vmcnt(7) lgkmcnt(8)\n"
"ds_read_b64_tr_b16 a[220:221], v235 offset:53248\n"
"v_mul_f32 v141, s88, v141\n"
"v_mfma_f32_16x16x32_bf16 v[144:147], a[192:195], a[52:55], v[144:147]\n"
"s_barrier\n"
"ds_read_b64_tr_b16 a[222:223], v235 offset:53760\n"
"v_mul_f32 v142, s88, v142\n"
"v_mfma_f32_16x16x32_bf16 v[148:151], a[168:171], a[84:87], v[148:151]\n"
"s_add_u32 m0, s95, 20096\n"
"ds_read_b64_tr_b16 a[112:113], v236 offset:0\n"
"v_mul_f32 v143, s88, v143\n"
"v_mfma_f32_16x16x32_bf16 v[148:151], a[172:175], a[88:91], v[148:151]\n"
"buffer_load_dwordx4 v228, s[40:43], s64 offen offset:384 lds\n"
"v_cvt_pk_bf16_f32 v34, v140, v141\n"
"v_mfma_f32_16x16x32_bf16 v[148:151], a[176:179], a[92:95], v[148:151]\n"
"ds_read_b64_tr_b16 a[114:115], v236 offset:512\n"
"ds_read_b64_tr_b16 a[116:117], v236 offset:4096\n"
"v_mfma_f32_16x16x32_bf16 v[148:151], a[180:183], a[96:99], v[148:151]\n"
"ds_read_b64_tr_b16 a[118:119], v236 offset:4608\n"
"v_cvt_pk_bf16_f32 v35, v142, v143\n"
"v_mfma_f32_16x16x32_bf16 v[148:151], a[184:187], a[100:103], v[148:151]\n"
"ds_read_b64_tr_b16 a[120:121], v236 offset:8192\n"
"v_mul_f32 v144, s88, v144\n"
"v_mfma_f32_16x16x32_bf16 v[148:151], a[188:191], a[104:107], v[148:151]\n"
"ds_read_b64_tr_b16 a[122:123], v236 offset:8704\n"
"v_permlane16_swap_b32 v32, v34\n"
"v_mfma_f32_16x16x32_bf16 v[148:151], a[192:195], a[108:111], v[148:151]\n"
"s_add_u32 m0, s95, 24192\n"
"ds_read_b64_tr_b16 a[124:125], v236 offset:12288\n"
"v_permlane16_swap_b32 v33, v35\n"
"v_mfma_f32_16x16x32_bf16 v[152:155], a[196:199], a[28:31], v[152:155]\n"
"buffer_load_dwordx4 v228, s[40:43], s65 offen offset:384 lds\n"
"v_mul_f32 v145, s88, v145\n"
"v_mfma_f32_16x16x32_bf16 v[152:155], a[200:203], a[32:35], v[152:155]\n"
"s_waitcnt lgkmcnt(7)\n"
"ds_read_b64_tr_b16 a[126:127], v236 offset:12800\n"
"ds_read_b64_tr_b16 a[128:129], v236 offset:16384\n"
"v_mfma_f32_16x16x32_bf16 v[152:155], a[204:207], a[36:39], v[152:155]\n"
"ds_read_b64_tr_b16 a[130:131], v236 offset:16896\n"
"v_mul_f32 v146, s88, v146\n"
"v_mfma_f32_16x16x32_bf16 v[152:155], a[208:211], a[40:43], v[152:155]\n"
"ds_read_b64_tr_b16 a[132:133], v236 offset:20480\n"
"v_mul_f32 v147, s88, v147\n"
"v_mfma_f32_16x16x32_bf16 v[152:155], a[212:215], a[44:47], v[152:155]\n"
"ds_read_b64_tr_b16 a[134:135], v236 offset:20992\n"
"v_cvt_pk_bf16_f32 v4, v144, v145\n"
"v_mfma_f32_16x16x32_bf16 v[152:155], a[216:219], a[48:51], v[152:155]\n"
"s_add_u32 m0, s95, 28288\n"
"ds_read_b64_tr_b16 a[136:137], v236 offset:24576\n"
"v_cvt_pk_bf16_f32 v5, v146, v147\n"
"v_mfma_f32_16x16x32_bf16 v[152:155], a[220:223], a[52:55], v[152:155]\n"
"buffer_load_dwordx4 v228, s[40:43], s66 offen offset:384 lds\n"
"v_mul_f32 v148, s88, v148\n"
"v_mfma_f32_16x16x32_bf16 v[156:159], a[196:199], a[84:87], v[156:159]\n"
"ds_read_b64_tr_b16 a[138:139], v236 offset:25088\n"
"ds_read_b64_tr_b16 a[140:141], v237 offset:0\n"
"v_mfma_f32_16x16x32_bf16 v[156:159], a[200:203], a[88:91], v[156:159]\n"
"ds_read_b64_tr_b16 a[142:143], v237 offset:512\n"
"v_mul_f32 v149, s88, v149\n"
"v_mfma_f32_16x16x32_bf16 v[156:159], a[204:207], a[92:95], v[156:159]\n"
"ds_read_b64_tr_b16 a[144:145], v237 offset:4096\n"
"v_mul_f32 v152, s88, v152\n"
"v_mfma_f32_16x16x32_bf16 v[156:159], a[208:211], a[96:99], v[156:159]\n"
"ds_read_b64_tr_b16 a[146:147], v237 offset:4608\n"
"v_mul_f32 v153, s88, v153\n"
"v_mfma_f32_16x16x32_bf16 v[156:159], a[212:215], a[100:103], v[156:159]\n"
"s_add_u32 m0, s95, 32384\n"
"ds_read_b64_tr_b16 a[148:149], v237 offset:8192\n"
"v_mul_f32 v154, s88, v154\n"
"v_mfma_f32_16x16x32_bf16 v[156:159], a[216:219], a[104:107], v[156:159]\n"
"buffer_load_dwordx4 v228, s[40:43], s67 offen offset:384 lds\n"
"v_mul_f32 v155, s88, v155\n"
"v_mfma_f32_16x16x32_bf16 v[156:159], a[220:223], a[108:111], v[156:159]\n"
"s_waitcnt lgkmcnt(7)\n"
"ds_read_b64_tr_b16 a[150:151], v237 offset:8704\n"
"ds_read_b64_tr_b16 a[152:153], v237 offset:12288\n"
"v_mfma_f32_16x16x32_bf16 v[160:163], a[112:115], a[28:31], v[160:163]\n"
"ds_read_b64_tr_b16 a[154:155], v237 offset:12800\n"
"v_cvt_pk_bf16_f32 v6, v152, v153\n"
"v_mfma_f32_16x16x32_bf16 v[160:163], a[116:119], a[32:35], v[160:163]\n"
"ds_read_b64_tr_b16 a[156:157], v237 offset:16384\n"
"v_cvt_pk_bf16_f32 v7, v154, v155\n"
"v_mfma_f32_16x16x32_bf16 v[160:163], a[120:123], a[36:39], v[160:163]\n"
"ds_read_b64_tr_b16 a[158:159], v237 offset:16896\n"
"v_mul_f32 v150, s88, v150\n"
"v_mfma_f32_16x16x32_bf16 v[160:163], a[124:127], a[40:43], v[160:163]\n"
"s_add_u32 m0, s95, 36480\n"
"ds_read_b64_tr_b16 a[160:161], v237 offset:20480\n"
"v_permlane16_swap_b32 v4, v6\n"
"v_mfma_f32_16x16x32_bf16 v[160:163], a[128:131], a[44:47], v[160:163]\n"
"buffer_load_dwordx4 v228, s[40:43], s68 offen offset:384 lds\n"
"v_permlane16_swap_b32 v5, v7\n"
"v_mfma_f32_16x16x32_bf16 v[160:163], a[132:135], a[48:51], v[160:163]\n"
"s_waitcnt lgkmcnt(7)\n"
"ds_read_b64_tr_b16 a[162:163], v237 offset:20992\n"
"ds_read_b64_tr_b16 a[164:165], v237 offset:24576\n"
"v_mfma_f32_16x16x32_bf16 v[160:163], a[136:139], a[52:55], v[160:163]\n"
"ds_read_b64_tr_b16 a[166:167], v237 offset:25088\n"
"v_mul_f32 v151, s88, v151\n"
"v_mfma_f32_16x16x32_bf16 v[164:167], a[112:115], a[84:87], v[164:167]\n"
"ds_read_b64_tr_b16 a[168:169], v238 offset:0\n"
"v_cvt_pk_bf16_f32 v36, v148, v149\n"
"v_mfma_f32_16x16x32_bf16 v[164:167], a[116:119], a[88:91], v[164:167]\n"
"ds_read_b64_tr_b16 a[170:171], v238 offset:512\n"
"v_cvt_pk_bf16_f32 v37, v150, v151\n"
"v_mfma_f32_16x16x32_bf16 v[164:167], a[120:123], a[92:95], v[164:167]\n"
"s_add_u32 m0, s95, 40576\n"
"ds_read_b64_tr_b16 a[172:173], v238 offset:4096\n"
"v_mul_f32 v156, s88, v156\n"
"v_mfma_f32_16x16x32_bf16 v[164:167], a[124:127], a[96:99], v[164:167]\n"
"buffer_load_dwordx4 v228, s[40:43], s69 offen offset:384 lds\n"
"v_mul_f32 v157, s88, v157\n"
"v_mfma_f32_16x16x32_bf16 v[164:167], a[128:131], a[100:103], v[164:167]\n"
"ds_read_b64_tr_b16 a[174:175], v238 offset:4608\n"
"ds_read_b64_tr_b16 a[176:177], v238 offset:8192\n"
"v_mfma_f32_16x16x32_bf16 v[164:167], a[132:135], a[104:107], v[164:167]\n"
"ds_read_b64_tr_b16 a[178:179], v238 offset:8704\n"
"v_mul_f32 v158, s88, v158\n"
"v_mfma_f32_16x16x32_bf16 v[164:167], a[136:139], a[108:111], v[164:167]\n"
"ds_read_b64_tr_b16 a[180:181], v238 offset:12288\n"
"v_mul_f32 v159, s88, v159\n"
"v_mfma_f32_16x16x32_bf16 v[168:171], a[140:143], a[28:31], v[168:171]\n"
"ds_read_b64_tr_b16 a[182:183], v238 offset:12800\n"
"v_cvt_pk_bf16_f32 v38, v156, v157\n"
"v_mfma_f32_16x16x32_bf16 v[168:171], a[144:147], a[32:35], v[168:171]\n"
"s_waitcnt lgkmcnt(8)\n"
"s_add_u32 m0, s95, 44672\n"
"ds_read_b64_tr_b16 a[184:185], v238 offset:16384\n"
"v_cvt_pk_bf16_f32 v39, v158, v159\n"
"v_mfma_f32_16x16x32_bf16 v[168:171], a[148:151], a[36:39], v[168:171]\n"
"buffer_load_dwordx4 v228, s[40:43], s70 offen offset:384 lds\n"
"v_mul_f32 v160, s88, v160\n"
"v_mfma_f32_16x16x32_bf16 v[168:171], a[152:155], a[40:43], v[168:171]\n"
"ds_read_b64_tr_b16 a[186:187], v238 offset:16896\n"
"ds_read_b64_tr_b16 a[188:189], v238 offset:20480\n"
"v_mfma_f32_16x16x32_bf16 v[168:171], a[156:159], a[44:47], v[168:171]\n"
"ds_read_b64_tr_b16 a[190:191], v238 offset:20992\n"
"v_permlane16_swap_b32 v36, v38\n"
"v_mfma_f32_16x16x32_bf16 v[168:171], a[160:163], a[48:51], v[168:171]\n"
"ds_read_b64_tr_b16 a[192:193], v238 offset:24576\n"
"v_permlane16_swap_b32 v37, v39\n"
"v_mfma_f32_16x16x32_bf16 v[168:171], a[164:167], a[52:55], v[168:171]\n"
"ds_read_b64_tr_b16 a[194:195], v238 offset:25088\n"
"v_mul_f32 v161, s88, v161\n"
"v_mfma_f32_16x16x32_bf16 v[172:175], a[140:143], a[84:87], v[172:175]\n"
"s_add_u32 m0, s95, 134784\n"
"ds_read_b64_tr_b16 a[196:197], v239 offset:0\n"
"v_mul_f32 v162, s88, v162\n"
"v_mfma_f32_16x16x32_bf16 v[172:175], a[144:147], a[88:91], v[172:175]\n"
"buffer_load_dwordx4 v228, s[40:43], s71 offen offset:384 lds\n"
"v_mul_f32 v163, s88, v163\n"
"v_mfma_f32_16x16x32_bf16 v[172:175], a[148:151], a[92:95], v[172:175]\n"
"ds_read_b64_tr_b16 a[198:199], v239 offset:512\n"
"ds_read_b64_tr_b16 a[200:201], v239 offset:4096\n"
"v_mfma_f32_16x16x32_bf16 v[172:175], a[152:155], a[96:99], v[172:175]\n"
"ds_read_b64_tr_b16 a[202:203], v239 offset:4608\n"
"v_cvt_pk_bf16_f32 v8, v160, v161\n"
"v_mfma_f32_16x16x32_bf16 v[172:175], a[156:159], a[100:103], v[172:175]\n"
"ds_read_b64_tr_b16 a[204:205], v239 offset:8192\n"
"v_cvt_pk_bf16_f32 v9, v162, v163\n"
"v_mfma_f32_16x16x32_bf16 v[172:175], a[160:163], a[104:107], v[172:175]\n"
"ds_read_b64_tr_b16 a[206:207], v239 offset:8704\n"
"v_mul_f32 v168, s88, v168\n"
"v_mfma_f32_16x16x32_bf16 v[172:175], a[164:167], a[108:111], v[172:175]\n"
"s_waitcnt lgkmcnt(8)\n"
"s_add_u32 m0, s95, 138880\n"
"ds_read_b64_tr_b16 a[208:209], v239 offset:12288\n"
"v_mul_f32 v169, s88, v169\n"
"v_mfma_f32_16x16x32_bf16 v[176:179], a[168:171], a[28:31], v[176:179]\n"
"buffer_load_dwordx4 v228, s[40:43], s72 offen offset:384 lds\n"
"v_mul_f32 v170, s88, v170\n"
"v_mfma_f32_16x16x32_bf16 v[176:179], a[172:175], a[32:35], v[176:179]\n"
"ds_read_b64_tr_b16 a[210:211], v239 offset:12800\n"
"ds_read_b64_tr_b16 a[212:213], v239 offset:16384\n"
"v_mfma_f32_16x16x32_bf16 v[176:179], a[176:179], a[36:39], v[176:179]\n"
"ds_read_b64_tr_b16 a[214:215], v239 offset:16896\n"
"v_mul_f32 v171, s88, v171\n"
"v_mfma_f32_16x16x32_bf16 v[176:179], a[180:183], a[40:43], v[176:179]\n"
"ds_read_b64_tr_b16 a[216:217], v239 offset:20480\n"
"v_cvt_pk_bf16_f32 v10, v168, v169\n"
"v_mfma_f32_16x16x32_bf16 v[176:179], a[184:187], a[44:47], v[176:179]\n"
"ds_read_b64_tr_b16 a[218:219], v239 offset:20992\n"
"v_cvt_pk_bf16_f32 v11, v170, v171\n"
"v_mfma_f32_16x16x32_bf16 v[176:179], a[188:191], a[48:51], v[176:179]\n"
"s_waitcnt vmcnt(9) lgkmcnt(8)\n"
"ds_read_b64_tr_b16 a[220:221], v239 offset:24576\n"
"v_mul_f32 v164, s88, v164\n"
"v_mfma_f32_16x16x32_bf16 v[176:179], a[192:195], a[52:55], v[176:179]\n"
"s_barrier\n"
"ds_read_b64_tr_b16 a[222:223], v239 offset:25088\n"
"v_permlane16_swap_b32 v8, v10\n"
"v_mfma_f32_16x16x32_bf16 v[180:183], a[168:171], a[84:87], v[180:183]\n"
"s_add_u32 m0, s95, 142976\n"
"ds_read_b64_tr_b16 a[112:113], v236 offset:28672\n"
"v_permlane16_swap_b32 v9, v11\n"
"v_mfma_f32_16x16x32_bf16 v[180:183], a[172:175], a[88:91], v[180:183]\n"
"buffer_load_dwordx4 v228, s[40:43], s73 offen offset:384 lds\n"
"v_mul_f32 v165, s88, v165\n"
"v_mfma_f32_16x16x32_bf16 v[180:183], a[176:179], a[92:95], v[180:183]\n"
"ds_read_b64_tr_b16 a[114:115], v236 offset:29184\n"
"ds_read_b64_tr_b16 a[116:117], v236 offset:32768\n"
"v_mfma_f32_16x16x32_bf16 v[180:183], a[180:183], a[96:99], v[180:183]\n"
"ds_read_b64_tr_b16 a[118:119], v236 offset:33280\n"
"v_mul_f32 v166, s88, v166\n"
"v_mfma_f32_16x16x32_bf16 v[180:183], a[184:187], a[100:103], v[180:183]\n"
"ds_read_b64_tr_b16 a[120:121], v236 offset:36864\n"
"v_mul_f32 v167, s88, v167\n"
"v_mfma_f32_16x16x32_bf16 v[180:183], a[188:191], a[104:107], v[180:183]\n"
"ds_read_b64_tr_b16 a[122:123], v236 offset:37376\n"
"v_cvt_pk_bf16_f32 v40, v164, v165\n"
"v_mfma_f32_16x16x32_bf16 v[180:183], a[192:195], a[108:111], v[180:183]\n"
"s_add_u32 m0, s95, 147072\n"
"ds_read_b64_tr_b16 a[124:125], v236 offset:40960\n"
"v_cvt_pk_bf16_f32 v41, v166, v167\n"
"v_mfma_f32_16x16x32_bf16 v[184:187], a[196:199], a[28:31], v[184:187]\n"
"buffer_load_dwordx4 v228, s[40:43], s74 offen offset:384 lds\n"
"v_mul_f32 v172, s88, v172\n"
"v_mfma_f32_16x16x32_bf16 v[184:187], a[200:203], a[32:35], v[184:187]\n"
"s_waitcnt lgkmcnt(7)\n"
"ds_read_b64_tr_b16 a[126:127], v236 offset:41472\n"
"ds_read_b64_tr_b16 a[128:129], v236 offset:45056\n"
"v_mfma_f32_16x16x32_bf16 v[184:187], a[204:207], a[36:39], v[184:187]\n"
"ds_read_b64_tr_b16 a[130:131], v236 offset:45568\n"
"v_mul_f32 v173, s88, v173\n"
"v_mfma_f32_16x16x32_bf16 v[184:187], a[208:211], a[40:43], v[184:187]\n"
"ds_read_b64_tr_b16 a[132:133], v236 offset:49152\n"
"v_mul_f32 v174, s88, v174\n"
"v_mfma_f32_16x16x32_bf16 v[184:187], a[212:215], a[44:47], v[184:187]\n"
"ds_read_b64_tr_b16 a[134:135], v236 offset:49664\n"
"v_mul_f32 v175, s88, v175\n"
"v_mfma_f32_16x16x32_bf16 v[184:187], a[216:219], a[48:51], v[184:187]\n"
"s_add_u32 m0, s95, 151168\n"
"ds_read_b64_tr_b16 a[136:137], v236 offset:53248\n"
"v_cvt_pk_bf16_f32 v42, v172, v173\n"
"v_mfma_f32_16x16x32_bf16 v[184:187], a[220:223], a[52:55], v[184:187]\n"
"buffer_load_dwordx4 v228, s[40:43], s75 offen offset:384 lds\n"
"v_cvt_pk_bf16_f32 v43, v174, v175\n"
"v_mfma_f32_16x16x32_bf16 v[188:191], a[196:199], a[84:87], v[188:191]\n"
"ds_read_b64_tr_b16 a[138:139], v236 offset:53760\n"
"ds_read_b64_tr_b16 a[140:141], v237 offset:28672\n"
"v_mfma_f32_16x16x32_bf16 v[188:191], a[200:203], a[88:91], v[188:191]\n"
"ds_read_b64_tr_b16 a[142:143], v237 offset:29184\n"
"v_permlane16_swap_b32 v40, v42\n"
"v_mfma_f32_16x16x32_bf16 v[188:191], a[204:207], a[92:95], v[188:191]\n"
"ds_read_b64_tr_b16 a[144:145], v237 offset:32768\n"
"v_permlane16_swap_b32 v41, v43\n"
"v_mfma_f32_16x16x32_bf16 v[188:191], a[208:211], a[96:99], v[188:191]\n"
"ds_read_b64_tr_b16 a[146:147], v237 offset:33280\n"
"v_mul_f32 v176, s88, v176\n"
"v_mfma_f32_16x16x32_bf16 v[188:191], a[212:215], a[100:103], v[188:191]\n"
"s_add_u32 m0, s95, 155264\n"
"ds_read_b64_tr_b16 a[148:149], v237 offset:36864\n"
"v_mul_f32 v177, s88, v177\n"
"v_mfma_f32_16x16x32_bf16 v[188:191], a[216:219], a[104:107], v[188:191]\n"
"buffer_load_dwordx4 v228, s[40:43], s76 offen offset:384 lds\n"
"v_mul_f32 v178, s88, v178\n"
"v_mfma_f32_16x16x32_bf16 v[188:191], a[220:223], a[108:111], v[188:191]\n"
"s_waitcnt lgkmcnt(7)\n"
"ds_read_b64_tr_b16 a[150:151], v237 offset:37376\n"
"ds_read_b64_tr_b16 a[152:153], v237 offset:40960\n"
"v_mfma_f32_16x16x32_bf16 v[192:195], a[112:115], a[28:31], v[192:195]\n"
"ds_read_b64_tr_b16 a[154:155], v237 offset:41472\n"
"v_mul_f32 v179, s88, v179\n"
"v_mfma_f32_16x16x32_bf16 v[192:195], a[116:119], a[32:35], v[192:195]\n"
"ds_read_b64_tr_b16 a[156:157], v237 offset:45056\n"
"v_cvt_pk_bf16_f32 v12, v176, v177\n"
"v_mfma_f32_16x16x32_bf16 v[192:195], a[120:123], a[36:39], v[192:195]\n"
"ds_read_b64_tr_b16 a[158:159], v237 offset:45568\n"
"v_cvt_pk_bf16_f32 v13, v178, v179\n"
"v_mfma_f32_16x16x32_bf16 v[192:195], a[124:127], a[40:43], v[192:195]\n"
"s_add_u32 m0, s95, 159360\n"
"ds_read_b64_tr_b16 a[160:161], v237 offset:49152\n"
"v_mul_f32 v184, s88, v184\n"
"v_mfma_f32_16x16x32_bf16 v[192:195], a[128:131], a[44:47], v[192:195]\n"
"buffer_load_dwordx4 v228, s[40:43], s77 offen offset:384 lds\n"
"v_mul_f32 v185, s88, v185\n"
"v_mfma_f32_16x16x32_bf16 v[192:195], a[132:135], a[48:51], v[192:195]\n"
"s_waitcnt lgkmcnt(7)\n"
"ds_read_b64_tr_b16 a[162:163], v237 offset:49664\n"
"ds_read_b64_tr_b16 a[164:165], v237 offset:53248\n"
"v_mfma_f32_16x16x32_bf16 v[192:195], a[136:139], a[52:55], v[192:195]\n"
"ds_read_b64_tr_b16 a[166:167], v237 offset:53760\n"
"v_mul_f32 v186, s88, v186\n"
"v_mfma_f32_16x16x32_bf16 v[196:199], a[112:115], a[84:87], v[196:199]\n"
"ds_read_b64_tr_b16 a[168:169], v238 offset:28672\n"
"v_mul_f32 v187, s88, v187\n"
"v_mfma_f32_16x16x32_bf16 v[196:199], a[116:119], a[88:91], v[196:199]\n"
"ds_read_b64_tr_b16 a[170:171], v238 offset:29184\n"
"v_cvt_pk_bf16_f32 v14, v184, v185\n"
"v_mfma_f32_16x16x32_bf16 v[196:199], a[120:123], a[92:95], v[196:199]\n"
"s_add_u32 m0, s95, 48640\n"
"ds_read_b64_tr_b16 a[172:173], v238 offset:32768\n"
"v_cvt_pk_bf16_f32 v15, v186, v187\n"
"v_mfma_f32_16x16x32_bf16 v[196:199], a[124:127], a[96:99], v[196:199]\n"
"buffer_load_dwordx4 v228, s[40:43], s64 offen offset:512 lds\n"
"v_mul_f32 v180, s88, v180\n"
"v_mfma_f32_16x16x32_bf16 v[196:199], a[128:131], a[100:103], v[196:199]\n"
"ds_read_b64_tr_b16 a[174:175], v238 offset:33280\n"
"ds_read_b64_tr_b16 a[176:177], v238 offset:36864\n"
"v_mfma_f32_16x16x32_bf16 v[196:199], a[132:135], a[104:107], v[196:199]\n"
"ds_read_b64_tr_b16 a[178:179], v238 offset:37376\n"
"v_permlane16_swap_b32 v12, v14\n"
"v_mfma_f32_16x16x32_bf16 v[196:199], a[136:139], a[108:111], v[196:199]\n"
"ds_read_b64_tr_b16 a[180:181], v238 offset:40960\n"
"v_permlane16_swap_b32 v13, v15\n"
"v_mfma_f32_16x16x32_bf16 v[200:203], a[140:143], a[28:31], v[200:203]\n"
"ds_read_b64_tr_b16 a[182:183], v238 offset:41472\n"
"v_mul_f32 v181, s88, v181\n"
"v_mfma_f32_16x16x32_bf16 v[200:203], a[144:147], a[32:35], v[200:203]\n"
"s_waitcnt lgkmcnt(8)\n"
"s_add_u32 m0, s95, 52736\n"
"ds_read_b64_tr_b16 a[184:185], v238 offset:45056\n"
"v_mul_f32 v182, s88, v182\n"
"v_mfma_f32_16x16x32_bf16 v[200:203], a[148:151], a[36:39], v[200:203]\n"
"buffer_load_dwordx4 v228, s[40:43], s65 offen offset:512 lds\n"
"v_mul_f32 v183, s88, v183\n"
"v_mfma_f32_16x16x32_bf16 v[200:203], a[152:155], a[40:43], v[200:203]\n"
"ds_read_b64_tr_b16 a[186:187], v238 offset:45568\n"
"ds_read_b64_tr_b16 a[188:189], v238 offset:49152\n"
"v_mfma_f32_16x16x32_bf16 v[200:203], a[156:159], a[44:47], v[200:203]\n"
"ds_read_b64_tr_b16 a[190:191], v238 offset:49664\n"
"v_cvt_pk_bf16_f32 v44, v180, v181\n"
"v_mfma_f32_16x16x32_bf16 v[200:203], a[160:163], a[48:51], v[200:203]\n"
"ds_read_b64_tr_b16 a[192:193], v238 offset:53248\n"
"v_cvt_pk_bf16_f32 v45, v182, v183\n"
"v_mfma_f32_16x16x32_bf16 v[200:203], a[164:167], a[52:55], v[200:203]\n"
"ds_read_b64_tr_b16 a[194:195], v238 offset:53760\n"
"v_mul_f32 v188, s88, v188\n"
"v_mfma_f32_16x16x32_bf16 v[204:207], a[140:143], a[84:87], v[204:207]\n"
"s_add_u32 m0, s95, 56832\n"
"ds_read_b64_tr_b16 a[196:197], v239 offset:28672\n"
"v_mul_f32 v189, s88, v189\n"
"v_mfma_f32_16x16x32_bf16 v[204:207], a[144:147], a[88:91], v[204:207]\n"
"buffer_load_dwordx4 v228, s[40:43], s66 offen offset:512 lds\n"
"v_mul_f32 v190, s88, v190\n"
"v_mfma_f32_16x16x32_bf16 v[204:207], a[148:151], a[92:95], v[204:207]\n"
"ds_read_b64_tr_b16 a[198:199], v239 offset:29184\n"
"ds_read_b64_tr_b16 a[200:201], v239 offset:32768\n"
"v_mfma_f32_16x16x32_bf16 v[204:207], a[152:155], a[96:99], v[204:207]\n"
"ds_read_b64_tr_b16 a[202:203], v239 offset:33280\n"
"v_mul_f32 v191, s88, v191\n"
"v_mfma_f32_16x16x32_bf16 v[204:207], a[156:159], a[100:103], v[204:207]\n"
"ds_read_b64_tr_b16 a[204:205], v239 offset:36864\n"
"v_cvt_pk_bf16_f32 v46, v188, v189\n"
"v_mfma_f32_16x16x32_bf16 v[204:207], a[160:163], a[104:107], v[204:207]\n"
"ds_read_b64_tr_b16 a[206:207], v239 offset:37376\n"
"v_cvt_pk_bf16_f32 v47, v190, v191\n"
"v_mfma_f32_16x16x32_bf16 v[204:207], a[164:167], a[108:111], v[204:207]\n"
"s_waitcnt lgkmcnt(8)\n"
"s_add_u32 m0, s95, 60928\n"
"ds_read_b64_tr_b16 a[208:209], v239 offset:40960\n"
"v_mul_f32 v192, s88, v192\n"
"v_mfma_f32_16x16x32_bf16 v[208:211], a[168:171], a[28:31], v[208:211]\n"
"buffer_load_dwordx4 v228, s[40:43], s67 offen offset:512 lds\n"
"v_permlane16_swap_b32 v44, v46\n"
"v_mfma_f32_16x16x32_bf16 v[208:211], a[172:175], a[32:35], v[208:211]\n"
"ds_read_b64_tr_b16 a[210:211], v239 offset:41472\n"
"ds_read_b64_tr_b16 a[212:213], v239 offset:45056\n"
"v_mfma_f32_16x16x32_bf16 v[208:211], a[176:179], a[36:39], v[208:211]\n"
"ds_read_b64_tr_b16 a[214:215], v239 offset:45568\n"
"v_permlane16_swap_b32 v45, v47\n"
"v_mfma_f32_16x16x32_bf16 v[208:211], a[180:183], a[40:43], v[208:211]\n"
"ds_read_b64_tr_b16 a[216:217], v239 offset:49152\n"
"v_mul_f32 v193, s88, v193\n"
"v_mfma_f32_16x16x32_bf16 v[208:211], a[184:187], a[44:47], v[208:211]\n"
"ds_read_b64_tr_b16 a[218:219], v239 offset:49664\n"
"v_mul_f32 v194, s88, v194\n"
"v_mfma_f32_16x16x32_bf16 v[208:211], a[188:191], a[48:51], v[208:211]\n"
"s_waitcnt vmcnt(11) lgkmcnt(8)\n"
"ds_read_b64_tr_b16 a[220:221], v239 offset:53248\n"
"v_mul_f32 v195, s88, v195\n"
"v_mfma_f32_16x16x32_bf16 v[208:211], a[192:195], a[52:55], v[208:211]\n"
"s_barrier\n"
"ds_read_b64_tr_b16 a[222:223], v239 offset:53760\n"
"v_cvt_pk_bf16_f32 v16, v192, v193\n"
"v_mfma_f32_16x16x32_bf16 v[212:215], a[168:171], a[84:87], v[212:215]\n"
"s_add_u32 m0, s95, 65024\n"
"ds_read_b64_tr_b16 a[112:113], v232 offset:0\n"
"v_cvt_pk_bf16_f32 v17, v194, v195\n"
"v_mfma_f32_16x16x32_bf16 v[212:215], a[172:175], a[88:91], v[212:215]\n"
"buffer_load_dwordx4 v228, s[40:43], s68 offen offset:512 lds\n"
"v_mul_f32 v200, s88, v200\n"
"v_mfma_f32_16x16x32_bf16 v[212:215], a[176:179], a[92:95], v[212:215]\n"
"ds_read_b64_tr_b16 a[114:115], v232 offset:512\n"
"ds_read_b64_tr_b16 a[116:117], v232 offset:4096\n"
"v_mfma_f32_16x16x32_bf16 v[212:215], a[180:183], a[96:99], v[212:215]\n"
"ds_read_b64_tr_b16 a[118:119], v232 offset:4608\n"
"v_mul_f32 v201, s88, v201\n"
"v_mfma_f32_16x16x32_bf16 v[212:215], a[184:187], a[100:103], v[212:215]\n"
"ds_read_b64_tr_b16 a[120:121], v232 offset:8192\n"
"v_mul_f32 v202, s88, v202\n"
"v_mfma_f32_16x16x32_bf16 v[212:215], a[188:191], a[104:107], v[212:215]\n"
"ds_read_b64_tr_b16 a[122:123], v232 offset:8704\n"
"v_mul_f32 v203, s88, v203\n"
"v_mfma_f32_16x16x32_bf16 v[212:215], a[192:195], a[108:111], v[212:215]\n"
"s_add_u32 m0, s95, 69120\n"
"ds_read_b64_tr_b16 a[124:125], v232 offset:12288\n"
"v_cvt_pk_bf16_f32 v18, v200, v201\n"
"v_mfma_f32_16x16x32_bf16 v[216:219], a[196:199], a[28:31], v[216:219]\n"
"buffer_load_dwordx4 v228, s[40:43], s69 offen offset:512 lds\n"
"v_cvt_pk_bf16_f32 v19, v202, v203\n"
"v_mfma_f32_16x16x32_bf16 v[216:219], a[200:203], a[32:35], v[216:219]\n"
"s_waitcnt lgkmcnt(7)\n"
"ds_read_b64_tr_b16 a[126:127], v232 offset:12800\n"
"ds_read_b64_tr_b16 a[128:129], v232 offset:16384\n"
"v_mfma_f32_16x16x32_bf16 v[216:219], a[204:207], a[36:39], v[216:219]\n"
"ds_read_b64_tr_b16 a[130:131], v232 offset:16896\n"
"v_permlane16_swap_b32 v16, v18\n"
"v_mfma_f32_16x16x32_bf16 v[216:219], a[208:211], a[40:43], v[216:219]\n"
"ds_read_b64_tr_b16 a[132:133], v232 offset:20480\n"
"v_permlane16_swap_b32 v17, v19\n"
"v_mfma_f32_16x16x32_bf16 v[216:219], a[212:215], a[44:47], v[216:219]\n"
"ds_read_b64_tr_b16 a[134:135], v232 offset:20992\n"
"v_mul_f32 v196, s88, v196\n"
"v_mfma_f32_16x16x32_bf16 v[216:219], a[216:219], a[48:51], v[216:219]\n"
"s_add_u32 m0, s95, 73216\n"
"ds_read_b64_tr_b16 a[136:137], v232 offset:24576\n"
"v_mul_f32 v197, s88, v197\n"
"v_mfma_f32_16x16x32_bf16 v[216:219], a[220:223], a[52:55], v[216:219]\n"
"buffer_load_dwordx4 v228, s[40:43], s70 offen offset:512 lds\n"
"v_mul_f32 v198, s88, v198\n"
"v_mfma_f32_16x16x32_bf16 v[220:223], a[196:199], a[84:87], v[220:223]\n"
"ds_read_b64_tr_b16 a[138:139], v232 offset:25088\n"
"ds_read_b64_tr_b16 a[140:141], v233 offset:0\n"
"v_mfma_f32_16x16x32_bf16 v[220:223], a[200:203], a[88:91], v[220:223]\n"
"ds_read_b64_tr_b16 a[142:143], v233 offset:512\n"
"v_mul_f32 v199, s88, v199\n"
"v_mfma_f32_16x16x32_bf16 v[220:223], a[204:207], a[92:95], v[220:223]\n"
"ds_read_b64_tr_b16 a[144:145], v233 offset:4096\n"
"v_cvt_pk_bf16_f32 v48, v196, v197\n"
"v_mfma_f32_16x16x32_bf16 v[220:223], a[208:211], a[96:99], v[220:223]\n"
"ds_read_b64_tr_b16 a[146:147], v233 offset:4608\n"
"v_cvt_pk_bf16_f32 v49, v198, v199\n"
"v_mfma_f32_16x16x32_bf16 v[220:223], a[212:215], a[100:103], v[220:223]\n"
"s_add_u32 m0, s95, 77312\n"
"ds_read_b64_tr_b16 a[148:149], v233 offset:8192\n"
"v_mul_f32 v204, s88, v204\n"
"v_mfma_f32_16x16x32_bf16 v[220:223], a[216:219], a[104:107], v[220:223]\n"
"buffer_load_dwordx4 v228, s[40:43], s71 offen offset:512 lds\n"
"v_mul_f32 v205, s88, v205\n"
"v_mfma_f32_16x16x32_bf16 v[220:223], a[220:223], a[108:111], v[220:223]\n"
"s_waitcnt lgkmcnt(7)\n"
"ds_read_b64_tr_b16 a[150:151], v233 offset:8704\n"
"ds_read_b64_tr_b16 a[152:153], v233 offset:12288\n"
"v_mfma_f32_16x16x32_bf16 v[128:131], a[112:115], a[0:3], 0\n"
"ds_read_b64_tr_b16 a[154:155], v233 offset:12800\n"
"v_mul_f32 v206, s88, v206\n"
"v_mfma_f32_16x16x32_bf16 v[128:131], a[116:119], a[4:7], v[128:131]\n"
"ds_read_b64_tr_b16 a[156:157], v233 offset:16384\n"
"v_mul_f32 v207, s88, v207\n"
"v_mfma_f32_16x16x32_bf16 v[128:131], a[120:123], a[8:11], v[128:131]\n"
"ds_read_b64_tr_b16 a[158:159], v233 offset:16896\n"
"v_cvt_pk_bf16_f32 v50, v204, v205\n"
"v_mfma_f32_16x16x32_bf16 v[128:131], a[124:127], a[12:15], v[128:131]\n"
"s_add_u32 m0, s95, 81408\n"
"ds_read_b64_tr_b16 a[160:161], v233 offset:20480\n"
"v_cvt_pk_bf16_f32 v51, v206, v207\n"
"v_mfma_f32_16x16x32_bf16 v[128:131], a[128:131], a[16:19], v[128:131]\n"
"buffer_load_dwordx4 v228, s[40:43], s72 offen offset:512 lds\n"
"v_mul_f32 v208, s88, v208\n"
"v_mfma_f32_16x16x32_bf16 v[128:131], a[132:135], a[20:23], v[128:131]\n"
"s_waitcnt lgkmcnt(7)\n"
"ds_read_b64_tr_b16 a[162:163], v233 offset:20992\n"
"ds_read_b64_tr_b16 a[164:165], v233 offset:24576\n"
"v_mfma_f32_16x16x32_bf16 v[128:131], a[136:139], a[24:27], v[128:131]\n"
"ds_read_b64_tr_b16 a[166:167], v233 offset:25088\n"
"v_permlane16_swap_b32 v48, v50\n"
"v_mfma_f32_16x16x32_bf16 v[132:135], a[112:115], a[56:59], 0\n"
"ds_read_b64_tr_b16 a[168:169], v234 offset:0\n"
"v_permlane16_swap_b32 v49, v51\n"
"v_mfma_f32_16x16x32_bf16 v[132:135], a[116:119], a[60:63], v[132:135]\n"
"ds_read_b64_tr_b16 a[170:171], v234 offset:512\n"
"v_mul_f32 v209, s88, v209\n"
"v_mfma_f32_16x16x32_bf16 v[132:135], a[120:123], a[64:67], v[132:135]\n"
"s_add_u32 m0, s95, 85504\n"
"ds_read_b64_tr_b16 a[172:173], v234 offset:4096\n"
"v_mul_f32 v210, s88, v210\n"
"v_mfma_f32_16x16x32_bf16 v[132:135], a[124:127], a[68:71], v[132:135]\n"
"buffer_load_dwordx4 v228, s[40:43], s73 offen offset:512 lds\n"
"v_mul_f32 v211, s88, v211\n"
"v_mfma_f32_16x16x32_bf16 v[132:135], a[128:131], a[72:75], v[132:135]\n"
"ds_read_b64_tr_b16 a[174:175], v234 offset:4608\n"
"ds_read_b64_tr_b16 a[176:177], v234 offset:8192\n"
"v_mfma_f32_16x16x32_bf16 v[132:135], a[132:135], a[76:79], v[132:135]\n"
"ds_read_b64_tr_b16 a[178:179], v234 offset:8704\n"
"v_cvt_pk_bf16_f32 v20, v208, v209\n"
"v_mfma_f32_16x16x32_bf16 v[132:135], a[136:139], a[80:83], v[132:135]\n"
"ds_read_b64_tr_b16 a[180:181], v234 offset:12288\n"
"v_cvt_pk_bf16_f32 v21, v210, v211\n"
"v_mfma_f32_16x16x32_bf16 v[136:139], a[140:143], a[0:3], 0\n"
"ds_read_b64_tr_b16 a[182:183], v234 offset:12800\n"
"v_mul_f32 v216, s88, v216\n"
"v_mfma_f32_16x16x32_bf16 v[136:139], a[144:147], a[4:7], v[136:139]\n"
"s_waitcnt lgkmcnt(8)\n"
"s_add_u32 m0, s95, 89600\n"
"ds_read_b64_tr_b16 a[184:185], v234 offset:16384\n"
"v_mul_f32 v217, s88, v217\n"
"v_mfma_f32_16x16x32_bf16 v[136:139], a[148:151], a[8:11], v[136:139]\n"
"buffer_load_dwordx4 v228, s[40:43], s74 offen offset:512 lds\n"
"v_mul_f32 v218, s88, v218\n"
"v_mfma_f32_16x16x32_bf16 v[136:139], a[152:155], a[12:15], v[136:139]\n"
"ds_read_b64_tr_b16 a[186:187], v234 offset:16896\n"
"ds_read_b64_tr_b16 a[188:189], v234 offset:20480\n"
"v_mfma_f32_16x16x32_bf16 v[136:139], a[156:159], a[16:19], v[136:139]\n"
"ds_read_b64_tr_b16 a[190:191], v234 offset:20992\n"
"v_mul_f32 v219, s88, v219\n"
"v_mfma_f32_16x16x32_bf16 v[136:139], a[160:163], a[20:23], v[136:139]\n"
"ds_read_b64_tr_b16 a[192:193], v234 offset:24576\n"
"v_cvt_pk_bf16_f32 v22, v216, v217\n"
"v_mfma_f32_16x16x32_bf16 v[136:139], a[164:167], a[24:27], v[136:139]\n"
"ds_read_b64_tr_b16 a[194:195], v234 offset:25088\n"
"v_cvt_pk_bf16_f32 v23, v218, v219\n"
"v_mfma_f32_16x16x32_bf16 v[140:143], a[140:143], a[56:59], 0\n"
"s_add_u32 m0, s95, 93696\n"
"ds_read_b64_tr_b16 a[196:197], v235 offset:0\n"
"v_mul_f32 v212, s88, v212\n"
"v_mfma_f32_16x16x32_bf16 v[140:143], a[144:147], a[60:63], v[140:143]\n"
"buffer_load_dwordx4 v228, s[40:43], s75 offen offset:512 lds\n"
"v_permlane16_swap_b32 v20, v22\n"
"v_mfma_f32_16x16x32_bf16 v[140:143], a[148:151], a[64:67], v[140:143]\n"
"ds_read_b64_tr_b16 a[198:199], v235 offset:512\n"
"ds_read_b64_tr_b16 a[200:201], v235 offset:4096\n"
"v_mfma_f32_16x16x32_bf16 v[140:143], a[152:155], a[68:71], v[140:143]\n"
"ds_read_b64_tr_b16 a[202:203], v235 offset:4608\n"
"v_permlane16_swap_b32 v21, v23\n"
"v_mfma_f32_16x16x32_bf16 v[140:143], a[156:159], a[72:75], v[140:143]\n"
"ds_read_b64_tr_b16 a[204:205], v235 offset:8192\n"
"v_mul_f32 v213, s88, v213\n"
"v_mfma_f32_16x16x32_bf16 v[140:143], a[160:163], a[76:79], v[140:143]\n"
"ds_read_b64_tr_b16 a[206:207], v235 offset:8704\n"
"v_mul_f32 v214, s88, v214\n"
"v_mfma_f32_16x16x32_bf16 v[140:143], a[164:167], a[80:83], v[140:143]\n"
"s_waitcnt lgkmcnt(8)\n"
"s_add_u32 m0, s95, 97792\n"
"ds_read_b64_tr_b16 a[208:209], v235 offset:12288\n"
"v_mul_f32 v215, s88, v215\n"
"v_mfma_f32_16x16x32_bf16 v[144:147], a[168:171], a[0:3], 0\n"
"buffer_load_dwordx4 v228, s[40:43], s76 offen offset:512 lds\n"
"v_cvt_pk_bf16_f32 v52, v212, v213\n"
"v_mfma_f32_16x16x32_bf16 v[144:147], a[172:175], a[4:7], v[144:147]\n"
"ds_read_b64_tr_b16 a[210:211], v235 offset:12800\n"
"ds_read_b64_tr_b16 a[212:213], v235 offset:16384\n"
"v_mfma_f32_16x16x32_bf16 v[144:147], a[176:179], a[8:11], v[144:147]\n"
"ds_read_b64_tr_b16 a[214:215], v235 offset:16896\n"
"v_cvt_pk_bf16_f32 v53, v214, v215\n"
"v_mfma_f32_16x16x32_bf16 v[144:147], a[180:183], a[12:15], v[144:147]\n"
"ds_read_b64_tr_b16 a[216:217], v235 offset:20480\n"
"v_mul_f32 v220, s88, v220\n"
"v_mfma_f32_16x16x32_bf16 v[144:147], a[184:187], a[16:19], v[144:147]\n"
"ds_read_b64_tr_b16 a[218:219], v235 offset:20992\n"
"v_mul_f32 v221, s88, v221\n"
"v_mfma_f32_16x16x32_bf16 v[144:147], a[188:191], a[20:23], v[144:147]\n"
"s_waitcnt vmcnt(13) lgkmcnt(8)\n"
"ds_read_b64_tr_b16 a[220:221], v235 offset:24576\n"
"v_mul_f32 v222, s88, v222\n"
"v_mfma_f32_16x16x32_bf16 v[144:147], a[192:195], a[24:27], v[144:147]\n"
"s_barrier\n"
"ds_read_b64_tr_b16 a[222:223], v235 offset:25088\n"
"v_mul_f32 v223, s88, v223\n"
"v_mfma_f32_16x16x32_bf16 v[148:151], a[168:171], a[56:59], 0\n"
"s_add_u32 m0, s95, 101888\n"
"ds_read_b64_tr_b16 a[112:113], v240 offset:0\n"
"v_cvt_pk_bf16_f32 v54, v220, v221\n"
"v_mfma_f32_16x16x32_bf16 v[148:151], a[172:175], a[60:63], v[148:151]\n"
"buffer_load_dwordx4 v228, s[40:43], s77 offen offset:512 lds\n"
"v_cvt_pk_bf16_f32 v55, v222, v223\n"
"v_mfma_f32_16x16x32_bf16 v[148:151], a[176:179], a[64:67], v[148:151]\n"
"ds_read_b64_tr_b16 a[114:115], v240 offset:512\n"
"ds_read_b64_tr_b16 a[116:117], v240 offset:4096\n"
"v_mfma_f32_16x16x32_bf16 v[148:151], a[180:183], a[68:71], v[148:151]\n"
"ds_read_b64_tr_b16 a[118:119], v240 offset:4608\n"
"v_permlane16_swap_b32 v52, v54\n"
"v_mfma_f32_16x16x32_bf16 v[148:151], a[184:187], a[72:75], v[148:151]\n"
"ds_read_b64_tr_b16 a[120:121], v240 offset:8192\n"
"v_permlane16_swap_b32 v53, v55\n"
"v_mfma_f32_16x16x32_bf16 v[148:151], a[188:191], a[76:79], v[148:151]\n"
"ds_read_b64_tr_b16 a[122:123], v240 offset:8704\n"
"v_mfma_f32_16x16x32_bf16 v[148:151], a[192:195], a[80:83], v[148:151]\n"
"s_add_u32 m0, s95, 105856\n"
"ds_read_b64_tr_b16 a[124:125], v240 offset:12288\n"
"v_mfma_f32_16x16x32_bf16 v[152:155], a[196:199], a[0:3], 0\n"
"buffer_load_dwordx4 v228, s[40:43], s64 offen offset:640 lds\n"
"v_mfma_f32_16x16x32_bf16 v[152:155], a[200:203], a[4:7], v[152:155]\n"
"s_waitcnt lgkmcnt(7)\n"
"ds_read_b64_tr_b16 a[126:127], v240 offset:12800\n"
"ds_read_b64_tr_b16 a[128:129], v240 offset:16384\n"
"v_mfma_f32_16x16x32_bf16 v[152:155], a[204:207], a[8:11], v[152:155]\n"
"ds_read_b64_tr_b16 a[130:131], v240 offset:16896\n"
"v_mfma_f32_16x16x32_bf16 v[152:155], a[208:211], a[12:15], v[152:155]\n"
"ds_read_b64_tr_b16 a[132:133], v240 offset:20480\n"
"v_mfma_f32_16x16x32_bf16 v[152:155], a[212:215], a[16:19], v[152:155]\n"
"ds_read_b64_tr_b16 a[134:135], v240 offset:20992\n"
"v_mfma_f32_16x16x32_bf16 v[152:155], a[216:219], a[20:23], v[152:155]\n"
"s_add_u32 m0, s95, 109952\n"
"ds_read_b64_tr_b16 a[136:137], v240 offset:24576\n"
"v_mfma_f32_16x16x32_bf16 v[152:155], a[220:223], a[24:27], v[152:155]\n"
"buffer_load_dwordx4 v228, s[40:43], s65 offen offset:640 lds\n"
"v_mfma_f32_16x16x32_bf16 v[156:159], a[196:199], a[56:59], 0\n"
"ds_read_b64_tr_b16 a[138:139], v240 offset:25088\n"
"ds_read_b64_tr_b16 a[140:141], v241 offset:0\n"
"v_mfma_f32_16x16x32_bf16 v[156:159], a[200:203], a[60:63], v[156:159]\n"
"ds_read_b64_tr_b16 a[142:143], v241 offset:512\n"
"v_mfma_f32_16x16x32_bf16 v[156:159], a[204:207], a[64:67], v[156:159]\n"
"ds_read_b64_tr_b16 a[144:145], v241 offset:4096\n"
"v_mfma_f32_16x16x32_bf16 v[156:159], a[208:211], a[68:71], v[156:159]\n"
"ds_read_b64_tr_b16 a[146:147], v241 offset:4608\n"
"v_mfma_f32_16x16x32_bf16 v[156:159], a[212:215], a[72:75], v[156:159]\n"
"s_add_u32 m0, s95, 114048\n"
"ds_read_b64_tr_b16 a[148:149], v241 offset:8192\n"
"v_mfma_f32_16x16x32_bf16 v[156:159], a[216:219], a[76:79], v[156:159]\n"
"buffer_load_dwordx4 v228, s[40:43], s66 offen offset:640 lds\n"
"v_mfma_f32_16x16x32_bf16 v[156:159], a[220:223], a[80:83], v[156:159]\n"
"s_waitcnt lgkmcnt(7)\n"
"ds_read_b64_tr_b16 a[150:151], v241 offset:8704\n"
"ds_read_b64_tr_b16 a[152:153], v241 offset:12288\n"
"v_mfma_f32_16x16x32_bf16 v[128:131], a[112:115], a[28:31], v[128:131]\n"
"ds_read_b64_tr_b16 a[154:155], v241 offset:12800\n"
"v_mfma_f32_16x16x32_bf16 v[128:131], a[116:119], a[32:35], v[128:131]\n"
"ds_read_b64_tr_b16 a[156:157], v241 offset:16384\n"
"v_mfma_f32_16x16x32_bf16 v[128:131], a[120:123], a[36:39], v[128:131]\n"
"ds_read_b64_tr_b16 a[158:159], v241 offset:16896\n"
"v_mfma_f32_16x16x32_bf16 v[128:131], a[124:127], a[40:43], v[128:131]\n"
"s_add_u32 m0, s95, 118144\n"
"ds_read_b64_tr_b16 a[160:161], v241 offset:20480\n"
"v_mfma_f32_16x16x32_bf16 v[128:131], a[128:131], a[44:47], v[128:131]\n"
"buffer_load_dwordx4 v228, s[40:43], s67 offen offset:640 lds\n"
"v_mfma_f32_16x16x32_bf16 v[128:131], a[132:135], a[48:51], v[128:131]\n"
"s_waitcnt lgkmcnt(7)\n"
"ds_read_b64_tr_b16 a[162:163], v241 offset:20992\n"
"ds_read_b64_tr_b16 a[164:165], v241 offset:24576\n"
"v_mfma_f32_16x16x32_bf16 v[128:131], a[136:139], a[52:55], v[128:131]\n"
"ds_read_b64_tr_b16 a[166:167], v241 offset:25088\n"
"v_mfma_f32_16x16x32_bf16 v[132:135], a[112:115], a[84:87], v[132:135]\n"
"ds_read_b64_tr_b16 a[168:169], v242 offset:0\n"
"v_mfma_f32_16x16x32_bf16 v[132:135], a[116:119], a[88:91], v[132:135]\n"
"s_bfe_u32 s28, s35, 0x50018\n"
"ds_read_b64_tr_b16 a[170:171], v242 offset:512\n"
"v_mfma_f32_16x16x32_bf16 v[132:135], a[120:123], a[92:95], v[132:135]\n"
"s_add_u32 m0, s95, 122240\n"
"ds_read_b64_tr_b16 a[172:173], v242 offset:4096\n"
"v_mul_f32 v128, s88, v128\n"
"v_mfma_f32_16x16x32_bf16 v[132:135], a[124:127], a[96:99], v[132:135]\n"
"s_lshl_b32 s28, s28, 10\n"
"buffer_load_dwordx4 v228, s[40:43], s68 offen offset:640 lds\n"
"v_mul_f32 v129, s88, v129\n"
"v_mfma_f32_16x16x32_bf16 v[132:135], a[128:131], a[100:103], v[132:135]\n"
"s_add_u32 s28, s28, Lcodeend%=-Lcodepc%=-32768\n"
"ds_read_b64_tr_b16 a[174:175], v242 offset:4608\n"
"ds_read_b64_tr_b16 a[176:177], v242 offset:8192\n"
"v_mfma_f32_16x16x32_bf16 v[132:135], a[132:135], a[104:107], v[132:135]\n"
"s_add_u32 s28, s28, s78\n"
"ds_read_b64_tr_b16 a[178:179], v242 offset:8704\n"
"v_mul_f32 v130, s88, v130\n"
"v_mfma_f32_16x16x32_bf16 v[132:135], a[136:139], a[108:111], v[132:135]\n"
"s_addc_u32 s29, s79, 0\n"
"ds_read_b64_tr_b16 a[180:181], v242 offset:12288\n"
"v_mul_f32 v131, s88, v131\n"
"v_mfma_f32_16x16x32_bf16 v[136:139], a[140:143], a[28:31], v[136:139]\n"
"s_and_b32 s29, s29, 0xffff\n"
"ds_read_b64_tr_b16 a[182:183], v242 offset:12800\n"
"v_cvt_pk_bf16_f32 v24, v128, v129\n"
"v_mfma_f32_16x16x32_bf16 v[136:139], a[144:147], a[32:35], v[136:139]\n"
"s_waitcnt lgkmcnt(8)\n"
"s_add_u32 m0, s95, 126336\n"
"ds_read_b64_tr_b16 a[184:185], v242 offset:16384\n"
"v_cvt_pk_bf16_f32 v25, v130, v131\n"
"v_mfma_f32_16x16x32_bf16 v[136:139], a[148:151], a[36:39], v[136:139]\n"
"s_or_b32 s29, s29, 0x80000\n"
"buffer_load_dwordx4 v228, s[40:43], s69 offen offset:640 lds\n"
"v_mul_f32 v132, s88, v132\n"
"v_mfma_f32_16x16x32_bf16 v[136:139], a[152:155], a[40:43], v[136:139]\n"
"s_mov_b32 s31, 0x20000\n"
"ds_read_b64_tr_b16 a[186:187], v242 offset:16896\n"
"ds_read_b64_tr_b16 a[188:189], v242 offset:20480\n"
"v_mfma_f32_16x16x32_bf16 v[136:139], a[156:159], a[44:47], v[136:139]\n"
"ds_read_b64_tr_b16 a[190:191], v242 offset:20992\n"
"v_mul_f32 v133, s88, v133\n"
"v_mfma_f32_16x16x32_bf16 v[136:139], a[160:163], a[48:51], v[136:139]\n"
"ds_read_b64_tr_b16 a[192:193], v242 offset:24576\n"
"v_mul_f32 v134, s88, v134\n"
"v_mfma_f32_16x16x32_bf16 v[136:139], a[164:167], a[52:55], v[136:139]\n"
"ds_read_b64_tr_b16 a[194:195], v242 offset:25088\n"
"v_mul_f32 v135, s88, v135\n"
"v_mfma_f32_16x16x32_bf16 v[140:143], a[140:143], a[84:87], v[140:143]\n"
"s_add_u32 m0, s95, 130432\n"
"ds_read_b64_tr_b16 a[196:197], v243 offset:0\n"
"v_cvt_pk_bf16_f32 v56, v132, v133\n"
"v_mfma_f32_16x16x32_bf16 v[140:143], a[144:147], a[88:91], v[140:143]\n"
"buffer_load_dwordx4 v228, s[40:43], s70 offen offset:640 lds\n"
"v_cvt_pk_bf16_f32 v57, v134, v135\n"
"v_mfma_f32_16x16x32_bf16 v[140:143], a[148:151], a[92:95], v[140:143]\n"
"ds_read_b64_tr_b16 a[198:199], v243 offset:512\n"
"ds_read_b64_tr_b16 a[200:201], v243 offset:4096\n"
"v_mfma_f32_16x16x32_bf16 v[140:143], a[152:155], a[96:99], v[140:143]\n"
"ds_read_b64_tr_b16 a[202:203], v243 offset:4608\n"
"v_mul_f32 v136, s88, v136\n"
"v_mfma_f32_16x16x32_bf16 v[140:143], a[156:159], a[100:103], v[140:143]\n"
"ds_read_b64_tr_b16 a[204:205], v243 offset:8192\n"
"v_mul_f32 v137, s88, v137\n"
"v_mfma_f32_16x16x32_bf16 v[140:143], a[160:163], a[104:107], v[140:143]\n"
"ds_read_b64_tr_b16 a[206:207], v243 offset:8704\n"
"v_mul_f32 v138, s88, v138\n"
"v_mfma_f32_16x16x32_bf16 v[140:143], a[164:167], a[108:111], v[140:143]\n"
"s_waitcnt lgkmcnt(8)\n"
"s_add_u32 m0, s82, 0\n"
"ds_read_b64_tr_b16 a[208:209], v243 offset:12288\n"
"v_mul_f32 v139, s88, v139\n"
"v_mfma_f32_16x16x32_bf16 v[144:147], a[168:171], a[28:31], v[144:147]\n"
"buffer_load_dwordx4 v255, s[44:47], s84 offen offset:0 lds\n"
"v_cvt_pk_bf16_f32 v26, v136, v137\n"
"v_mfma_f32_16x16x32_bf16 v[144:147], a[172:175], a[32:35], v[144:147]\n"
"ds_read_b64_tr_b16 a[210:211], v243 offset:12800\n"
"ds_read_b64_tr_b16 a[212:213], v243 offset:16384\n"
"v_mfma_f32_16x16x32_bf16 v[144:147], a[176:179], a[36:39], v[144:147]\n"
"ds_read_b64_tr_b16 a[214:215], v243 offset:16896\n"
"v_cvt_pk_bf16_f32 v27, v138, v139\n"
"v_mfma_f32_16x16x32_bf16 v[144:147], a[180:183], a[40:43], v[144:147]\n"
"s_add_u32 m0, s82, 544\n"
"ds_read_b64_tr_b16 a[216:217], v243 offset:20480\n"
"v_mul_f32 v140, s88, v140\n"
"v_mfma_f32_16x16x32_bf16 v[144:147], a[184:187], a[44:47], v[144:147]\n"
"buffer_load_dwordx4 v255, s[44:47], s84 offen offset:512 lds\n"
"v_permlane16_swap_b32 v24, v26\n"
"v_mfma_f32_16x16x32_bf16 v[144:147], a[188:191], a[48:51], v[144:147]\n"
"s_waitcnt vmcnt(16) lgkmcnt(7)\n"
"ds_read_b64_tr_b16 a[218:219], v243 offset:20992\n"
"ds_read_b64_tr_b16 a[220:221], v243 offset:24576\n"
"v_mfma_f32_16x16x32_bf16 v[144:147], a[192:195], a[52:55], v[144:147]\n"
"s_barrier\n"
"ds_read_b64_tr_b16 a[222:223], v243 offset:25088\n"
"v_permlane16_swap_b32 v25, v27\n"
"v_mfma_f32_16x16x32_bf16 v[148:151], a[168:171], a[84:87], v[148:151]\n"
"s_add_u32 m0, s82, 1088\n"
"ds_read_b64_tr_b16 a[112:113], v232 offset:28672\n"
"v_mul_f32 v141, s88, v141\n"
"v_mfma_f32_16x16x32_bf16 v[148:151], a[172:175], a[88:91], v[148:151]\n"
"buffer_load_dwordx4 v255, s[44:47], s84 offen offset:1024 nt lds\n"
"v_mul_f32 v142, s88, v142\n"
"v_mfma_f32_16x16x32_bf16 v[148:151], a[176:179], a[92:95], v[148:151]\n"
"ds_read_b64_tr_b16 a[114:115], v232 offset:29184\n"
"ds_read_b64_tr_b16 a[116:117], v232 offset:32768\n"
"v_mfma_f32_16x16x32_bf16 v[148:151], a[180:183], a[96:99], v[148:151]\n"
"ds_read_b64_tr_b16 a[118:119], v232 offset:33280\n"
"v_mul_f32 v143, s88, v143\n"
"v_mfma_f32_16x16x32_bf16 v[148:151], a[184:187], a[100:103], v[148:151]\n"
"s_add_u32 m0, s82, 1632\n"
"ds_read_b64_tr_b16 a[120:121], v232 offset:36864\n"
"v_cvt_pk_bf16_f32 v58, v140, v141\n"
"v_mfma_f32_16x16x32_bf16 v[148:151], a[188:191], a[104:107], v[148:151]\n"
"buffer_load_dwordx4 v255, s[44:47], s84 offen offset:1536 nt lds\n"
"v_cvt_pk_bf16_f32 v59, v142, v143\n"
"v_mfma_f32_16x16x32_bf16 v[148:151], a[192:195], a[108:111], v[148:151]\n"
"ds_read_b64_tr_b16 a[122:123], v232 offset:37376\n"
"ds_read_b64_tr_b16 a[124:125], v232 offset:40960\n"
"v_mfma_f32_16x16x32_bf16 v[152:155], a[196:199], a[28:31], v[152:155]\n"
"ds_read_b64_tr_b16 a[126:127], v232 offset:41472\n"
"v_permlane16_swap_b32 v56, v58\n"
"v_mfma_f32_16x16x32_bf16 v[152:155], a[200:203], a[32:35], v[152:155]\n"
"s_waitcnt lgkmcnt(8)\n"
"s_add_u32 s84, s84, 0x4000\n"
"ds_read_b64_tr_b16 a[128:129], v232 offset:45056\n"
"v_permlane16_swap_b32 v57, v59\n"
"v_mfma_f32_16x16x32_bf16 v[152:155], a[204:207], a[36:39], v[152:155]\n"
"s_cmp_lt_u32 s84, 0x80000\n"
"ds_read_b64_tr_b16 a[130:131], v232 offset:45568\n"
"v_mul_f32 v144, s88, v144\n"
"v_mfma_f32_16x16x32_bf16 v[152:155], a[208:211], a[40:43], v[152:155]\n"
"s_cselect_b32 s46, 0x80000, 0\n"
"ds_read_b64_tr_b16 a[132:133], v232 offset:49152\n"
"v_mul_f32 v145, s88, v145\n"
"v_mfma_f32_16x16x32_bf16 v[152:155], a[212:215], a[44:47], v[152:155]\n"
"s_add_u32 m0, s95, 19840\n"
"ds_read_b64_tr_b16 a[134:135], v232 offset:49664\n"
"v_mul_f32 v146, s88, v146\n"
"v_mfma_f32_16x16x32_bf16 v[152:155], a[216:219], a[48:51], v[152:155]\n"
"buffer_load_dwordx4 v228, s[40:43], s71 offen offset:640 lds\n"
"v_mul_f32 v147, s88, v147\n"
"v_mfma_f32_16x16x32_bf16 v[152:155], a[220:223], a[52:55], v[152:155]\n"
"ds_read_b64_tr_b16 a[136:137], v232 offset:53248\n"
"ds_read_b64_tr_b16 a[138:139], v232 offset:53760\n"
"v_mfma_f32_16x16x32_bf16 v[156:159], a[196:199], a[84:87], v[156:159]\n"
"ds_read_b64_tr_b16 a[140:141], v233 offset:28672\n"
"v_cvt_pk_bf16_f32 v28, v144, v145\n"
"v_mfma_f32_16x16x32_bf16 v[156:159], a[200:203], a[88:91], v[156:159]\n"
"ds_read_b64_tr_b16 a[142:143], v233 offset:29184\n"
"v_cvt_pk_bf16_f32 v29, v146, v147\n"
"v_mfma_f32_16x16x32_bf16 v[156:159], a[204:207], a[92:95], v[156:159]\n"
"ds_read_b64_tr_b16 a[144:145], v233 offset:32768\n"
"v_mul_f32 v152, s88, v152\n"
"v_mfma_f32_16x16x32_bf16 v[156:159], a[208:211], a[96:99], v[156:159]\n"
"s_add_u32 m0, s95, 23936\n"
"ds_read_b64_tr_b16 a[146:147], v233 offset:33280\n"
"v_mul_f32 v153, s88, v153\n"
"v_mfma_f32_16x16x32_bf16 v[156:159], a[212:215], a[100:103], v[156:159]\n"
"buffer_load_dwordx4 v228, s[40:43], s72 offen offset:640 lds\n"
"v_mul_f32 v154, s88, v154\n"
"v_mfma_f32_16x16x32_bf16 v[156:159], a[216:219], a[104:107], v[156:159]\n"
"ds_read_b64_tr_b16 a[148:149], v233 offset:36864\n"
"ds_read_b64_tr_b16 a[150:151], v233 offset:37376\n"
"v_mfma_f32_16x16x32_bf16 v[156:159], a[220:223], a[108:111], v[156:159]\n"
"s_waitcnt lgkmcnt(8)\n"
"ds_read_b64_tr_b16 a[152:153], v233 offset:40960\n"
"v_mul_f32 v155, s88, v155\n"
"v_mfma_f32_16x16x32_bf16 v[160:163], a[112:115], a[0:3], 0\n"
"ds_read_b64_tr_b16 a[154:155], v233 offset:41472\n"
"v_cvt_pk_bf16_f32 v30, v152, v153\n"
"v_mfma_f32_16x16x32_bf16 v[160:163], a[116:119], a[4:7], v[160:163]\n"
"ds_read_b64_tr_b16 a[156:157], v233 offset:45056\n"
"v_cvt_pk_bf16_f32 v31, v154, v155\n"
"v_mfma_f32_16x16x32_bf16 v[160:163], a[120:123], a[8:11], v[160:163]\n"
"s_add_u32 m0, s95, 28032\n"
"ds_read_b64_tr_b16 a[158:159], v233 offset:45568\n"
"v_mul_f32 v148, s88, v148\n"
"v_mfma_f32_16x16x32_bf16 v[160:163], a[124:127], a[12:15], v[160:163]\n"
"buffer_load_dwordx4 v228, s[40:43], s73 offen offset:640 lds\n"
"v_permlane16_swap_b32 v28, v30\n"
"v_mfma_f32_16x16x32_bf16 v[160:163], a[128:131], a[16:19], v[160:163]\n"
"ds_read_b64_tr_b16 a[160:161], v233 offset:49152\n"
"ds_read_b64_tr_b16 a[162:163], v233 offset:49664\n"
"v_mfma_f32_16x16x32_bf16 v[160:163], a[132:135], a[20:23], v[160:163]\n"
"s_waitcnt lgkmcnt(8)\n"
"ds_read_b64_tr_b16 a[164:165], v233 offset:53248\n"
"v_permlane16_swap_b32 v29, v31\n"
"v_mfma_f32_16x16x32_bf16 v[160:163], a[136:139], a[24:27], v[160:163]\n"
"ds_read_b64_tr_b16 a[166:167], v233 offset:53760\n"
"v_mul_f32 v149, s88, v149\n"
"v_mfma_f32_16x16x32_bf16 v[164:167], a[112:115], a[56:59], 0\n"
"ds_read_b64_tr_b16 a[168:169], v234 offset:28672\n"
"v_mul_f32 v150, s88, v150\n"
"v_mfma_f32_16x16x32_bf16 v[164:167], a[116:119], a[60:63], v[164:167]\n"
"s_add_u32 m0, s95, 32128\n"
"ds_read_b64_tr_b16 a[170:171], v234 offset:29184\n"
"v_mul_f32 v151, s88, v151\n"
"v_mfma_f32_16x16x32_bf16 v[164:167], a[120:123], a[64:67], v[164:167]\n"
"buffer_load_dwordx4 v228, s[40:43], s74 offen offset:640 lds\n"
"v_cvt_pk_bf16_f32 v60, v148, v149\n"
"v_mfma_f32_16x16x32_bf16 v[164:167], a[124:127], a[68:71], v[164:167]\n"
"ds_read_b64_tr_b16 a[172:173], v234 offset:32768\n"
"ds_read_b64_tr_b16 a[174:175], v234 offset:33280\n"
"v_mfma_f32_16x16x32_bf16 v[164:167], a[128:131], a[72:75], v[164:167]\n"
"ds_read_b64_tr_b16 a[176:177], v234 offset:36864\n"
"v_cvt_pk_bf16_f32 v61, v150, v151\n"
"v_mfma_f32_16x16x32_bf16 v[164:167], a[132:135], a[76:79], v[164:167]\n"
"ds_read_b64_tr_b16 a[178:179], v234 offset:37376\n"
"v_mul_f32 v156, s88, v156\n"
"v_mfma_f32_16x16x32_bf16 v[164:167], a[136:139], a[80:83], v[164:167]\n"
"ds_read_b64_tr_b16 a[180:181], v234 offset:40960\n"
"v_mul_f32 v157, s88, v157\n"
"v_mfma_f32_16x16x32_bf16 v[168:171], a[140:143], a[0:3], 0\n"
"s_add_u32 m0, s95, 36224\n"
"ds_read_b64_tr_b16 a[182:183], v234 offset:41472\n"
"v_mul_f32 v158, s88, v158\n"
"v_mfma_f32_16x16x32_bf16 v[168:171], a[144:147], a[4:7], v[168:171]\n"
"s_waitcnt lgkmcnt(8)\n"
"buffer_load_dwordx4 v228, s[40:43], s75 offen offset:640 lds\n"
"v_mul_f32 v159, s88, v159\n"
"v_mfma_f32_16x16x32_bf16 v[168:171], a[148:151], a[8:11], v[168:171]\n"
"ds_read_b64_tr_b16 a[184:185], v234 offset:45056\n"
"ds_read_b64_tr_b16 a[186:187], v234 offset:45568\n"
"v_mfma_f32_16x16x32_bf16 v[168:171], a[152:155], a[12:15], v[168:171]\n"
"ds_read_b64_tr_b16 a[188:189], v234 offset:49152\n"
"v_cvt_pk_bf16_f32 v62, v156, v157\n"
"v_mfma_f32_16x16x32_bf16 v[168:171], a[156:159], a[16:19], v[168:171]\n"
"ds_read_b64_tr_b16 a[190:191], v234 offset:49664\n"
"v_cvt_pk_bf16_f32 v63, v158, v159\n"
"v_mfma_f32_16x16x32_bf16 v[168:171], a[160:163], a[20:23], v[168:171]\n"
"ds_read_b64_tr_b16 a[192:193], v234 offset:53248\n"
"v_mfma_f32_16x16x32_bf16 v[168:171], a[164:167], a[24:27], v[168:171]\n"
"s_add_u32 m0, s95, 40320\n"
"ds_read_b64_tr_b16 a[194:195], v234 offset:53760\n"
"v_permlane16_swap_b32 v60, v62\n"
"v_mfma_f32_16x16x32_bf16 v[172:175], a[140:143], a[56:59], 0\n"
"buffer_load_dwordx4 v228, s[40:43], s76 offen offset:640 lds\n"
"v_permlane16_swap_b32 v61, v63\n"
"v_mfma_f32_16x16x32_bf16 v[172:175], a[144:147], a[60:63], v[172:175]\n"
"ds_read_b64_tr_b16 a[196:197], v235 offset:28672\n"
"ds_read_b64_tr_b16 a[198:199], v235 offset:29184\n"
"v_mfma_f32_16x16x32_bf16 v[172:175], a[148:151], a[64:67], v[172:175]\n"
"ds_read_b64_tr_b16 a[200:201], v235 offset:32768\n"
"v_mfma_f32_16x16x32_bf16 v[172:175], a[152:155], a[68:71], v[172:175]\n"
"ds_read_b64_tr_b16 a[202:203], v235 offset:33280\n"
"v_mfma_f32_16x16x32_bf16 v[172:175], a[156:159], a[72:75], v[172:175]\n"
"ds_read_b64_tr_b16 a[204:205], v235 offset:36864\n"
"v_mfma_f32_16x16x32_bf16 v[172:175], a[160:163], a[76:79], v[172:175]\n"
"s_add_u32 m0, s95, 44416\n"
"ds_read_b64_tr_b16 a[206:207], v235 offset:37376\n"
"v_mfma_f32_16x16x32_bf16 v[172:175], a[164:167], a[80:83], v[172:175]\n"
"s_waitcnt lgkmcnt(8)\n"
"buffer_load_dwordx4 v228, s[40:43], s77 offen offset:640 lds\n"
"v_mfma_f32_16x16x32_bf16 v[176:179], a[168:171], a[0:3], 0\n"
"ds_read_b64_tr_b16 a[208:209], v235 offset:40960\n"
"ds_read_b64_tr_b16 a[210:211], v235 offset:41472\n"
"v_mfma_f32_16x16x32_bf16 v[176:179], a[172:175], a[4:7], v[176:179]\n"
"ds_read_b64_tr_b16 a[212:213], v235 offset:45056\n"
"v_mfma_f32_16x16x32_bf16 v[176:179], a[176:179], a[8:11], v[176:179]\n"
"ds_read_b64_tr_b16 a[214:215], v235 offset:45568\n"
"v_mfma_f32_16x16x32_bf16 v[176:179], a[180:183], a[12:15], v[176:179]\n"
"ds_read_b64_tr_b16 a[216:217], v235 offset:49152\n"
"v_mfma_f32_16x16x32_bf16 v[176:179], a[184:187], a[16:19], v[176:179]\n"
"ds_read_b64_tr_b16 a[218:219], v235 offset:49664\n"
"v_mfma_f32_16x16x32_bf16 v[176:179], a[188:191], a[20:23], v[176:179]\n"
"s_waitcnt vmcnt(18) lgkmcnt(8)\n"
"ds_read_b64_tr_b16 a[220:221], v235 offset:53248\n"
"v_mfma_f32_16x16x32_bf16 v[176:179], a[192:195], a[24:27], v[176:179]\n"
"s_barrier\n"
"ds_read_b64_tr_b16 a[222:223], v235 offset:53760\n"
"v_mfma_f32_16x16x32_bf16 v[180:183], a[168:171], a[56:59], 0\n"
"s_add_u32 m0, s95, 134400\n"
"ds_read_b64_tr_b16 a[112:113], v236 offset:0\n"
"v_mfma_f32_16x16x32_bf16 v[180:183], a[172:175], a[60:63], v[180:183]\n"
"buffer_load_dwordx4 v228, s[40:43], s64 offen offset:768 lds\n"
"v_mfma_f32_16x16x32_bf16 v[180:183], a[176:179], a[64:67], v[180:183]\n"
"ds_read_b64_tr_b16 a[114:115], v236 offset:512\n"
"ds_read_b64_tr_b16 a[116:117], v236 offset:4096\n"
"v_mfma_f32_16x16x32_bf16 v[180:183], a[180:183], a[68:71], v[180:183]\n"
"ds_read_b64_tr_b16 a[118:119], v236 offset:4608\n"
"v_mfma_f32_16x16x32_bf16 v[180:183], a[184:187], a[72:75], v[180:183]\n"
"ds_read_b64_tr_b16 a[120:121], v236 offset:8192\n"
"v_mfma_f32_16x16x32_bf16 v[180:183], a[188:191], a[76:79], v[180:183]\n"
"ds_read_b64_tr_b16 a[122:123], v236 offset:8704\n"
"v_mfma_f32_16x16x32_bf16 v[180:183], a[192:195], a[80:83], v[180:183]\n"
"s_add_u32 m0, s95, 138496\n"
"ds_read_b64_tr_b16 a[124:125], v236 offset:12288\n"
"v_mfma_f32_16x16x32_bf16 v[184:187], a[196:199], a[0:3], 0\n"
"buffer_load_dwordx4 v228, s[40:43], s65 offen offset:768 lds\n"
"v_mfma_f32_16x16x32_bf16 v[184:187], a[200:203], a[4:7], v[184:187]\n"
"s_waitcnt lgkmcnt(7)\n"
"ds_read_b64_tr_b16 a[126:127], v236 offset:12800\n"
"ds_read_b64_tr_b16 a[128:129], v236 offset:16384\n"
"v_mfma_f32_16x16x32_bf16 v[184:187], a[204:207], a[8:11], v[184:187]\n"
"ds_read_b64_tr_b16 a[130:131], v236 offset:16896\n"
"v_mfma_f32_16x16x32_bf16 v[184:187], a[208:211], a[12:15], v[184:187]\n"
"ds_read_b64_tr_b16 a[132:133], v236 offset:20480\n"
"v_mfma_f32_16x16x32_bf16 v[184:187], a[212:215], a[16:19], v[184:187]\n"
"ds_read_b64_tr_b16 a[134:135], v236 offset:20992\n"
"v_mfma_f32_16x16x32_bf16 v[184:187], a[216:219], a[20:23], v[184:187]\n"
"s_add_u32 m0, s95, 142592\n"
"ds_read_b64_tr_b16 a[136:137], v236 offset:24576\n"
"v_mfma_f32_16x16x32_bf16 v[184:187], a[220:223], a[24:27], v[184:187]\n"
"buffer_load_dwordx4 v228, s[40:43], s66 offen offset:768 lds\n"
"v_mfma_f32_16x16x32_bf16 v[188:191], a[196:199], a[56:59], 0\n"
"ds_read_b64_tr_b16 a[138:139], v236 offset:25088\n"
"ds_read_b64_tr_b16 a[140:141], v237 offset:0\n"
"v_mfma_f32_16x16x32_bf16 v[188:191], a[200:203], a[60:63], v[188:191]\n"
"ds_read_b64_tr_b16 a[142:143], v237 offset:512\n"
"v_mfma_f32_16x16x32_bf16 v[188:191], a[204:207], a[64:67], v[188:191]\n"
"ds_read_b64_tr_b16 a[144:145], v237 offset:4096\n"
"v_mfma_f32_16x16x32_bf16 v[188:191], a[208:211], a[68:71], v[188:191]\n"
"ds_read_b64_tr_b16 a[146:147], v237 offset:4608\n"
"v_mfma_f32_16x16x32_bf16 v[188:191], a[212:215], a[72:75], v[188:191]\n"
"s_add_u32 m0, s95, 146688\n"
"ds_read_b64_tr_b16 a[148:149], v237 offset:8192\n"
"v_mfma_f32_16x16x32_bf16 v[188:191], a[216:219], a[76:79], v[188:191]\n"
"buffer_load_dwordx4 v228, s[40:43], s67 offen offset:768 lds\n"
"v_mfma_f32_16x16x32_bf16 v[188:191], a[220:223], a[80:83], v[188:191]\n"
"s_waitcnt lgkmcnt(7)\n"
"ds_read_b64_tr_b16 a[150:151], v237 offset:8704\n"
"ds_read_b64_tr_b16 a[152:153], v237 offset:12288\n"
"v_mfma_f32_16x16x32_bf16 v[160:163], a[112:115], a[28:31], v[160:163]\n"
"ds_read_b64_tr_b16 a[154:155], v237 offset:12800\n"
"v_mfma_f32_16x16x32_bf16 v[160:163], a[116:119], a[32:35], v[160:163]\n"
"ds_read_b64_tr_b16 a[156:157], v237 offset:16384\n"
"v_mfma_f32_16x16x32_bf16 v[160:163], a[120:123], a[36:39], v[160:163]\n"
"ds_read_b64_tr_b16 a[158:159], v237 offset:16896\n"
"v_mfma_f32_16x16x32_bf16 v[160:163], a[124:127], a[40:43], v[160:163]\n"
"s_add_u32 m0, s95, 150784\n"
"ds_read_b64_tr_b16 a[160:161], v237 offset:20480\n"
"v_mfma_f32_16x16x32_bf16 v[160:163], a[128:131], a[44:47], v[160:163]\n"
"buffer_load_dwordx4 v228, s[40:43], s68 offen offset:768 lds\n"
"v_mfma_f32_16x16x32_bf16 v[160:163], a[132:135], a[48:51], v[160:163]\n"
"s_waitcnt lgkmcnt(7)\n"
"ds_read_b64_tr_b16 a[162:163], v237 offset:20992\n"
"ds_read_b64_tr_b16 a[164:165], v237 offset:24576\n"
"v_mfma_f32_16x16x32_bf16 v[160:163], a[136:139], a[52:55], v[160:163]\n"
"ds_read_b64_tr_b16 a[166:167], v237 offset:25088\n"
"v_mfma_f32_16x16x32_bf16 v[164:167], a[112:115], a[84:87], v[164:167]\n"
"ds_read_b64_tr_b16 a[168:169], v238 offset:0\n"
"v_mfma_f32_16x16x32_bf16 v[164:167], a[116:119], a[88:91], v[164:167]\n"
"ds_read_b64_tr_b16 a[170:171], v238 offset:512\n"
"v_mfma_f32_16x16x32_bf16 v[164:167], a[120:123], a[92:95], v[164:167]\n"
"s_add_u32 m0, s95, 154880\n"
"ds_read_b64_tr_b16 a[172:173], v238 offset:4096\n"
"v_mul_f32 v160, s88, v160\n"
"v_mfma_f32_16x16x32_bf16 v[164:167], a[124:127], a[96:99], v[164:167]\n"
"buffer_load_dwordx4 v228, s[40:43], s69 offen offset:768 lds\n"
"v_mul_f32 v161, s88, v161\n"
"v_mfma_f32_16x16x32_bf16 v[164:167], a[128:131], a[100:103], v[164:167]\n"
"ds_read_b64_tr_b16 a[174:175], v238 offset:4608\n"
"ds_read_b64_tr_b16 a[176:177], v238 offset:8192\n"
"v_mfma_f32_16x16x32_bf16 v[164:167], a[132:135], a[104:107], v[164:167]\n"
"ds_read_b64_tr_b16 a[178:179], v238 offset:8704\n"
"v_mul_f32 v162, s88, v162\n"
"v_mfma_f32_16x16x32_bf16 v[164:167], a[136:139], a[108:111], v[164:167]\n"
"ds_read_b64_tr_b16 a[180:181], v238 offset:12288\n"
"v_mul_f32 v163, s88, v163\n"
"v_mfma_f32_16x16x32_bf16 v[168:171], a[140:143], a[28:31], v[168:171]\n"
"ds_read_b64_tr_b16 a[182:183], v238 offset:12800\n"
"v_cvt_pk_bf16_f32 v64, v160, v161\n"
"v_mfma_f32_16x16x32_bf16 v[168:171], a[144:147], a[32:35], v[168:171]\n"
"s_waitcnt lgkmcnt(8)\n"
"s_add_u32 m0, s95, 158976\n"
"ds_read_b64_tr_b16 a[184:185], v238 offset:16384\n"
"v_cvt_pk_bf16_f32 v65, v162, v163\n"
"v_mfma_f32_16x16x32_bf16 v[168:171], a[148:151], a[36:39], v[168:171]\n"
"buffer_load_dwordx4 v228, s[40:43], s70 offen offset:768 lds\n"
"v_mul_f32 v164, s88, v164\n"
"v_mfma_f32_16x16x32_bf16 v[168:171], a[152:155], a[40:43], v[168:171]\n"
"ds_read_b64_tr_b16 a[186:187], v238 offset:16896\n"
"ds_read_b64_tr_b16 a[188:189], v238 offset:20480\n"
"v_mfma_f32_16x16x32_bf16 v[168:171], a[156:159], a[44:47], v[168:171]\n"
"ds_read_b64_tr_b16 a[190:191], v238 offset:20992\n"
"v_mul_f32 v165, s88, v165\n"
"v_mfma_f32_16x16x32_bf16 v[168:171], a[160:163], a[48:51], v[168:171]\n"
"ds_read_b64_tr_b16 a[192:193], v238 offset:24576\n"
"v_mul_f32 v166, s88, v166\n"
"v_mfma_f32_16x16x32_bf16 v[168:171], a[164:167], a[52:55], v[168:171]\n"
"ds_read_b64_tr_b16 a[194:195], v238 offset:25088\n"
"v_mul_f32 v167, s88, v167\n"
"v_mfma_f32_16x16x32_bf16 v[172:175], a[140:143], a[84:87], v[172:175]\n"
"ds_read_b64_tr_b16 a[196:197], v239 offset:0\n"
"v_cvt_pk_bf16_f32 v96, v164, v165\n"
"v_mfma_f32_16x16x32_bf16 v[172:175], a[144:147], a[88:91], v[172:175]\n"
"ds_read_b64_tr_b16 a[198:199], v239 offset:512\n"
"v_cvt_pk_bf16_f32 v97, v166, v167\n"
"v_mfma_f32_16x16x32_bf16 v[172:175], a[148:151], a[92:95], v[172:175]\n"
"ds_read_b64_tr_b16 a[200:201], v239 offset:4096\n"
"v_mul_f32 v168, s88, v168\n"
"v_mfma_f32_16x16x32_bf16 v[172:175], a[152:155], a[96:99], v[172:175]\n"
"ds_read_b64_tr_b16 a[202:203], v239 offset:4608\n"
"v_mul_f32 v169, s88, v169\n"
"v_mfma_f32_16x16x32_bf16 v[172:175], a[156:159], a[100:103], v[172:175]\n"
"ds_read_b64_tr_b16 a[204:205], v239 offset:8192\n"
"v_mul_f32 v170, s88, v170\n"
"v_mfma_f32_16x16x32_bf16 v[172:175], a[160:163], a[104:107], v[172:175]\n"
"ds_read_b64_tr_b16 a[206:207], v239 offset:8704\n"
"v_mul_f32 v171, s88, v171\n"
"v_mfma_f32_16x16x32_bf16 v[172:175], a[164:167], a[108:111], v[172:175]\n"
"s_waitcnt lgkmcnt(8)\n"
"ds_read_b64_tr_b16 a[208:209], v239 offset:12288\n"
"v_cvt_pk_bf16_f32 v66, v168, v169\n"
"v_mfma_f32_16x16x32_bf16 v[176:179], a[168:171], a[28:31], v[176:179]\n"
"ds_read_b64_tr_b16 a[210:211], v239 offset:12800\n"
"v_cvt_pk_bf16_f32 v67, v170, v171\n"
"v_mfma_f32_16x16x32_bf16 v[176:179], a[172:175], a[32:35], v[176:179]\n"
"ds_read_b64_tr_b16 a[212:213], v239 offset:16384\n"
"v_mfma_f32_16x16x32_bf16 v[176:179], a[176:179], a[36:39], v[176:179]\n"
"ds_read_b64_tr_b16 a[214:215], v239 offset:16896\n"
"v_permlane16_swap_b32 v64, v66\n"
"v_mfma_f32_16x16x32_bf16 v[176:179], a[180:183], a[40:43], v[176:179]\n"
"ds_read_b64_tr_b16 a[216:217], v239 offset:20480\n"
"v_permlane16_swap_b32 v65, v67\n"
"v_mfma_f32_16x16x32_bf16 v[176:179], a[184:187], a[44:47], v[176:179]\n"
"ds_read_b64_tr_b16 a[218:219], v239 offset:20992\n"
"v_mul_f32 v172, s88, v172\n"
"v_mfma_f32_16x16x32_bf16 v[176:179], a[188:191], a[48:51], v[176:179]\n"
"s_waitcnt vmcnt(18) lgkmcnt(8)\n"
"ds_read_b64_tr_b16 a[220:221], v239 offset:24576\n"
"v_mul_f32 v173, s88, v173\n"
"v_mfma_f32_16x16x32_bf16 v[176:179], a[192:195], a[52:55], v[176:179]\n"
"s_barrier\n"
"ds_read_b64_tr_b16 a[222:223], v239 offset:25088\n"
"v_mul_f32 v174, s88, v174\n"
"v_mfma_f32_16x16x32_bf16 v[180:183], a[168:171], a[84:87], v[180:183]\n"
"s_add_u32 m0, s95, 48384\n"
"ds_read_b64_tr_b16 a[112:113], v236 offset:28672\n"
"v_mul_f32 v175, s88, v175\n"
"v_mfma_f32_16x16x32_bf16 v[180:183], a[172:175], a[88:91], v[180:183]\n"
"buffer_load_dwordx4 v228, s[40:43], s71 offen offset:768 lds\n"
"v_cvt_pk_bf16_f32 v98, v172, v173\n"
"v_mfma_f32_16x16x32_bf16 v[180:183], a[176:179], a[92:95], v[180:183]\n"
"ds_read_b64_tr_b16 a[114:115], v236 offset:29184\n"
"ds_read_b64_tr_b16 a[116:117], v236 offset:32768\n"
"v_mfma_f32_16x16x32_bf16 v[180:183], a[180:183], a[96:99], v[180:183]\n"
"ds_read_b64_tr_b16 a[118:119], v236 offset:33280\n"
"v_cvt_pk_bf16_f32 v99, v174, v175\n"
"v_mfma_f32_16x16x32_bf16 v[180:183], a[184:187], a[100:103], v[180:183]\n"
"ds_read_b64_tr_b16 a[120:121], v236 offset:36864\n"
"v_mul_f32 v176, s88, v176\n"
"v_mfma_f32_16x16x32_bf16 v[180:183], a[188:191], a[104:107], v[180:183]\n"
"ds_read_b64_tr_b16 a[122:123], v236 offset:37376\n"
"v_permlane16_swap_b32 v96, v98\n"
"v_mfma_f32_16x16x32_bf16 v[180:183], a[192:195], a[108:111], v[180:183]\n"
"s_add_u32 m0, s95, 52480\n"
"ds_read_b64_tr_b16 a[124:125], v236 offset:40960\n"
"v_permlane16_swap_b32 v97, v99\n"
"v_mfma_f32_16x16x32_bf16 v[184:187], a[196:199], a[28:31], v[184:187]\n"
"buffer_load_dwordx4 v228, s[40:43], s72 offen offset:768 lds\n"
"v_mul_f32 v177, s88, v177\n"
"v_mfma_f32_16x16x32_bf16 v[184:187], a[200:203], a[32:35], v[184:187]\n"
"s_waitcnt lgkmcnt(7)\n"
"ds_read_b64_tr_b16 a[126:127], v236 offset:41472\n"
"ds_read_b64_tr_b16 a[128:129], v236 offset:45056\n"
"v_mfma_f32_16x16x32_bf16 v[184:187], a[204:207], a[36:39], v[184:187]\n"
"ds_read_b64_tr_b16 a[130:131], v236 offset:45568\n"
"v_mul_f32 v178, s88, v178\n"
"v_mfma_f32_16x16x32_bf16 v[184:187], a[208:211], a[40:43], v[184:187]\n"
"ds_read_b64_tr_b16 a[132:133], v236 offset:49152\n"
"v_mul_f32 v179, s88, v179\n"
"v_mfma_f32_16x16x32_bf16 v[184:187], a[212:215], a[44:47], v[184:187]\n"
"ds_read_b64_tr_b16 a[134:135], v236 offset:49664\n"
"v_cvt_pk_bf16_f32 v68, v176, v177\n"
"v_mfma_f32_16x16x32_bf16 v[184:187], a[216:219], a[48:51], v[184:187]\n"
"s_add_u32 m0, s95, 56576\n"
"ds_read_b64_tr_b16 a[136:137], v236 offset:53248\n"
"v_cvt_pk_bf16_f32 v69, v178, v179\n"
"v_mfma_f32_16x16x32_bf16 v[184:187], a[220:223], a[52:55], v[184:187]\n"
"buffer_load_dwordx4 v228, s[40:43], s73 offen offset:768 lds\n"
"v_mul_f32 v180, s88, v180\n"
"v_mfma_f32_16x16x32_bf16 v[188:191], a[196:199], a[84:87], v[188:191]\n"
"ds_read_b64_tr_b16 a[138:139], v236 offset:53760\n"
"ds_read_b64_tr_b16 a[140:141], v237 offset:28672\n"
"v_mfma_f32_16x16x32_bf16 v[188:191], a[200:203], a[88:91], v[188:191]\n"
"ds_read_b64_tr_b16 a[142:143], v237 offset:29184\n"
"v_mul_f32 v181, s88, v181\n"
"v_mfma_f32_16x16x32_bf16 v[188:191], a[204:207], a[92:95], v[188:191]\n"
"ds_read_b64_tr_b16 a[144:145], v237 offset:32768\n"
"v_mul_f32 v184, s88, v184\n"
"v_mfma_f32_16x16x32_bf16 v[188:191], a[208:211], a[96:99], v[188:191]\n"
"ds_read_b64_tr_b16 a[146:147], v237 offset:33280\n"
"v_mul_f32 v185, s88, v185\n"
"v_mfma_f32_16x16x32_bf16 v[188:191], a[212:215], a[100:103], v[188:191]\n"
"s_add_u32 m0, s95, 60672\n"
"ds_read_b64_tr_b16 a[148:149], v237 offset:36864\n"
"v_mul_f32 v186, s88, v186\n"
"v_mfma_f32_16x16x32_bf16 v[188:191], a[216:219], a[104:107], v[188:191]\n"
"buffer_load_dwordx4 v228, s[40:43], s74 offen offset:768 lds\n"
"v_mul_f32 v187, s88, v187\n"
"v_mfma_f32_16x16x32_bf16 v[188:191], a[220:223], a[108:111], v[188:191]\n"
"s_waitcnt lgkmcnt(7)\n"
"ds_read_b64_tr_b16 a[150:151], v237 offset:37376\n"
"ds_read_b64_tr_b16 a[152:153], v237 offset:40960\n"
"v_mfma_f32_16x16x32_bf16 v[192:195], a[112:115], a[0:3], 0\n"
"ds_read_b64_tr_b16 a[154:155], v237 offset:41472\n"
"v_cvt_pk_bf16_f32 v70, v184, v185\n"
"v_mfma_f32_16x16x32_bf16 v[192:195], a[116:119], a[4:7], v[192:195]\n"
"ds_read_b64_tr_b16 a[156:157], v237 offset:45056\n"
"v_cvt_pk_bf16_f32 v71, v186, v187\n"
"v_mfma_f32_16x16x32_bf16 v[192:195], a[120:123], a[8:11], v[192:195]\n"
"ds_read_b64_tr_b16 a[158:159], v237 offset:45568\n"
"v_mul_f32 v182, s88, v182\n"
"v_mfma_f32_16x16x32_bf16 v[192:195], a[124:127], a[12:15], v[192:195]\n"
"s_add_u32 m0, s95, 64768\n"
"ds_read_b64_tr_b16 a[160:161], v237 offset:49152\n"
"v_permlane16_swap_b32 v68, v70\n"
"v_mfma_f32_16x16x32_bf16 v[192:195], a[128:131], a[16:19], v[192:195]\n"
"buffer_load_dwordx4 v228, s[40:43], s75 offen offset:768 lds\n"
"v_permlane16_swap_b32 v69, v71\n"
"v_mfma_f32_16x16x32_bf16 v[192:195], a[132:135], a[20:23], v[192:195]\n"
"s_waitcnt lgkmcnt(7)\n"
"ds_read_b64_tr_b16 a[162:163], v237 offset:49664\n"
"ds_read_b64_tr_b16 a[164:165], v237 offset:53248\n"
"v_mfma_f32_16x16x32_bf16 v[192:195], a[136:139], a[24:27], v[192:195]\n"
"ds_read_b64_tr_b16 a[166:167], v237 offset:53760\n"
"v_mul_f32 v183, s88, v183\n"
"v_mfma_f32_16x16x32_bf16 v[196:199], a[112:115], a[56:59], 0\n"
"ds_read_b64_tr_b16 a[168:169], v238 offset:28672\n"
"v_cvt_pk_bf16_f32 v100, v180, v181\n"
"v_mfma_f32_16x16x32_bf16 v[196:199], a[116:119], a[60:63], v[196:199]\n"
"ds_read_b64_tr_b16 a[170:171], v238 offset:29184\n"
"v_cvt_pk_bf16_f32 v101, v182, v183\n"
"v_mfma_f32_16x16x32_bf16 v[196:199], a[120:123], a[64:67], v[196:199]\n"
"s_add_u32 m0, s95, 68864\n"
"ds_read_b64_tr_b16 a[172:173], v238 offset:32768\n"
"v_mul_f32 v188, s88, v188\n"
"v_mfma_f32_16x16x32_bf16 v[196:199], a[124:127], a[68:71], v[196:199]\n"
"buffer_load_dwordx4 v228, s[40:43], s76 offen offset:768 lds\n"
"v_mul_f32 v189, s88, v189\n"
"v_mfma_f32_16x16x32_bf16 v[196:199], a[128:131], a[72:75], v[196:199]\n"
"ds_read_b64_tr_b16 a[174:175], v238 offset:33280\n"
"ds_read_b64_tr_b16 a[176:177], v238 offset:36864\n"
"v_mfma_f32_16x16x32_bf16 v[196:199], a[132:135], a[76:79], v[196:199]\n"
"ds_read_b64_tr_b16 a[178:179], v238 offset:37376\n"
"v_mul_f32 v190, s88, v190\n"
"v_mfma_f32_16x16x32_bf16 v[196:199], a[136:139], a[80:83], v[196:199]\n"
"ds_read_b64_tr_b16 a[180:181], v238 offset:40960\n"
"v_mul_f32 v191, s88, v191\n"
"v_mfma_f32_16x16x32_bf16 v[200:203], a[140:143], a[0:3], 0\n"
"ds_read_b64_tr_b16 a[182:183], v238 offset:41472\n"
"v_cvt_pk_bf16_f32 v102, v188, v189\n"
"v_mfma_f32_16x16x32_bf16 v[200:203], a[144:147], a[4:7], v[200:203]\n"
"s_waitcnt lgkmcnt(8)\n"
"s_add_u32 m0, s95, 72960\n"
"ds_read_b64_tr_b16 a[184:185], v238 offset:45056\n"
"v_cvt_pk_bf16_f32 v103, v190, v191\n"
"v_mfma_f32_16x16x32_bf16 v[200:203], a[148:151], a[8:11], v[200:203]\n"
"buffer_load_dwordx4 v228, s[40:43], s77 offen offset:768 lds\n"
"v_mfma_f32_16x16x32_bf16 v[200:203], a[152:155], a[12:15], v[200:203]\n"
"ds_read_b64_tr_b16 a[186:187], v238 offset:45568\n"
"ds_read_b64_tr_b16 a[188:189], v238 offset:49152\n"
"v_mfma_f32_16x16x32_bf16 v[200:203], a[156:159], a[16:19], v[200:203]\n"
"ds_read_b64_tr_b16 a[190:191], v238 offset:49664\n"
"v_permlane16_swap_b32 v100, v102\n"
"v_mfma_f32_16x16x32_bf16 v[200:203], a[160:163], a[20:23], v[200:203]\n"
"ds_read_b64_tr_b16 a[192:193], v238 offset:53248\n"
"v_permlane16_swap_b32 v101, v103\n"
"v_mfma_f32_16x16x32_bf16 v[200:203], a[164:167], a[24:27], v[200:203]\n"
"ds_read_b64_tr_b16 a[194:195], v238 offset:53760\n"
"v_mfma_f32_16x16x32_bf16 v[204:207], a[140:143], a[56:59], 0\n"
"ds_read_b64_tr_b16 a[196:197], v239 offset:28672\n"
"v_mfma_f32_16x16x32_bf16 v[204:207], a[144:147], a[60:63], v[204:207]\n"
"ds_read_b64_tr_b16 a[198:199], v239 offset:29184\n"
"v_mfma_f32_16x16x32_bf16 v[204:207], a[148:151], a[64:67], v[204:207]\n"
"ds_read_b64_tr_b16 a[200:201], v239 offset:32768\n"
"v_mfma_f32_16x16x32_bf16 v[204:207], a[152:155], a[68:71], v[204:207]\n"
"ds_read_b64_tr_b16 a[202:203], v239 offset:33280\n"
"v_mfma_f32_16x16x32_bf16 v[204:207], a[156:159], a[72:75], v[204:207]\n"
"ds_read_b64_tr_b16 a[204:205], v239 offset:36864\n"
"v_mfma_f32_16x16x32_bf16 v[204:207], a[160:163], a[76:79], v[204:207]\n"
"ds_read_b64_tr_b16 a[206:207], v239 offset:37376\n"
"v_mfma_f32_16x16x32_bf16 v[204:207], a[164:167], a[80:83], v[204:207]\n"
"s_waitcnt lgkmcnt(8)\n"
"ds_read_b64_tr_b16 a[208:209], v239 offset:40960\n"
"v_mfma_f32_16x16x32_bf16 v[208:211], a[168:171], a[0:3], 0\n"
"ds_read_b64_tr_b16 a[210:211], v239 offset:41472\n"
"v_mfma_f32_16x16x32_bf16 v[208:211], a[172:175], a[4:7], v[208:211]\n"
"ds_read_b64_tr_b16 a[212:213], v239 offset:45056\n"
"v_mfma_f32_16x16x32_bf16 v[208:211], a[176:179], a[8:11], v[208:211]\n"
"ds_read_b64_tr_b16 a[214:215], v239 offset:45568\n"
"v_mfma_f32_16x16x32_bf16 v[208:211], a[180:183], a[12:15], v[208:211]\n"
"ds_read_b64_tr_b16 a[216:217], v239 offset:49152\n"
"v_mfma_f32_16x16x32_bf16 v[208:211], a[184:187], a[16:19], v[208:211]\n"
"ds_read_b64_tr_b16 a[218:219], v239 offset:49664\n"
"v_mfma_f32_16x16x32_bf16 v[208:211], a[188:191], a[20:23], v[208:211]\n"
"s_waitcnt vmcnt(14) lgkmcnt(8)\n"
"ds_read_b64_tr_b16 a[220:221], v239 offset:53248\n"
"v_mfma_f32_16x16x32_bf16 v[208:211], a[192:195], a[24:27], v[208:211]\n"
"s_barrier\n"
"ds_read_b64_tr_b16 a[222:223], v239 offset:53760\n"
"v_mfma_f32_16x16x32_bf16 v[212:215], a[168:171], a[56:59], 0\n"
"s_add_u32 m0, s95, 76928\n"
"ds_read_b64_tr_b16 a[112:113], v232 offset:0\n"
"v_mfma_f32_16x16x32_bf16 v[212:215], a[172:175], a[60:63], v[212:215]\n"
"buffer_load_dwordx4 v228, s[40:43], s64 offen offset:896 lds\n"
"v_mfma_f32_16x16x32_bf16 v[212:215], a[176:179], a[64:67], v[212:215]\n"
"ds_read_b64_tr_b16 a[114:115], v232 offset:512\n"
"ds_read_b64_tr_b16 a[116:117], v232 offset:4096\n"
"v_mfma_f32_16x16x32_bf16 v[212:215], a[180:183], a[68:71], v[212:215]\n"
"ds_read_b64_tr_b16 a[118:119], v232 offset:4608\n"
"v_mfma_f32_16x16x32_bf16 v[212:215], a[184:187], a[72:75], v[212:215]\n"
"ds_read_b64_tr_b16 a[120:121], v232 offset:8192\n"
"v_mfma_f32_16x16x32_bf16 v[212:215], a[188:191], a[76:79], v[212:215]\n"
"ds_read_b64_tr_b16 a[122:123], v232 offset:8704\n"
"v_mfma_f32_16x16x32_bf16 v[212:215], a[192:195], a[80:83], v[212:215]\n"
"s_add_u32 m0, s95, 81024\n"
"ds_read_b64_tr_b16 a[124:125], v232 offset:12288\n"
"v_mfma_f32_16x16x32_bf16 v[216:219], a[196:199], a[0:3], 0\n"
"s_memtime vcc\n"
"buffer_load_dwordx4 v228, s[40:43], s65 offen offset:896 lds\n"
"v_mfma_f32_16x16x32_bf16 v[216:219], a[200:203], a[4:7], v[216:219]\n"
"s_waitcnt lgkmcnt(7)\n"
"s_memrealtime s[92:93]\n"
"ds_read_b64_tr_b16 a[126:127], v232 offset:12800\n"
"ds_read_b64_tr_b16 a[128:129], v232 offset:16384\n"
"v_mfma_f32_16x16x32_bf16 v[216:219], a[204:207], a[8:11], v[216:219]\n"
"ds_read_b64_tr_b16 a[130:131], v232 offset:16896\n"
"v_mfma_f32_16x16x32_bf16 v[216:219], a[208:211], a[12:15], v[216:219]\n"
"ds_read_b64_tr_b16 a[132:133], v232 offset:20480\n"
"v_mfma_f32_16x16x32_bf16 v[216:219], a[212:215], a[16:19], v[216:219]\n"
"ds_read_b64_tr_b16 a[134:135], v232 offset:20992\n"
"v_mfma_f32_16x16x32_bf16 v[216:219], a[216:219], a[20:23], v[216:219]\n"
"s_add_u32 m0, s95, 85120\n"
"ds_read_b64_tr_b16 a[136:137], v232 offset:24576\n"
"v_mfma_f32_16x16x32_bf16 v[216:219], a[220:223], a[24:27], v[216:219]\n"
"buffer_load_dwordx4 v228, s[40:43], s66 offen offset:896 lds\n"
"v_mfma_f32_16x16x32_bf16 v[220:223], a[196:199], a[56:59], 0\n"
"ds_read_b64_tr_b16 a[138:139], v232 offset:25088\n"
"ds_read_b64_tr_b16 a[140:141], v233 offset:0\n"
"v_mfma_f32_16x16x32_bf16 v[220:223], a[200:203], a[60:63], v[220:223]\n"
"ds_read_b64_tr_b16 a[142:143], v233 offset:512\n"
"v_mfma_f32_16x16x32_bf16 v[220:223], a[204:207], a[64:67], v[220:223]\n"
"ds_read_b64_tr_b16 a[144:145], v233 offset:4096\n"
"v_mfma_f32_16x16x32_bf16 v[220:223], a[208:211], a[68:71], v[220:223]\n"
"ds_read_b64_tr_b16 a[146:147], v233 offset:4608\n"
"v_mfma_f32_16x16x32_bf16 v[220:223], a[212:215], a[72:75], v[220:223]\n"
"s_add_u32 m0, s95, 89216\n"
"ds_read_b64_tr_b16 a[148:149], v233 offset:8192\n"
"v_mfma_f32_16x16x32_bf16 v[220:223], a[216:219], a[76:79], v[220:223]\n"
"buffer_load_dwordx4 v228, s[40:43], s67 offen offset:896 lds\n"
"v_mfma_f32_16x16x32_bf16 v[220:223], a[220:223], a[80:83], v[220:223]\n"
"s_waitcnt lgkmcnt(7)\n"
"ds_read_b64_tr_b16 a[150:151], v233 offset:8704\n"
"ds_read_b64_tr_b16 a[152:153], v233 offset:12288\n"
"v_mfma_f32_16x16x32_bf16 v[192:195], a[112:115], a[28:31], v[192:195]\n"
"ds_read_b64_tr_b16 a[154:155], v233 offset:12800\n"
"v_mfma_f32_16x16x32_bf16 v[192:195], a[116:119], a[32:35], v[192:195]\n"
"ds_read_b64_tr_b16 a[156:157], v233 offset:16384\n"
"v_mfma_f32_16x16x32_bf16 v[192:195], a[120:123], a[36:39], v[192:195]\n"
"ds_read_b64_tr_b16 a[158:159], v233 offset:16896\n"
"v_mfma_f32_16x16x32_bf16 v[192:195], a[124:127], a[40:43], v[192:195]\n"
"s_add_u32 m0, s95, 93312\n"
"ds_read_b64_tr_b16 a[160:161], v233 offset:20480\n"
"v_mfma_f32_16x16x32_bf16 v[192:195], a[128:131], a[44:47], v[192:195]\n"
"buffer_load_dwordx4 v228, s[40:43], s68 offen offset:896 lds\n"
"v_mfma_f32_16x16x32_bf16 v[192:195], a[132:135], a[48:51], v[192:195]\n"
"s_waitcnt lgkmcnt(7)\n"
"ds_read_b64_tr_b16 a[162:163], v233 offset:20992\n"
"ds_read_b64_tr_b16 a[164:165], v233 offset:24576\n"
"v_mfma_f32_16x16x32_bf16 v[192:195], a[136:139], a[52:55], v[192:195]\n"
"ds_read_b64_tr_b16 a[166:167], v233 offset:25088\n"
"v_mfma_f32_16x16x32_bf16 v[196:199], a[112:115], a[84:87], v[196:199]\n"
"ds_read_b64_tr_b16 a[168:169], v234 offset:0\n"
"v_mfma_f32_16x16x32_bf16 v[196:199], a[116:119], a[88:91], v[196:199]\n"
"ds_read_b64_tr_b16 a[170:171], v234 offset:512\n"
"v_mfma_f32_16x16x32_bf16 v[196:199], a[120:123], a[92:95], v[196:199]\n"
"s_add_u32 m0, s95, 97408\n"
"ds_read_b64_tr_b16 a[172:173], v234 offset:4096\n"
"v_mul_f32 v192, s88, v192\n"
"v_mfma_f32_16x16x32_bf16 v[196:199], a[124:127], a[96:99], v[196:199]\n"
"buffer_load_dwordx4 v228, s[40:43], s69 offen offset:896 lds\n"
"v_mul_f32 v193, s88, v193\n"
"v_mfma_f32_16x16x32_bf16 v[196:199], a[128:131], a[100:103], v[196:199]\n"
"ds_read_b64_tr_b16 a[174:175], v234 offset:4608\n"
"ds_read_b64_tr_b16 a[176:177], v234 offset:8192\n"
"v_mfma_f32_16x16x32_bf16 v[196:199], a[132:135], a[104:107], v[196:199]\n"
"ds_read_b64_tr_b16 a[178:179], v234 offset:8704\n"
"v_mul_f32 v194, s88, v194\n"
"v_mfma_f32_16x16x32_bf16 v[196:199], a[136:139], a[108:111], v[196:199]\n"
"ds_read_b64_tr_b16 a[180:181], v234 offset:12288\n"
"v_mul_f32 v195, s88, v195\n"
"v_mfma_f32_16x16x32_bf16 v[200:203], a[140:143], a[28:31], v[200:203]\n"
"ds_read_b64_tr_b16 a[182:183], v234 offset:12800\n"
"v_cvt_pk_bf16_f32 v72, v192, v193\n"
"v_mfma_f32_16x16x32_bf16 v[200:203], a[144:147], a[32:35], v[200:203]\n"
"s_waitcnt lgkmcnt(8)\n"
"s_add_u32 m0, s95, 101504\n"
"ds_read_b64_tr_b16 a[184:185], v234 offset:16384\n"
"v_cvt_pk_bf16_f32 v73, v194, v195\n"
"v_mfma_f32_16x16x32_bf16 v[200:203], a[148:151], a[36:39], v[200:203]\n"
"buffer_load_dwordx4 v228, s[40:43], s70 offen offset:896 lds\n"
"v_mul_f32 v196, s88, v196\n"
"v_mfma_f32_16x16x32_bf16 v[200:203], a[152:155], a[40:43], v[200:203]\n"
"ds_read_b64_tr_b16 a[186:187], v234 offset:16896\n"
"ds_read_b64_tr_b16 a[188:189], v234 offset:20480\n"
"v_mfma_f32_16x16x32_bf16 v[200:203], a[156:159], a[44:47], v[200:203]\n"
"ds_read_b64_tr_b16 a[190:191], v234 offset:20992\n"
"v_mul_f32 v197, s88, v197\n"
"v_mfma_f32_16x16x32_bf16 v[200:203], a[160:163], a[48:51], v[200:203]\n"
"ds_read_b64_tr_b16 a[192:193], v234 offset:24576\n"
"v_mul_f32 v198, s88, v198\n"
"v_mfma_f32_16x16x32_bf16 v[200:203], a[164:167], a[52:55], v[200:203]\n"
"ds_read_b64_tr_b16 a[194:195], v234 offset:25088\n"
"v_mul_f32 v199, s88, v199\n"
"v_mfma_f32_16x16x32_bf16 v[204:207], a[140:143], a[84:87], v[204:207]\n"
"ds_read_b64_tr_b16 a[196:197], v235 offset:0\n"
"v_cvt_pk_bf16_f32 v104, v196, v197\n"
"v_mfma_f32_16x16x32_bf16 v[204:207], a[144:147], a[88:91], v[204:207]\n"
"ds_read_b64_tr_b16 a[198:199], v235 offset:512\n"
"v_cvt_pk_bf16_f32 v105, v198, v199\n"
"v_mfma_f32_16x16x32_bf16 v[204:207], a[148:151], a[92:95], v[204:207]\n"
"ds_read_b64_tr_b16 a[200:201], v235 offset:4096\n"
"v_mul_f32 v200, s88, v200\n"
"v_mfma_f32_16x16x32_bf16 v[204:207], a[152:155], a[96:99], v[204:207]\n"
"ds_read_b64_tr_b16 a[202:203], v235 offset:4608\n"
"v_mul_f32 v201, s88, v201\n"
"v_mfma_f32_16x16x32_bf16 v[204:207], a[156:159], a[100:103], v[204:207]\n"
"ds_read_b64_tr_b16 a[204:205], v235 offset:8192\n"
"v_mul_f32 v202, s88, v202\n"
"v_mfma_f32_16x16x32_bf16 v[204:207], a[160:163], a[104:107], v[204:207]\n"
"ds_read_b64_tr_b16 a[206:207], v235 offset:8704\n"
"v_mul_f32 v203, s88, v203\n"
"v_mfma_f32_16x16x32_bf16 v[204:207], a[164:167], a[108:111], v[204:207]\n"
"s_waitcnt lgkmcnt(8)\n"
"ds_read_b64_tr_b16 a[208:209], v235 offset:12288\n"
"v_cvt_pk_bf16_f32 v74, v200, v201\n"
"v_mfma_f32_16x16x32_bf16 v[208:211], a[168:171], a[28:31], v[208:211]\n"
"ds_read_b64_tr_b16 a[210:211], v235 offset:12800\n"
"v_cvt_pk_bf16_f32 v75, v202, v203\n"
"v_mfma_f32_16x16x32_bf16 v[208:211], a[172:175], a[32:35], v[208:211]\n"
"ds_read_b64_tr_b16 a[212:213], v235 offset:16384\n"
"v_mfma_f32_16x16x32_bf16 v[208:211], a[176:179], a[36:39], v[208:211]\n"
"ds_read_b64_tr_b16 a[214:215], v235 offset:16896\n"
"v_permlane16_swap_b32 v72, v74\n"
"v_mfma_f32_16x16x32_bf16 v[208:211], a[180:183], a[40:43], v[208:211]\n"
"ds_read_b64_tr_b16 a[216:217], v235 offset:20480\n"
"v_permlane16_swap_b32 v73, v75\n"
"v_mfma_f32_16x16x32_bf16 v[208:211], a[184:187], a[44:47], v[208:211]\n"
"ds_read_b64_tr_b16 a[218:219], v235 offset:20992\n"
"v_mul_f32 v204, s88, v204\n"
"v_mfma_f32_16x16x32_bf16 v[208:211], a[188:191], a[48:51], v[208:211]\n"
"s_waitcnt vmcnt(14) lgkmcnt(8)\n"
"ds_read_b64_tr_b16 a[220:221], v235 offset:24576\n"
"v_mul_f32 v205, s88, v205\n"
"v_mfma_f32_16x16x32_bf16 v[208:211], a[192:195], a[52:55], v[208:211]\n"
"s_barrier\n"
"ds_read_b64_tr_b16 a[222:223], v235 offset:25088\n"
"v_mul_f32 v206, s88, v206\n"
"v_mfma_f32_16x16x32_bf16 v[212:215], a[168:171], a[84:87], v[212:215]\n"
"s_add_u32 m0, s95, 105600\n"
"ds_read_b64_tr_b16 a[112:113], v240 offset:0\n"
"v_mul_f32 v207, s88, v207\n"
"v_mfma_f32_16x16x32_bf16 v[212:215], a[172:175], a[88:91], v[212:215]\n"
"buffer_load_dwordx4 v228, s[40:43], s71 offen offset:896 lds\n"
"v_cvt_pk_bf16_f32 v106, v204, v205\n"
"v_mfma_f32_16x16x32_bf16 v[212:215], a[176:179], a[92:95], v[212:215]\n"
"ds_read_b64_tr_b16 a[114:115], v240 offset:512\n"
"ds_read_b64_tr_b16 a[116:117], v240 offset:4096\n"
"v_mfma_f32_16x16x32_bf16 v[212:215], a[180:183], a[96:99], v[212:215]\n"
"ds_read_b64_tr_b16 a[118:119], v240 offset:4608\n"
"v_cvt_pk_bf16_f32 v107, v206, v207\n"
"v_mfma_f32_16x16x32_bf16 v[212:215], a[184:187], a[100:103], v[212:215]\n"
"ds_read_b64_tr_b16 a[120:121], v240 offset:8192\n"
"v_mul_f32 v208, s88, v208\n"
"v_mfma_f32_16x16x32_bf16 v[212:215], a[188:191], a[104:107], v[212:215]\n"
"ds_read_b64_tr_b16 a[122:123], v240 offset:8704\n"
"v_permlane16_swap_b32 v104, v106\n"
"v_mfma_f32_16x16x32_bf16 v[212:215], a[192:195], a[108:111], v[212:215]\n"
"s_add_u32 m0, s95, 109696\n"
"ds_read_b64_tr_b16 a[124:125], v240 offset:12288\n"
"v_permlane16_swap_b32 v105, v107\n"
"v_mfma_f32_16x16x32_bf16 v[216:219], a[196:199], a[28:31], v[216:219]\n"
"buffer_load_dwordx4 v228, s[40:43], s72 offen offset:896 lds\n"
"v_mul_f32 v209, s88, v209\n"
"v_mfma_f32_16x16x32_bf16 v[216:219], a[200:203], a[32:35], v[216:219]\n"
"s_waitcnt lgkmcnt(7)\n"
"ds_read_b64_tr_b16 a[126:127], v240 offset:12800\n"
"ds_read_b64_tr_b16 a[128:129], v240 offset:16384\n"
"v_mfma_f32_16x16x32_bf16 v[216:219], a[204:207], a[36:39], v[216:219]\n"
"ds_read_b64_tr_b16 a[130:131], v240 offset:16896\n"
"v_mul_f32 v210, s88, v210\n"
"v_mfma_f32_16x16x32_bf16 v[216:219], a[208:211], a[40:43], v[216:219]\n"
"ds_read_b64_tr_b16 a[132:133], v240 offset:20480\n"
"v_mul_f32 v211, s88, v211\n"
"v_mfma_f32_16x16x32_bf16 v[216:219], a[212:215], a[44:47], v[216:219]\n"
"ds_read_b64_tr_b16 a[134:135], v240 offset:20992\n"
"v_cvt_pk_bf16_f32 v76, v208, v209\n"
"v_mfma_f32_16x16x32_bf16 v[216:219], a[216:219], a[48:51], v[216:219]\n"
"s_add_u32 m0, s95, 113792\n"
"ds_read_b64_tr_b16 a[136:137], v240 offset:24576\n"
"v_cvt_pk_bf16_f32 v77, v210, v211\n"
"v_mfma_f32_16x16x32_bf16 v[216:219], a[220:223], a[52:55], v[216:219]\n"
"buffer_load_dwordx4 v228, s[40:43], s73 offen offset:896 lds\n"
"v_mul_f32 v212, s88, v212\n"
"v_mfma_f32_16x16x32_bf16 v[220:223], a[196:199], a[84:87], v[220:223]\n"
"ds_read_b64_tr_b16 a[138:139], v240 offset:25088\n"
"ds_read_b64_tr_b16 a[140:141], v241 offset:0\n"
"v_mfma_f32_16x16x32_bf16 v[220:223], a[200:203], a[88:91], v[220:223]\n"
"ds_read_b64_tr_b16 a[142:143], v241 offset:512\n"
"v_mul_f32 v213, s88, v213\n"
"v_mfma_f32_16x16x32_bf16 v[220:223], a[204:207], a[92:95], v[220:223]\n"
"ds_read_b64_tr_b16 a[144:145], v241 offset:4096\n"
"v_mul_f32 v216, s88, v216\n"
"v_mfma_f32_16x16x32_bf16 v[220:223], a[208:211], a[96:99], v[220:223]\n"
"ds_read_b64_tr_b16 a[146:147], v241 offset:4608\n"
"v_mul_f32 v217, s88, v217\n"
"v_mfma_f32_16x16x32_bf16 v[220:223], a[212:215], a[100:103], v[220:223]\n"
"s_add_u32 m0, s95, 117888\n"
"ds_read_b64_tr_b16 a[148:149], v241 offset:8192\n"
"v_mul_f32 v218, s88, v218\n"
"v_mfma_f32_16x16x32_bf16 v[220:223], a[216:219], a[104:107], v[220:223]\n"
"buffer_load_dwordx4 v228, s[40:43], s74 offen offset:896 lds\n"
"v_mul_f32 v219, s88, v219\n"
"v_mfma_f32_16x16x32_bf16 v[220:223], a[220:223], a[108:111], v[220:223]\n"
"s_waitcnt lgkmcnt(7)\n"
"ds_read_b64_tr_b16 a[150:151], v241 offset:8704\n"
"ds_read_b64_tr_b16 a[152:153], v241 offset:12288\n"
"v_mfma_f32_16x16x32_bf16 v[128:131], a[112:115], a[0:3], 0\n"
"ds_read_b64_tr_b16 a[154:155], v241 offset:12800\n"
"v_cvt_pk_bf16_f32 v78, v216, v217\n"
"v_mfma_f32_16x16x32_bf16 v[128:131], a[116:119], a[4:7], v[128:131]\n"
"ds_read_b64_tr_b16 a[156:157], v241 offset:16384\n"
"v_cvt_pk_bf16_f32 v79, v218, v219\n"
"v_mfma_f32_16x16x32_bf16 v[128:131], a[120:123], a[8:11], v[128:131]\n"
"ds_read_b64_tr_b16 a[158:159], v241 offset:16896\n"
"v_mul_f32 v214, s88, v214\n"
"v_mfma_f32_16x16x32_bf16 v[128:131], a[124:127], a[12:15], v[128:131]\n"
"s_add_u32 m0, s95, 121984\n"
"ds_read_b64_tr_b16 a[160:161], v241 offset:20480\n"
"v_permlane16_swap_b32 v76, v78\n"
"v_mfma_f32_16x16x32_bf16 v[128:131], a[128:131], a[16:19], v[128:131]\n"
"buffer_load_dwordx4 v228, s[40:43], s75 offen offset:896 lds\n"
"v_permlane16_swap_b32 v77, v79\n"
"v_mfma_f32_16x16x32_bf16 v[128:131], a[132:135], a[20:23], v[128:131]\n"
"s_waitcnt lgkmcnt(7)\n"
"ds_read_b64_tr_b16 a[162:163], v241 offset:20992\n"
"ds_read_b64_tr_b16 a[164:165], v241 offset:24576\n"
"v_mfma_f32_16x16x32_bf16 v[128:131], a[136:139], a[24:27], v[128:131]\n"
"ds_read_b64_tr_b16 a[166:167], v241 offset:25088\n"
"v_mul_f32 v215, s88, v215\n"
"v_mfma_f32_16x16x32_bf16 v[132:135], a[112:115], a[56:59], 0\n"
"ds_read_b64_tr_b16 a[168:169], v242 offset:0\n"
"v_cvt_pk_bf16_f32 v108, v212, v213\n"
"v_mfma_f32_16x16x32_bf16 v[132:135], a[116:119], a[60:63], v[132:135]\n"
"ds_read_b64_tr_b16 a[170:171], v242 offset:512\n"
"v_cvt_pk_bf16_f32 v109, v214, v215\n"
"v_mfma_f32_16x16x32_bf16 v[132:135], a[120:123], a[64:67], v[132:135]\n"
"s_add_u32 m0, s95, 126080\n"
"ds_read_b64_tr_b16 a[172:173], v242 offset:4096\n"
"v_mul_f32 v220, s88, v220\n"
"v_mfma_f32_16x16x32_bf16 v[132:135], a[124:127], a[68:71], v[132:135]\n"
"buffer_load_dwordx4 v228, s[40:43], s76 offen offset:896 lds\n"
"v_mul_f32 v221, s88, v221\n"
"v_mfma_f32_16x16x32_bf16 v[132:135], a[128:131], a[72:75], v[132:135]\n"
"ds_read_b64_tr_b16 a[174:175], v242 offset:4608\n"
"ds_read_b64_tr_b16 a[176:177], v242 offset:8192\n"
"v_mfma_f32_16x16x32_bf16 v[132:135], a[132:135], a[76:79], v[132:135]\n"
"ds_read_b64_tr_b16 a[178:179], v242 offset:8704\n"
"v_mul_f32 v222, s88, v222\n"
"v_mfma_f32_16x16x32_bf16 v[132:135], a[136:139], a[80:83], v[132:135]\n"
"ds_read_b64_tr_b16 a[180:181], v242 offset:12288\n"
"v_mul_f32 v223, s88, v223\n"
"v_mfma_f32_16x16x32_bf16 v[136:139], a[140:143], a[0:3], 0\n"
"ds_read_b64_tr_b16 a[182:183], v242 offset:12800\n"
"v_cvt_pk_bf16_f32 v110, v220, v221\n"
"v_mfma_f32_16x16x32_bf16 v[136:139], a[144:147], a[4:7], v[136:139]\n"
"s_waitcnt lgkmcnt(8)\n"
"s_add_u32 m0, s95, 130176\n"
"ds_read_b64_tr_b16 a[184:185], v242 offset:16384\n"
"v_cvt_pk_bf16_f32 v111, v222, v223\n"
"v_mfma_f32_16x16x32_bf16 v[136:139], a[148:151], a[8:11], v[136:139]\n"
"buffer_load_dwordx4 v228, s[40:43], s77 offen offset:896 lds\n"
"v_mfma_f32_16x16x32_bf16 v[136:139], a[152:155], a[12:15], v[136:139]\n"
"ds_read_b64_tr_b16 a[186:187], v242 offset:16896\n"
"ds_read_b64_tr_b16 a[188:189], v242 offset:20480\n"
"v_mfma_f32_16x16x32_bf16 v[136:139], a[156:159], a[16:19], v[136:139]\n"
"ds_read_b64_tr_b16 a[190:191], v242 offset:20992\n"
"v_permlane16_swap_b32 v108, v110\n"
"v_mfma_f32_16x16x32_bf16 v[136:139], a[160:163], a[20:23], v[136:139]\n"
"ds_read_b64_tr_b16 a[192:193], v242 offset:24576\n"
"v_permlane16_swap_b32 v109, v111\n"
"v_mfma_f32_16x16x32_bf16 v[136:139], a[164:167], a[24:27], v[136:139]\n"
"ds_read_b64_tr_b16 a[194:195], v242 offset:25088\n"
"v_mfma_f32_16x16x32_bf16 v[140:143], a[140:143], a[56:59], 0\n"
"ds_read_b64_tr_b16 a[196:197], v243 offset:0\n"
"global_load_dword v249, v245, s[78:79]\n"
"v_mfma_f32_16x16x32_bf16 v[140:143], a[144:147], a[60:63], v[140:143]\n"
"ds_read_b64_tr_b16 a[198:199], v243 offset:512\n"
"buffer_load_dword v248, v246, s[56:59], 0 offen sc1\n"
"v_mfma_f32_16x16x32_bf16 v[140:143], a[148:151], a[64:67], v[140:143]\n"
"ds_read_b64_tr_b16 a[200:201], v243 offset:4096\n"
"buffer_load_dwordx2 v[250:251], off, s[56:59], s96 sc1\n"
"v_mfma_f32_16x16x32_bf16 v[140:143], a[152:155], a[68:71], v[140:143]\n"
"ds_read_b64_tr_b16 a[202:203], v243 offset:4608\n"
"v_mfma_f32_16x16x32_bf16 v[140:143], a[156:159], a[72:75], v[140:143]\n"
"ds_read_b64_tr_b16 a[204:205], v243 offset:8192\n"
"v_mfma_f32_16x16x32_bf16 v[140:143], a[160:163], a[76:79], v[140:143]\n"
"ds_read_b64_tr_b16 a[206:207], v243 offset:8704\n"
"v_mfma_f32_16x16x32_bf16 v[140:143], a[164:167], a[80:83], v[140:143]\n"
"s_waitcnt lgkmcnt(8)\n"
"ds_read_b64_tr_b16 a[208:209], v243 offset:12288\n"
"v_mfma_f32_16x16x32_bf16 v[144:147], a[168:171], a[0:3], 0\n"
"ds_read_b64_tr_b16 a[210:211], v243 offset:12800\n"
"v_mfma_f32_16x16x32_bf16 v[144:147], a[172:175], a[4:7], v[144:147]\n"
"ds_read_b64_tr_b16 a[212:213], v243 offset:16384\n"
"v_mfma_f32_16x16x32_bf16 v[144:147], a[176:179], a[8:11], v[144:147]\n"
"ds_read_b64_tr_b16 a[214:215], v243 offset:16896\n"
"v_mfma_f32_16x16x32_bf16 v[144:147], a[180:183], a[12:15], v[144:147]\n"
"ds_read_b64_tr_b16 a[216:217], v243 offset:20480\n"
"v_mfma_f32_16x16x32_bf16 v[144:147], a[184:187], a[16:19], v[144:147]\n"
"ds_read_b64_tr_b16 a[218:219], v243 offset:20992\n"
"v_mfma_f32_16x16x32_bf16 v[144:147], a[188:191], a[20:23], v[144:147]\n"
"s_waitcnt vmcnt(17) lgkmcnt(8)\n"
"ds_read_b64_tr_b16 a[220:221], v243 offset:24576\n"
"v_mfma_f32_16x16x32_bf16 v[144:147], a[192:195], a[24:27], v[144:147]\n"
"s_barrier\n"
"ds_read_b64_tr_b16 a[222:223], v243 offset:25088\n"
"v_mfma_f32_16x16x32_bf16 v[148:151], a[168:171], a[56:59], 0\n"
"s_add_u32 m0, s82, 16896\n"
"ds_read_b64_tr_b16 a[112:113], v232 offset:28672\n"
"v_mfma_f32_16x16x32_bf16 v[148:151], a[172:175], a[60:63], v[148:151]\n"
"buffer_load_dwordx4 v255, s[44:47], s84 offen offset:0 lds\n"
"v_mfma_f32_16x16x32_bf16 v[148:151], a[176:179], a[64:67], v[148:151]\n"
"ds_read_b64_tr_b16 a[114:115], v232 offset:29184\n"
"ds_read_b64_tr_b16 a[116:117], v232 offset:32768\n"
"v_mfma_f32_16x16x32_bf16 v[148:151], a[180:183], a[68:71], v[148:151]\n"
"ds_read_b64_tr_b16 a[118:119], v232 offset:33280\n"
"v_mfma_f32_16x16x32_bf16 v[148:151], a[184:187], a[72:75], v[148:151]\n"
"s_add_u32 m0, s82, 17440\n"
"ds_read_b64_tr_b16 a[120:121], v232 offset:36864\n"
"v_mfma_f32_16x16x32_bf16 v[148:151], a[188:191], a[76:79], v[148:151]\n"
"buffer_load_dwordx4 v255, s[44:47], s84 offen offset:512 lds\n"
"v_mfma_f32_16x16x32_bf16 v[148:151], a[192:195], a[80:83], v[148:151]\n"
"ds_read_b64_tr_b16 a[122:123], v232 offset:37376\n"
"ds_read_b64_tr_b16 a[124:125], v232 offset:40960\n"
"v_mfma_f32_16x16x32_bf16 v[152:155], a[196:199], a[0:3], 0\n"
"ds_read_b64_tr_b16 a[126:127], v232 offset:41472\n"
"v_mfma_f32_16x16x32_bf16 v[152:155], a[200:203], a[4:7], v[152:155]\n"
"s_waitcnt lgkmcnt(8)\n"
"s_add_u32 m0, s82, 17984\n"
"ds_read_b64_tr_b16 a[128:129], v232 offset:45056\n"
"v_mfma_f32_16x16x32_bf16 v[152:155], a[204:207], a[8:11], v[152:155]\n"
"buffer_load_dwordx4 v255, s[44:47], s84 offen offset:1024 nt lds\n"
"v_mfma_f32_16x16x32_bf16 v[152:155], a[208:211], a[12:15], v[152:155]\n"
"ds_read_b64_tr_b16 a[130:131], v232 offset:45568\n"
"ds_read_b64_tr_b16 a[132:133], v232 offset:49152\n"
"v_mfma_f32_16x16x32_bf16 v[152:155], a[212:215], a[16:19], v[152:155]\n"
"ds_read_b64_tr_b16 a[134:135], v232 offset:49664\n"
"v_mfma_f32_16x16x32_bf16 v[152:155], a[216:219], a[20:23], v[152:155]\n"
"s_add_u32 m0, s82, 18528\n"
"ds_read_b64_tr_b16 a[136:137], v232 offset:53248\n"
"v_mfma_f32_16x16x32_bf16 v[152:155], a[220:223], a[24:27], v[152:155]\n"
"buffer_load_dwordx4 v255, s[44:47], s84 offen offset:1536 nt lds\n"
"v_mfma_f32_16x16x32_bf16 v[156:159], a[196:199], a[56:59], 0\n"
"ds_read_b64_tr_b16 a[138:139], v232 offset:53760\n"
"ds_read_b64_tr_b16 a[140:141], v233 offset:28672\n"
"v_mfma_f32_16x16x32_bf16 v[156:159], a[200:203], a[60:63], v[156:159]\n"
"ds_read_b64_tr_b16 a[142:143], v233 offset:29184\n"
"v_mfma_f32_16x16x32_bf16 v[156:159], a[204:207], a[64:67], v[156:159]\n"
"s_add_u32 s84, s84, 0x4000\n"
"ds_read_b64_tr_b16 a[144:145], v233 offset:32768\n"
"v_mfma_f32_16x16x32_bf16 v[156:159], a[208:211], a[68:71], v[156:159]\n"
"s_cmp_lt_u32 s84, 0x80000\n"
"ds_read_b64_tr_b16 a[146:147], v233 offset:33280\n"
"v_mfma_f32_16x16x32_bf16 v[156:159], a[212:215], a[72:75], v[156:159]\n"
"s_cselect_b32 s46, 0x80000, 0\n"
"ds_read_b64_tr_b16 a[148:149], v233 offset:36864\n"
"v_mfma_f32_16x16x32_bf16 v[156:159], a[216:219], a[76:79], v[156:159]\n"
"ds_read_b64_tr_b16 a[150:151], v233 offset:37376\n"
"v_mfma_f32_16x16x32_bf16 v[156:159], a[220:223], a[80:83], v[156:159]\n"
"s_waitcnt lgkmcnt(8)\n"
"ds_read_b64_tr_b16 a[152:153], v233 offset:40960\n"
"v_mfma_f32_16x16x32_bf16 v[128:131], a[112:115], a[28:31], v[128:131]\n"
"ds_read_b64_tr_b16 a[154:155], v233 offset:41472\n"
"v_mfma_f32_16x16x32_bf16 v[128:131], a[116:119], a[32:35], v[128:131]\n"
"ds_read_b64_tr_b16 a[156:157], v233 offset:45056\n"
"v_mfma_f32_16x16x32_bf16 v[128:131], a[120:123], a[36:39], v[128:131]\n"
"ds_read_b64_tr_b16 a[158:159], v233 offset:45568\n"
"v_mfma_f32_16x16x32_bf16 v[128:131], a[124:127], a[40:43], v[128:131]\n"
"ds_read_b64_tr_b16 a[160:161], v233 offset:49152\n"
"v_mfma_f32_16x16x32_bf16 v[128:131], a[128:131], a[44:47], v[128:131]\n"
"ds_read_b64_tr_b16 a[162:163], v233 offset:49664\n"
"v_mfma_f32_16x16x32_bf16 v[128:131], a[132:135], a[48:51], v[128:131]\n"
"s_waitcnt lgkmcnt(8)\n"
"ds_read_b64_tr_b16 a[164:165], v233 offset:53248\n"
"v_mfma_f32_16x16x32_bf16 v[128:131], a[136:139], a[52:55], v[128:131]\n"
"ds_read_b64_tr_b16 a[166:167], v233 offset:53760\n"
"v_mfma_f32_16x16x32_bf16 v[132:135], a[112:115], a[84:87], v[132:135]\n"
"ds_read_b64_tr_b16 a[168:169], v234 offset:28672\n"
"v_mfma_f32_16x16x32_bf16 v[132:135], a[116:119], a[88:91], v[132:135]\n"
"ds_read_b64_tr_b16 a[170:171], v234 offset:29184\n"
"v_mfma_f32_16x16x32_bf16 v[132:135], a[120:123], a[92:95], v[132:135]\n"
"ds_read_b64_tr_b16 a[172:173], v234 offset:32768\n"
"v_mul_f32 v128, s88, v128\n"
"v_mfma_f32_16x16x32_bf16 v[132:135], a[124:127], a[96:99], v[132:135]\n"
"ds_read_b64_tr_b16 a[174:175], v234 offset:33280\n"
"v_mul_f32 v129, s88, v129\n"
"v_mfma_f32_16x16x32_bf16 v[132:135], a[128:131], a[100:103], v[132:135]\n"
"ds_read_b64_tr_b16 a[176:177], v234 offset:36864\n"
"v_mul_f32 v130, s88, v130\n"
"v_mfma_f32_16x16x32_bf16 v[132:135], a[132:135], a[104:107], v[132:135]\n"
"ds_read_b64_tr_b16 a[178:179], v234 offset:37376\n"
"v_mul_f32 v131, s88, v131\n"
"v_mfma_f32_16x16x32_bf16 v[132:135], a[136:139], a[108:111], v[132:135]\n"
"ds_read_b64_tr_b16 a[180:181], v234 offset:40960\n"
"v_cvt_pk_bf16_f32 v80, v128, v129\n"
"v_mfma_f32_16x16x32_bf16 v[136:139], a[140:143], a[28:31], v[136:139]\n"
"ds_read_b64_tr_b16 a[182:183], v234 offset:41472\n"
"v_cvt_pk_bf16_f32 v81, v130, v131\n"
"v_mfma_f32_16x16x32_bf16 v[136:139], a[144:147], a[32:35], v[136:139]\n"
"s_waitcnt lgkmcnt(8)\n"
"ds_read_b64_tr_b16 a[184:185], v234 offset:45056\n"
"v_mfma_f32_16x16x32_bf16 v[136:139], a[148:151], a[36:39], v[136:139]\n"
"ds_read_b64_tr_b16 a[186:187], v234 offset:45568\n"
"v_mul_f32 v132, s88, v132\n"
"v_mfma_f32_16x16x32_bf16 v[136:139], a[152:155], a[40:43], v[136:139]\n"
"ds_read_b64_tr_b16 a[188:189], v234 offset:49152\n"
"v_mul_f32 v133, s88, v133\n"
"v_mfma_f32_16x16x32_bf16 v[136:139], a[156:159], a[44:47], v[136:139]\n"
"ds_read_b64_tr_b16 a[190:191], v234 offset:49664\n"
"v_mul_f32 v134, s88, v134\n"
"v_mfma_f32_16x16x32_bf16 v[136:139], a[160:163], a[48:51], v[136:139]\n"
"ds_read_b64_tr_b16 a[192:193], v234 offset:53248\n"
"v_mul_f32 v135, s88, v135\n"
"v_mfma_f32_16x16x32_bf16 v[136:139], a[164:167], a[52:55], v[136:139]\n"
"ds_read_b64_tr_b16 a[194:195], v234 offset:53760\n"
"v_cvt_pk_bf16_f32 v112, v132, v133\n"
"v_mfma_f32_16x16x32_bf16 v[140:143], a[140:143], a[84:87], v[140:143]\n"
"ds_read_b64_tr_b16 a[196:197], v235 offset:28672\n"
"v_cvt_pk_bf16_f32 v113, v134, v135\n"
"v_mfma_f32_16x16x32_bf16 v[140:143], a[144:147], a[88:91], v[140:143]\n"
"ds_read_b64_tr_b16 a[198:199], v235 offset:29184\n"
"v_mfma_f32_16x16x32_bf16 v[140:143], a[148:151], a[92:95], v[140:143]\n"
"ds_read_b64_tr_b16 a[200:201], v235 offset:32768\n"
"v_mul_f32 v136, s88, v136\n"
"v_mfma_f32_16x16x32_bf16 v[140:143], a[152:155], a[96:99], v[140:143]\n"
"ds_read_b64_tr_b16 a[202:203], v235 offset:33280\n"
"v_mul_f32 v137, s88, v137\n"
"v_mfma_f32_16x16x32_bf16 v[140:143], a[156:159], a[100:103], v[140:143]\n"
"ds_read_b64_tr_b16 a[204:205], v235 offset:36864\n"
"v_mul_f32 v138, s88, v138\n"
"v_mfma_f32_16x16x32_bf16 v[140:143], a[160:163], a[104:107], v[140:143]\n"
"ds_read_b64_tr_b16 a[206:207], v235 offset:37376\n"
"v_mul_f32 v139, s88, v139\n"
"v_mfma_f32_16x16x32_bf16 v[140:143], a[164:167], a[108:111], v[140:143]\n"
"s_waitcnt lgkmcnt(8)\n"
"ds_read_b64_tr_b16 a[208:209], v235 offset:40960\n"
"v_cvt_pk_bf16_f32 v82, v136, v137\n"
"v_mfma_f32_16x16x32_bf16 v[144:147], a[168:171], a[28:31], v[144:147]\n"
"ds_read_b64_tr_b16 a[210:211], v235 offset:41472\n"
"v_cvt_pk_bf16_f32 v83, v138, v139\n"
"v_mfma_f32_16x16x32_bf16 v[144:147], a[172:175], a[32:35], v[144:147]\n"
"ds_read_b64_tr_b16 a[212:213], v235 offset:45056\n"
"v_mfma_f32_16x16x32_bf16 v[144:147], a[176:179], a[36:39], v[144:147]\n"
"ds_read_b64_tr_b16 a[214:215], v235 offset:45568\n"
"v_permlane16_swap_b32 v80, v82\n"
"v_mfma_f32_16x16x32_bf16 v[144:147], a[180:183], a[40:43], v[144:147]\n"
"ds_read_b64_tr_b16 a[216:217], v235 offset:49152\n"
"v_permlane16_swap_b32 v81, v83\n"
"v_mfma_f32_16x16x32_bf16 v[144:147], a[184:187], a[44:47], v[144:147]\n"
"ds_read_b64_tr_b16 a[218:219], v235 offset:49664\n"
"v_mul_f32 v140, s88, v140\n"
"v_mfma_f32_16x16x32_bf16 v[144:147], a[188:191], a[48:51], v[144:147]\n"
"s_waitcnt vmcnt(14) lgkmcnt(8)\n"
"ds_read_b64_tr_b16 a[220:221], v235 offset:53248\n"
"v_mul_f32 v141, s88, v141\n"
"v_mfma_f32_16x16x32_bf16 v[144:147], a[192:195], a[52:55], v[144:147]\n"
"s_barrier\n"
"ds_read_b64_tr_b16 a[222:223], v235 offset:53760\n"
"v_mul_f32 v142, s88, v142\n"
"v_mfma_f32_16x16x32_bf16 v[148:151], a[168:171], a[84:87], v[148:151]\n"
"s_add_u32 m0, s83, 0\n"
"ds_read_b64_tr_b16 a[112:113], v236 offset:0\n"
"v_mul_f32 v143, s88, v143\n"
"v_mfma_f32_16x16x32_bf16 v[148:151], a[172:175], a[88:91], v[148:151]\n"
"buffer_load_dwordx4 v255, s[48:51], s85 offen offset:0 nt lds\n"
"v_cvt_pk_bf16_f32 v114, v140, v141\n"
"v_mfma_f32_16x16x32_bf16 v[148:151], a[176:179], a[92:95], v[148:151]\n"
"ds_read_b64_tr_b16 a[114:115], v236 offset:512\n"
"ds_read_b64_tr_b16 a[116:117], v236 offset:4096\n"
"v_mfma_f32_16x16x32_bf16 v[148:151], a[180:183], a[96:99], v[148:151]\n"
"ds_read_b64_tr_b16 a[118:119], v236 offset:4608\n"
"v_cvt_pk_bf16_f32 v115, v142, v143\n"
"v_mfma_f32_16x16x32_bf16 v[148:151], a[184:187], a[100:103], v[148:151]\n"
"s_add_u32 m0, s83, 544\n"
"ds_read_b64_tr_b16 a[120:121], v236 offset:8192\n"
"v_mul_f32 v144, s88, v144\n"
"v_mfma_f32_16x16x32_bf16 v[148:151], a[188:191], a[104:107], v[148:151]\n"
"buffer_load_dwordx4 v255, s[48:51], s85 offen offset:512 nt lds\n"
"v_permlane16_swap_b32 v112, v114\n"
"v_mfma_f32_16x16x32_bf16 v[148:151], a[192:195], a[108:111], v[148:151]\n"
"ds_read_b64_tr_b16 a[122:123], v236 offset:8704\n"
"ds_read_b64_tr_b16 a[124:125], v236 offset:12288\n"
"v_mfma_f32_16x16x32_bf16 v[152:155], a[196:199], a[28:31], v[152:155]\n"
"ds_read_b64_tr_b16 a[126:127], v236 offset:12800\n"
"v_permlane16_swap_b32 v113, v115\n"
"v_mfma_f32_16x16x32_bf16 v[152:155], a[200:203], a[32:35], v[152:155]\n"
"s_waitcnt lgkmcnt(8)\n"
"s_add_u32 m0, s83, 1088\n"
"ds_read_b64_tr_b16 a[128:129], v236 offset:16384\n"
"v_mul_f32 v145, s88, v145\n"
"v_mfma_f32_16x16x32_bf16 v[152:155], a[204:207], a[36:39], v[152:155]\n"
"buffer_load_dwordx4 v255, s[48:51], s85 offen offset:1024 nt lds\n"
"v_mul_f32 v146, s88, v146\n"
"v_mfma_f32_16x16x32_bf16 v[152:155], a[208:211], a[40:43], v[152:155]\n"
"ds_read_b64_tr_b16 a[130:131], v236 offset:16896\n"
"ds_read_b64_tr_b16 a[132:133], v236 offset:20480\n"
"v_mfma_f32_16x16x32_bf16 v[152:155], a[212:215], a[44:47], v[152:155]\n"
"ds_read_b64_tr_b16 a[134:135], v236 offset:20992\n"
"v_mul_f32 v147, s88, v147\n"
"v_mfma_f32_16x16x32_bf16 v[152:155], a[216:219], a[48:51], v[152:155]\n"
"s_add_u32 m0, s83, 1632\n"
"ds_read_b64_tr_b16 a[136:137], v236 offset:24576\n"
"v_cvt_pk_bf16_f32 v84, v144, v145\n"
"v_mfma_f32_16x16x32_bf16 v[152:155], a[220:223], a[52:55], v[152:155]\n"
"buffer_load_dwordx4 v255, s[48:51], s85 offen offset:1536 nt lds\n"
"v_cvt_pk_bf16_f32 v85, v146, v147\n"
"v_mfma_f32_16x16x32_bf16 v[156:159], a[196:199], a[84:87], v[156:159]\n"
"ds_read_b64_tr_b16 a[138:139], v236 offset:25088\n"
"ds_read_b64_tr_b16 a[140:141], v237 offset:0\n"
"v_mfma_f32_16x16x32_bf16 v[156:159], a[200:203], a[88:91], v[156:159]\n"
"ds_read_b64_tr_b16 a[142:143], v237 offset:512\n"
"v_mul_f32 v148, s88, v148\n"
"v_mfma_f32_16x16x32_bf16 v[156:159], a[204:207], a[92:95], v[156:159]\n"
"s_add_u32 s85, s85, 0x4000\n"
"ds_read_b64_tr_b16 a[144:145], v237 offset:4096\n"
"v_mul_f32 v152, s88, v152\n"
"v_mfma_f32_16x16x32_bf16 v[156:159], a[208:211], a[96:99], v[156:159]\n"
"s_cmp_lt_u32 s85, 0x80000\n"
"ds_read_b64_tr_b16 a[146:147], v237 offset:4608\n"
"v_mul_f32 v153, s88, v153\n"
"v_mfma_f32_16x16x32_bf16 v[156:159], a[212:215], a[100:103], v[156:159]\n"
"s_cselect_b32 s50, 0x80000, 0\n"
"ds_read_b64_tr_b16 a[148:149], v237 offset:8192\n"
"v_mul_f32 v154, s88, v154\n"
"v_mfma_f32_16x16x32_bf16 v[156:159], a[216:219], a[104:107], v[156:159]\n"
"s_add_u32 s83, s83, 16896\n"
"ds_read_b64_tr_b16 a[150:151], v237 offset:8704\n"
"v_mul_f32 v155, s88, v155\n"
"v_mfma_f32_16x16x32_bf16 v[156:159], a[220:223], a[108:111], v[156:159]\n"
"s_waitcnt lgkmcnt(8)\n"
"s_cmp_ge_u32 s83, s91\n"
"ds_read_b64_tr_b16 a[152:153], v237 offset:12288\n"
"v_cvt_pk_bf16_f32 v86, v152, v153\n"
"v_mfma_f32_16x16x32_bf16 v[160:163], a[112:115], a[0:3], 0\n"
"s_cselect_b32 s87, 84480, 0\n"
"ds_read_b64_tr_b16 a[154:155], v237 offset:12800\n"
"v_cvt_pk_bf16_f32 v87, v154, v155\n"
"v_mfma_f32_16x16x32_bf16 v[160:163], a[116:119], a[4:7], v[160:163]\n"
"s_sub_u32 s83, s83, s87\n"
"ds_read_b64_tr_b16 a[156:157], v237 offset:16384\n"
"v_mul_f32 v149, s88, v149\n"
"v_mfma_f32_16x16x32_bf16 v[160:163], a[120:123], a[8:11], v[160:163]\n"
"ds_read_b64_tr_b16 a[158:159], v237 offset:16896\n"
"v_permlane16_swap_b32 v84, v86\n"
"v_mfma_f32_16x16x32_bf16 v[160:163], a[124:127], a[12:15], v[160:163]\n"
"ds_read_b64_tr_b16 a[160:161], v237 offset:20480\n"
"v_permlane16_swap_b32 v85, v87\n"
"v_mfma_f32_16x16x32_bf16 v[160:163], a[128:131], a[16:19], v[160:163]\n"
"ds_read_b64_tr_b16 a[162:163], v237 offset:20992\n"
"v_mul_f32 v150, s88, v150\n"
"v_mfma_f32_16x16x32_bf16 v[160:163], a[132:135], a[20:23], v[160:163]\n"
"s_waitcnt lgkmcnt(8)\n"
"ds_read_b64_tr_b16 a[164:165], v237 offset:24576\n"
"v_mul_f32 v151, s88, v151\n"
"v_mfma_f32_16x16x32_bf16 v[160:163], a[136:139], a[24:27], v[160:163]\n"
"ds_read_b64_tr_b16 a[166:167], v237 offset:25088\n"
"v_cvt_pk_bf16_f32 v116, v148, v149\n"
"v_mfma_f32_16x16x32_bf16 v[164:167], a[112:115], a[56:59], 0\n"
"ds_read_b64_tr_b16 a[168:169], v238 offset:0\n"
"v_cvt_pk_bf16_f32 v117, v150, v151\n"
"v_mfma_f32_16x16x32_bf16 v[164:167], a[116:119], a[60:63], v[164:167]\n"
"ds_read_b64_tr_b16 a[170:171], v238 offset:512\n"
"v_mul_f32 v156, s88, v156\n"
"v_mfma_f32_16x16x32_bf16 v[164:167], a[120:123], a[64:67], v[164:167]\n"
"ds_read_b64_tr_b16 a[172:173], v238 offset:4096\n"
"v_mul_f32 v157, s88, v157\n"
"v_mfma_f32_16x16x32_bf16 v[164:167], a[124:127], a[68:71], v[164:167]\n"
"ds_read_b64_tr_b16 a[174:175], v238 offset:4608\n"
"v_mul_f32 v158, s88, v158\n"
"v_mfma_f32_16x16x32_bf16 v[164:167], a[128:131], a[72:75], v[164:167]\n"
"ds_read_b64_tr_b16 a[176:177], v238 offset:8192\n"
"v_mul_f32 v159, s88, v159\n"
"v_mfma_f32_16x16x32_bf16 v[164:167], a[132:135], a[76:79], v[164:167]\n"
"ds_read_b64_tr_b16 a[178:179], v238 offset:8704\n"
"v_cvt_pk_bf16_f32 v118, v156, v157\n"
"v_mfma_f32_16x16x32_bf16 v[164:167], a[136:139], a[80:83], v[164:167]\n"
"ds_read_b64_tr_b16 a[180:181], v238 offset:12288\n"
"v_cvt_pk_bf16_f32 v119, v158, v159\n"
"v_mfma_f32_16x16x32_bf16 v[168:171], a[140:143], a[0:3], 0\n"
"ds_read_b64_tr_b16 a[182:183], v238 offset:12800\n"
"v_mfma_f32_16x16x32_bf16 v[168:171], a[144:147], a[4:7], v[168:171]\n"
"s_waitcnt lgkmcnt(8)\n"
"ds_read_b64_tr_b16 a[184:185], v238 offset:16384\n"
"v_permlane16_swap_b32 v116, v118\n"
"v_mfma_f32_16x16x32_bf16 v[168:171], a[148:151], a[8:11], v[168:171]\n"
"ds_read_b64_tr_b16 a[186:187], v238 offset:16896\n"
"v_permlane16_swap_b32 v117, v119\n"
"v_mfma_f32_16x16x32_bf16 v[168:171], a[152:155], a[12:15], v[168:171]\n"
"ds_read_b64_tr_b16 a[188:189], v238 offset:20480\n"
"v_mfma_f32_16x16x32_bf16 v[168:171], a[156:159], a[16:19], v[168:171]\n"
"ds_read_b64_tr_b16 a[190:191], v238 offset:20992\n"
"v_mfma_f32_16x16x32_bf16 v[168:171], a[160:163], a[20:23], v[168:171]\n"
"ds_read_b64_tr_b16 a[192:193], v238 offset:24576\n"
"v_mfma_f32_16x16x32_bf16 v[168:171], a[164:167], a[24:27], v[168:171]\n"
"ds_read_b64_tr_b16 a[194:195], v238 offset:25088\n"
"v_mfma_f32_16x16x32_bf16 v[172:175], a[140:143], a[56:59], 0\n"
"ds_read_b64_tr_b16 a[196:197], v239 offset:0\n"
"v_mfma_f32_16x16x32_bf16 v[172:175], a[144:147], a[60:63], v[172:175]\n"
"ds_read_b64_tr_b16 a[198:199], v239 offset:512\n"
"v_mfma_f32_16x16x32_bf16 v[172:175], a[148:151], a[64:67], v[172:175]\n"
"ds_read_b64_tr_b16 a[200:201], v239 offset:4096\n"
"v_mfma_f32_16x16x32_bf16 v[172:175], a[152:155], a[68:71], v[172:175]\n"
"ds_read_b64_tr_b16 a[202:203], v239 offset:4608\n"
"v_mfma_f32_16x16x32_bf16 v[172:175], a[156:159], a[72:75], v[172:175]\n"
"ds_read_b64_tr_b16 a[204:205], v239 offset:8192\n"
"v_mfma_f32_16x16x32_bf16 v[172:175], a[160:163], a[76:79], v[172:175]\n"
"ds_read_b64_tr_b16 a[206:207], v239 offset:8704\n"
"v_mfma_f32_16x16x32_bf16 v[172:175], a[164:167], a[80:83], v[172:175]\n"
"s_waitcnt lgkmcnt(8)\n"
"ds_read_b64_tr_b16 a[208:209], v239 offset:12288\n"
"v_mfma_f32_16x16x32_bf16 v[176:179], a[168:171], a[0:3], 0\n"
"ds_read_b64_tr_b16 a[210:211], v239 offset:12800\n"
"v_mfma_f32_16x16x32_bf16 v[176:179], a[172:175], a[4:7], v[176:179]\n"
"ds_read_b64_tr_b16 a[212:213], v239 offset:16384\n"
"v_mfma_f32_16x16x32_bf16 v[176:179], a[176:179], a[8:11], v[176:179]\n"
"ds_read_b64_tr_b16 a[214:215], v239 offset:16896\n"
"v_mfma_f32_16x16x32_bf16 v[176:179], a[180:183], a[12:15], v[176:179]\n"
"ds_read_b64_tr_b16 a[216:217], v239 offset:20480\n"
"v_mfma_f32_16x16x32_bf16 v[176:179], a[184:187], a[16:19], v[176:179]\n"
"s_waitcnt vmcnt(46)\n"
"ds_read_b64_tr_b16 a[218:219], v239 offset:20992\n"
"v_mfma_f32_16x16x32_bf16 v[176:179], a[188:191], a[20:23], v[176:179]\n"
"s_waitcnt vmcnt(11) lgkmcnt(8)\n"
"ds_read_b64_tr_b16 a[220:221], v239 offset:24576\n"
"v_mfma_f32_16x16x32_bf16 v[176:179], a[192:195], a[24:27], v[176:179]\n"
"s_barrier\n"
"ds_read_b64_tr_b16 a[222:223], v239 offset:25088\n"
"v_mfma_f32_16x16x32_bf16 v[180:183], a[168:171], a[56:59], 0\n"
"s_add_u32 m0, s82, 33792\n"
"ds_read_b64_tr_b16 a[112:113], v236 offset:28672\n"
"v_mfma_f32_16x16x32_bf16 v[180:183], a[172:175], a[60:63], v[180:183]\n"
"buffer_load_dwordx4 v255, s[44:47], s84 offen offset:0 lds\n"
"v_mfma_f32_16x16x32_bf16 v[180:183], a[176:179], a[64:67], v[180:183]\n"
"ds_read_b64_tr_b16 a[114:115], v236 offset:29184\n"
"ds_read_b64_tr_b16 a[116:117], v236 offset:32768\n"
"v_mfma_f32_16x16x32_bf16 v[180:183], a[180:183], a[68:71], v[180:183]\n"
"ds_read_b64_tr_b16 a[118:119], v236 offset:33280\n"
"v_mfma_f32_16x16x32_bf16 v[180:183], a[184:187], a[72:75], v[180:183]\n"
"s_add_u32 m0, s82, 34336\n"
"ds_read_b64_tr_b16 a[120:121], v236 offset:36864\n"
"v_mfma_f32_16x16x32_bf16 v[180:183], a[188:191], a[76:79], v[180:183]\n"
"buffer_load_dwordx4 v255, s[44:47], s84 offen offset:512 lds\n"
"v_mfma_f32_16x16x32_bf16 v[180:183], a[192:195], a[80:83], v[180:183]\n"
"ds_read_b64_tr_b16 a[122:123], v236 offset:37376\n"
"ds_read_b64_tr_b16 a[124:125], v236 offset:40960\n"
"v_mfma_f32_16x16x32_bf16 v[184:187], a[196:199], a[0:3], 0\n"
"ds_read_b64_tr_b16 a[126:127], v236 offset:41472\n"
"v_mfma_f32_16x16x32_bf16 v[184:187], a[200:203], a[4:7], v[184:187]\n"
"s_waitcnt lgkmcnt(8)\n"
"s_add_u32 m0, s82, 34880\n"
"ds_read_b64_tr_b16 a[128:129], v236 offset:45056\n"
"v_mfma_f32_16x16x32_bf16 v[184:187], a[204:207], a[8:11], v[184:187]\n"
"buffer_load_dwordx4 v255, s[44:47], s84 offen offset:1024 nt lds\n"
"v_mfma_f32_16x16x32_bf16 v[184:187], a[208:211], a[12:15], v[184:187]\n"
"ds_read_b64_tr_b16 a[130:131], v236 offset:45568\n"
"ds_read_b64_tr_b16 a[132:133], v236 offset:49152\n"
"v_mfma_f32_16x16x32_bf16 v[184:187], a[212:215], a[16:19], v[184:187]\n"
"ds_read_b64_tr_b16 a[134:135], v236 offset:49664\n"
"v_mfma_f32_16x16x32_bf16 v[184:187], a[216:219], a[20:23], v[184:187]\n"
"s_add_u32 m0, s82, 35424\n"
"ds_read_b64_tr_b16 a[136:137], v236 offset:53248\n"
"v_mfma_f32_16x16x32_bf16 v[184:187], a[220:223], a[24:27], v[184:187]\n"
"buffer_load_dwordx4 v255, s[44:47], s84 offen offset:1536 nt lds\n"
"v_mfma_f32_16x16x32_bf16 v[188:191], a[196:199], a[56:59], 0\n"
"ds_read_b64_tr_b16 a[138:139], v236 offset:53760\n"
"ds_read_b64_tr_b16 a[140:141], v237 offset:28672\n"
"v_mfma_f32_16x16x32_bf16 v[188:191], a[200:203], a[60:63], v[188:191]\n"
"ds_read_b64_tr_b16 a[142:143], v237 offset:29184\n"
"v_mfma_f32_16x16x32_bf16 v[188:191], a[204:207], a[64:67], v[188:191]\n"
"s_add_u32 s84, s84, 0x4000\n"
"ds_read_b64_tr_b16 a[144:145], v237 offset:32768\n"
"v_mfma_f32_16x16x32_bf16 v[188:191], a[208:211], a[68:71], v[188:191]\n"
"s_cmp_lt_u32 s84, 0x80000\n"
"ds_read_b64_tr_b16 a[146:147], v237 offset:33280\n"
"v_mfma_f32_16x16x32_bf16 v[188:191], a[212:215], a[72:75], v[188:191]\n"
"s_cselect_b32 s46, 0x80000, 0\n"
"ds_read_b64_tr_b16 a[148:149], v237 offset:36864\n"
"v_mfma_f32_16x16x32_bf16 v[188:191], a[216:219], a[76:79], v[188:191]\n"
"ds_read_b64_tr_b16 a[150:151], v237 offset:37376\n"
"v_mfma_f32_16x16x32_bf16 v[188:191], a[220:223], a[80:83], v[188:191]\n"
"s_waitcnt lgkmcnt(8)\n"
"ds_read_b64_tr_b16 a[152:153], v237 offset:40960\n"
"v_mfma_f32_16x16x32_bf16 v[160:163], a[112:115], a[28:31], v[160:163]\n"
"ds_read_b64_tr_b16 a[154:155], v237 offset:41472\n"
"v_mfma_f32_16x16x32_bf16 v[160:163], a[116:119], a[32:35], v[160:163]\n"
"ds_read_b64_tr_b16 a[156:157], v237 offset:45056\n"
"v_mfma_f32_16x16x32_bf16 v[160:163], a[120:123], a[36:39], v[160:163]\n"
"ds_read_b64_tr_b16 a[158:159], v237 offset:45568\n"
"v_mfma_f32_16x16x32_bf16 v[160:163], a[124:127], a[40:43], v[160:163]\n"
"ds_read_b64_tr_b16 a[160:161], v237 offset:49152\n"
"v_mfma_f32_16x16x32_bf16 v[160:163], a[128:131], a[44:47], v[160:163]\n"
"ds_read_b64_tr_b16 a[162:163], v237 offset:49664\n"
"v_mfma_f32_16x16x32_bf16 v[160:163], a[132:135], a[48:51], v[160:163]\n"
"s_waitcnt lgkmcnt(8)\n"
"ds_read_b64_tr_b16 a[164:165], v237 offset:53248\n"
"v_mfma_f32_16x16x32_bf16 v[160:163], a[136:139], a[52:55], v[160:163]\n"
"ds_read_b64_tr_b16 a[166:167], v237 offset:53760\n"
"v_mfma_f32_16x16x32_bf16 v[164:167], a[112:115], a[84:87], v[164:167]\n"
"ds_read_b64_tr_b16 a[168:169], v238 offset:28672\n"
"v_mfma_f32_16x16x32_bf16 v[164:167], a[116:119], a[88:91], v[164:167]\n"
"ds_read_b64_tr_b16 a[170:171], v238 offset:29184\n"
"v_mfma_f32_16x16x32_bf16 v[164:167], a[120:123], a[92:95], v[164:167]\n"
"ds_read_b64_tr_b16 a[172:173], v238 offset:32768\n"
"v_mul_f32 v160, s88, v160\n"
"v_mfma_f32_16x16x32_bf16 v[164:167], a[124:127], a[96:99], v[164:167]\n"
"ds_read_b64_tr_b16 a[174:175], v238 offset:33280\n"
"v_mul_f32 v161, s88, v161\n"
"v_mfma_f32_16x16x32_bf16 v[164:167], a[128:131], a[100:103], v[164:167]\n"
"ds_read_b64_tr_b16 a[176:177], v238 offset:36864\n"
"v_mul_f32 v162, s88, v162\n"
"v_mfma_f32_16x16x32_bf16 v[164:167], a[132:135], a[104:107], v[164:167]\n"
"ds_read_b64_tr_b16 a[178:179], v238 offset:37376\n"
"v_mul_f32 v163, s88, v163\n"
"v_mfma_f32_16x16x32_bf16 v[164:167], a[136:139], a[108:111], v[164:167]\n"
"ds_read_b64_tr_b16 a[180:181], v238 offset:40960\n"
"v_cvt_pk_bf16_f32 v88, v160, v161\n"
"v_mfma_f32_16x16x32_bf16 v[168:171], a[140:143], a[28:31], v[168:171]\n"
"ds_read_b64_tr_b16 a[182:183], v238 offset:41472\n"
"v_cvt_pk_bf16_f32 v89, v162, v163\n"
"v_mfma_f32_16x16x32_bf16 v[168:171], a[144:147], a[32:35], v[168:171]\n"
"s_waitcnt lgkmcnt(8)\n"
"ds_read_b64_tr_b16 a[184:185], v238 offset:45056\n"
"v_mfma_f32_16x16x32_bf16 v[168:171], a[148:151], a[36:39], v[168:171]\n"
"s_waitcnt vmcnt(12)\n"
"ds_read_b64_tr_b16 a[186:187], v238 offset:45568\n"
"v_mul_f32 v164, s88, v164\n"
"v_mfma_f32_16x16x32_bf16 v[168:171], a[152:155], a[40:43], v[168:171]\n"
"ds_read_b64_tr_b16 a[188:189], v238 offset:49152\n"
"v_readfirstlane_b32 s96, v250\n"
"v_mfma_f32_16x16x32_bf16 v[168:171], a[156:159], a[44:47], v[168:171]\n"
"ds_read_b64_tr_b16 a[190:191], v238 offset:49664\n"
"v_readfirstlane_b32 s97, v251\n"
"v_mfma_f32_16x16x32_bf16 v[168:171], a[160:163], a[48:51], v[168:171]\n"
"ds_read_b64_tr_b16 a[192:193], v238 offset:53248\n"
"v_mul_f32 v165, s88, v165\n"
"v_mfma_f32_16x16x32_bf16 v[168:171], a[164:167], a[52:55], v[168:171]\n"
"ds_read_b64_tr_b16 a[194:195], v238 offset:53760\n"
"v_mul_f32 v166, s88, v166\n"
"v_mfma_f32_16x16x32_bf16 v[172:175], a[140:143], a[84:87], v[172:175]\n"
"ds_read_b64_tr_b16 a[196:197], v239 offset:28672\n"
"v_mul_f32 v167, s88, v167\n"
"v_mfma_f32_16x16x32_bf16 v[172:175], a[144:147], a[88:91], v[172:175]\n"
"ds_read_b64_tr_b16 a[198:199], v239 offset:29184\n"
"v_cvt_pk_bf16_f32 v120, v164, v165\n"
"v_mfma_f32_16x16x32_bf16 v[172:175], a[148:151], a[92:95], v[172:175]\n"
"ds_read_b64_tr_b16 a[200:201], v239 offset:32768\n"
"v_mul_f32 v168, s88, v168\n"
"v_mfma_f32_16x16x32_bf16 v[172:175], a[152:155], a[96:99], v[172:175]\n"
"ds_read_b64_tr_b16 a[202:203], v239 offset:33280\n"
"v_mul_f32 v169, s88, v169\n"
"v_mfma_f32_16x16x32_bf16 v[172:175], a[156:159], a[100:103], v[172:175]\n"
"ds_read_b64_tr_b16 a[204:205], v239 offset:36864\n"
"v_mul_f32 v170, s88, v170\n"
"v_mfma_f32_16x16x32_bf16 v[172:175], a[160:163], a[104:107], v[172:175]\n"
"ds_read_b64_tr_b16 a[206:207], v239 offset:37376\n"
"v_mul_f32 v171, s88, v171\n"
"v_mfma_f32_16x16x32_bf16 v[172:175], a[164:167], a[108:111], v[172:175]\n"
"s_waitcnt lgkmcnt(8)\n"
"ds_read_b64_tr_b16 a[208:209], v239 offset:40960\n"
"v_cvt_pk_bf16_f32 v90, v168, v169\n"
"v_mfma_f32_16x16x32_bf16 v[176:179], a[168:171], a[28:31], v[176:179]\n"
"ds_read_b64_tr_b16 a[210:211], v239 offset:41472\n"
"v_cvt_pk_bf16_f32 v91, v170, v171\n"
"v_mfma_f32_16x16x32_bf16 v[176:179], a[172:175], a[32:35], v[176:179]\n"
"ds_read_b64_tr_b16 a[212:213], v239 offset:45056\n"
"v_cvt_pk_bf16_f32 v121, v166, v167\n"
"v_mfma_f32_16x16x32_bf16 v[176:179], a[176:179], a[36:39], v[176:179]\n"
"ds_read_b64_tr_b16 a[214:215], v239 offset:45568\n"
"v_permlane16_swap_b32 v88, v90\n"
"v_mfma_f32_16x16x32_bf16 v[176:179], a[180:183], a[40:43], v[176:179]\n"
"ds_read_b64_tr_b16 a[216:217], v239 offset:49152\n"
"v_permlane16_swap_b32 v89, v91\n"
"v_mfma_f32_16x16x32_bf16 v[176:179], a[184:187], a[44:47], v[176:179]\n"
"ds_read_b64_tr_b16 a[218:219], v239 offset:49664\n"
"v_mul_f32 v172, s88, v172\n"
"v_mfma_f32_16x16x32_bf16 v[176:179], a[188:191], a[48:51], v[176:179]\n"
"s_waitcnt lgkmcnt(8)\n"
"ds_read_b64_tr_b16 a[220:221], v239 offset:53248\n"
"v_mul_f32 v173, s88, v173\n"
"v_mfma_f32_16x16x32_bf16 v[176:179], a[192:195], a[52:55], v[176:179]\n"
"ds_read_b64_tr_b16 a[222:223], v239 offset:53760\n"
"v_mul_f32 v174, s88, v174\n"
"v_mfma_f32_16x16x32_bf16 v[180:183], a[168:171], a[84:87], v[180:183]\n"
"v_mul_f32 v175, s88, v175\n"
"v_cvt_pk_bf16_f32 v122, v172, v173\n"
"v_mfma_f32_16x16x32_bf16 v[180:183], a[172:175], a[88:91], v[180:183]\n"
"v_cvt_pk_bf16_f32 v123, v174, v175\n"
"v_mfma_f32_16x16x32_bf16 v[180:183], a[176:179], a[92:95], v[180:183]\n"
"v_mul_f32 v176, s88, v176\n"
"v_mul_f32 v177, s88, v177\n"
"v_mfma_f32_16x16x32_bf16 v[180:183], a[180:183], a[96:99], v[180:183]\n"
"v_permlane16_swap_b32 v120, v122\n"
"v_mfma_f32_16x16x32_bf16 v[180:183], a[184:187], a[100:103], v[180:183]\n"
"v_permlane16_swap_b32 v121, v123\n"
"v_mfma_f32_16x16x32_bf16 v[180:183], a[188:191], a[104:107], v[180:183]\n"
"v_mul_f32 v178, s88, v178\n"
"v_mul_f32 v179, s88, v179\n"
"v_mfma_f32_16x16x32_bf16 v[180:183], a[192:195], a[108:111], v[180:183]\n"
"v_cvt_pk_bf16_f32 v92, v176, v177\n"
"v_cvt_pk_bf16_f32 v93, v178, v179\n"
"v_mfma_f32_16x16x32_bf16 v[184:187], a[196:199], a[28:31], v[184:187]\n"
"v_mfma_f32_16x16x32_bf16 v[184:187], a[200:203], a[32:35], v[184:187]\n"
"s_waitcnt lgkmcnt(0)\n"
"v_mfma_f32_16x16x32_bf16 v[184:187], a[204:207], a[36:39], v[184:187]\n"
"s_add_u32 s96, s96, 0x7f4a7c15\n"
"ds_read_b128 v[200:203], v252 offset:0\n"
"v_mul_f32 v180, s88, v180\n"
"v_mfma_f32_16x16x32_bf16 v[184:187], a[208:211], a[40:43], v[184:187]\n"
"s_addc_u32 s97, s97, 0x9e3779b9\n"
"v_mul_f32 v181, s88, v181\n"
"v_mul_f32 v182, s88, v182\n"
"v_mfma_f32_16x16x32_bf16 v[184:187], a[212:215], a[44:47], v[184:187]\n"
"s_sub_u32 s36, vcc_lo, s36\n"
"ds_read_b128 v[204:207], v252 offset:64\n"
"v_mul_f32 v183, s88, v183\n"
"v_mfma_f32_16x16x32_bf16 v[184:187], a[216:219], a[48:51], v[184:187]\n"
"s_sub_u32 s38, s92, s38\n"
"v_cvt_pk_bf16_f32 v124, v180, v181\n"
"v_cvt_pk_bf16_f32 v125, v182, v183\n"
"v_mfma_f32_16x16x32_bf16 v[184:187], a[220:223], a[52:55], v[184:187]\n"
"s_mul_i32 s38, s38, 159\n"
"ds_read_b128 v[208:211], v252 offset:128\n"
"v_mfma_f32_16x16x32_bf16 v[188:191], a[196:199], a[84:87], v[188:191]\n"
"s_mul_i32 s36, s36, 10\n"
"v_mfma_f32_16x16x32_bf16 v[188:191], a[200:203], a[88:91], v[188:191]\n"
"s_cmp_gt_u32 s36, s38\n"
"ds_read_b128 v[212:215], v252 offset:192\n"
"v_mfma_f32_16x16x32_bf16 v[188:191], a[204:207], a[92:95], v[188:191]\n"
"s_cselect_b32 s41, 2304, 0\n"
"v_mul_f32 v184, s88, v184\n"
"v_mul_f32 v185, s88, v185\n"
"v_mfma_f32_16x16x32_bf16 v[188:191], a[208:211], a[96:99], v[188:191]\n"
"s_mov_b32 s39, 0x20000\n"
"ds_read_b128 v[216:219], v252 offset:256\n"
"v_mul_f32 v186, s88, v186\n"
"v_mfma_f32_16x16x32_bf16 v[188:191], a[212:215], a[100:103], v[188:191]\n"
"s_mov_b32 s92, 33792\n"
"v_mul_f32 v187, s88, v187\n"
"v_cvt_pk_bf16_f32 v94, v184, v185\n"
"v_mfma_f32_16x16x32_bf16 v[188:191], a[216:219], a[104:107], v[188:191]\n"
"s_mov_b32 s93, -50688\n"
"ds_read_b128 v[220:223], v252 offset:320\n"
"v_cvt_pk_bf16_f32 v95, v186, v187\n"
"v_mfma_f32_16x16x32_bf16 v[188:191], a[220:223], a[108:111], v[188:191]\n"
"s_add_u32 s25, s18, 1270\n"
"v_mov_b32 v232, 0x7f800000\n"
"v_mov_b32 v236, 0x7f800000\n"
"v_mov_b32 v240, 0x7f800000\n"
"v_mov_b32 v244, 0x7f800000\n"
"s_nop 5\n"
"v_permlane16_swap_b32 v92, v94\n"
"v_permlane16_swap_b32 v93, v95\n"
"v_mul_f32 v188, s88, v188\n"
"v_mul_f32 v189, s88, v189\n"
"v_mul_f32 v190, s88, v190\n"
"v_mul_f32 v191, s88, v191\n"
"v_cvt_pk_bf16_f32 v126, v188, v189\n"
"v_cvt_pk_bf16_f32 v127, v190, v191\n"
"s_nop 1\n"
"v_permlane16_swap_b32 v124, v126\n"
"v_permlane16_swap_b32 v125, v127\n"
"Lrerun%=:\n"
"s_waitcnt lgkmcnt(5)\n"
"v_mfma_f32_16x16x32_bf16 v[138:141], v[200:203], v[0:3], 0\n"
"s_waitcnt vmcnt(13) lgkmcnt(4)\n"
"ds_read_b128 v[224:227], v252 offset:384\n"
"v_mfma_f32_16x16x32_bf16 v[138:141], v[204:207], v[4:7], v[138:141]\n"
"s_barrier\n"
"ds_read_b128 v[228:231], v252 offset:448\n"
"v_mfma_f32_16x16x32_bf16 v[156:159], v[200:203], v[32:35], 0\n"
"s_mov_b32 s64, 0\n"
"v_mov_b32 v248, 0\n"
"v_mov_b32 v249, 0\n"
"v_mfma_f32_16x16x32_bf16 v[156:159], v[204:207], v[36:39], v[156:159]\n"
"s_mov_b32 s65, 28672\n"
"v_mov_b32 v250, 0\n"
"v_mov_b32 v251, 0\n"
"v_mfma_f32_16x16x32_bf16 v[174:177], v[200:203], v[64:67], 0\n"
"s_mov_b32 s66, 57344\n"
"v_mov_b32 v132, 0\n"
"v_mov_b32 v133, 0\n"
"v_mfma_f32_16x16x32_bf16 v[174:177], v[204:207], v[68:71], v[174:177]\n"
"s_add_u32 m0, s83, 0\n"
"v_mov_b32 v134, 0\n"
"v_mov_b32 v135, 0\n"
"v_mfma_f32_16x16x32_bf16 v[192:195], v[200:203], v[96:99], 0\n"
"s_mov_b32 s67, 86016\n"
"buffer_load_dwordx4 v255, s[48:51], s85 offen offset:0 nt lds\n"
"v_mfma_f32_16x16x32_bf16 v[192:195], v[204:207], v[100:103], v[192:195]\n"
"s_waitcnt lgkmcnt(5)\n"
"s_add_u32 m0, s83, 544\n"
"v_mfma_f32_16x16x32_bf16 v[138:141], v[208:211], v[8:11], v[138:141]\n"
"s_waitcnt lgkmcnt(2)\n"
"s_mov_b32 s68, 114688\n"
"buffer_load_dwordx4 v255, s[48:51], s85 offen offset:512 nt lds\n"
"v_mfma_f32_16x16x32_bf16 v[138:141], v[212:215], v[12:15], v[138:141]\n"
"s_add_u32 m0, s83, 1088\n"
"ds_read_b128 v[200:203], v252 offset:512\n"
"ds_read_b128 v[204:207], v252 offset:576\n"
"v_mfma_f32_16x16x32_bf16 v[156:159], v[208:211], v[40:43], v[156:159]\n"
"s_mov_b32 s69, 143360\n"
"buffer_load_dwordx4 v255, s[48:51], s85 offen offset:1024 nt lds\n"
"v_mfma_f32_16x16x32_bf16 v[156:159], v[212:215], v[44:47], v[156:159]\n"
"s_add_u32 m0, s83, 1632\n"
"v_mfma_f32_16x16x32_bf16 v[174:177], v[208:211], v[72:75], v[174:177]\n"
"s_mov_b32 s70, 172032\n"
"buffer_load_dwordx4 v255, s[48:51], s85 offen offset:1536 nt lds\n"
"v_mfma_f32_16x16x32_bf16 v[174:177], v[212:215], v[76:79], v[174:177]\n"
"s_add_u32 s85, s85, 0x4000\n"
"v_mfma_f32_16x16x32_bf16 v[192:195], v[208:211], v[104:107], v[192:195]\n"
"s_cmp_lt_u32 s85, 0x80000\n"
"v_mfma_f32_16x16x32_bf16 v[192:195], v[212:215], v[108:111], v[192:195]\n"
"s_cselect_b32 s50, 0x80000, 0\n"
"v_mfma_f32_16x16x32_bf16 v[138:141], v[216:219], v[16:19], v[138:141]\n"
"s_add_u32 s83, s83, 16896\n"
"ds_read_b128 v[208:211], v252 offset:640\n"
"v_mfma_f32_16x16x32_bf16 v[138:141], v[220:223], v[20:23], v[138:141]\n"
"s_cmp_ge_u32 s83, s91\n"
"ds_read_b128 v[212:215], v252 offset:704\n"
"v_mfma_f32_16x16x32_bf16 v[156:159], v[216:219], v[48:51], v[156:159]\n"
"s_cselect_b32 s87, 84480, 0\n"
"v_mfma_f32_16x16x32_bf16 v[156:159], v[220:223], v[52:55], v[156:159]\n"
"s_sub_u32 s83, s83, s87\n"
"v_mfma_f32_16x16x32_bf16 v[174:177], v[216:219], v[80:83], v[174:177]\n"
"s_add_u32 m0, s82, 50688\n"
"v_mfma_f32_16x16x32_bf16 v[174:177], v[220:223], v[84:87], v[174:177]\n"
"s_mov_b32 s71, 200704\n"
"buffer_load_dwordx4 v255, s[44:47], s84 offen offset:0 lds\n"
"v_mfma_f32_16x16x32_bf16 v[192:195], v[216:219], v[112:115], v[192:195]\n"
"s_add_u32 m0, s82, 51232\n"
"v_mfma_f32_16x16x32_bf16 v[192:195], v[220:223], v[116:119], v[192:195]\n"
"s_waitcnt lgkmcnt(2)\n"
"s_mov_b32 s72, 229376\n"
"buffer_load_dwordx4 v255, s[44:47], s84 offen offset:512 lds\n"
"v_mfma_f32_16x16x32_bf16 v[138:141], v[224:227], v[24:27], v[138:141]\n"
"s_add_u32 m0, s82, 51776\n"
"ds_read_b128 v[216:219], v252 offset:768\n"
"v_mfma_f32_16x16x32_bf16 v[138:141], v[228:231], v[28:31], v[138:141]\n"
"s_mov_b32 s73, 258048\n"
"buffer_load_dwordx4 v255, s[44:47], s84 offen offset:1024 nt lds\n"
"v_mfma_f32_16x16x32_bf16 v[156:159], v[224:227], v[56:59], v[156:159]\n"
"s_add_u32 m0, s82, 52320\n"
"ds_read_b128 v[220:223], v252 offset:832\n"
"v_mfma_f32_16x16x32_bf16 v[156:159], v[228:231], v[60:63], v[156:159]\n"
"s_mov_b32 s74, 286720\n"
"buffer_load_dwordx4 v255, s[44:47], s84 offen offset:1536 nt lds\n"
"v_mfma_f32_16x16x32_bf16 v[174:177], v[224:227], v[88:91], v[174:177]\n"
"s_add_u32 s84, s84, 0x4000\n"
"v_mfma_f32_16x16x32_bf16 v[174:177], v[228:231], v[92:95], v[174:177]\n"
"s_cmp_lt_u32 s84, 0x80000\n"
"v_mfma_f32_16x16x32_bf16 v[192:195], v[224:227], v[120:123], v[192:195]\n"
"s_cselect_b32 s46, 0x80000, 0\n"
"v_mfma_f32_16x16x32_bf16 v[192:195], v[228:231], v[124:127], v[192:195]\n"
"s_mov_b32 s75, 315392\n"
"v_mfma_f32_32x32x16_bf16 a[0:15], v[132:135], v[132:135], 0\n"
"s_mov_b32 s76, 344064\n"
"v_mfma_f32_32x32x16_bf16 a[16:31], v[132:135], v[132:135], 0\n"
"s_mov_b32 s77, 372736\n"
"v_max3_f32 v128, v138, v139, v140\n"
"v_max_f32 v128, v128, v141\n"
"v_mov_b32 v129, v128\n"
"v_max3_f32 v146, v156, v157, v158\n"
"v_max_f32 v146, v146, v159\n"
"v_mov_b32 v147, v146\n"
"v_max3_f32 v164, v174, v175, v176\n"
"v_mfma_f32_32x32x16_bf16 a[32:47], v[132:135], v[132:135], 0\n"
"s_mov_b32 s78, 401408\n"
"v_max_f32 v164, v164, v177\n"
"v_mov_b32 v165, v164\n"
"v_max3_f32 v182, v192, v193, v194\n"
"v_max_f32 v182, v182, v195\n"
"v_mov_b32 v183, v182\n"
"v_permlane32_swap_b32 v128, v129\n"
"v_mfma_f32_32x32x16_bf16 a[48:63], v[132:135], v[132:135], 0\n"
"s_mov_b32 s79, 430080\n"
"v_permlane32_swap_b32 v146, v147\n"
"v_permlane32_swap_b32 v164, v165\n"
"v_permlane32_swap_b32 v182, v183\n"
"v_max_f32 v128, v128, v129\n"
"v_mfma_f32_32x32x16_bf16 a[64:79], v[132:135], v[132:135], 0\n"
"v_mov_b32 v129, v128\n"
"v_max_f32 v146, v146, v147\n"
"v_mov_b32 v147, v146\n"
"v_max_f32 v164, v164, v165\n"
"v_mov_b32 v165, v164\n"
"v_max_f32 v182, v182, v183\n"
"v_mov_b32 v183, v182\n"
"v_mfma_f32_32x32x16_bf16 a[80:95], v[132:135], v[132:135], 0\n"
"v_permlane16_swap_b32 v128, v129\n"
"v_permlane16_swap_b32 v146, v147\n"
"v_permlane16_swap_b32 v164, v165\n"
"v_permlane16_swap_b32 v182, v183\n"
"v_mfma_f32_32x32x16_bf16 a[96:111], v[132:135], v[132:135], 0\n"
"v_max_f32 v128, v128, v129\n"
"v_min_f32_e64 v232, -v128, v232\n"
"v_mov_b32 v233, v232\n"
"v_mov_b32 v234, v232\n"
"v_mov_b32 v235, v232\n"
"v_max_f32 v146, v146, v147\n"
"v_min_f32_e64 v236, -v146, v236\n"
"v_mfma_f32_32x32x16_bf16 a[112:127], v[132:135], v[132:135], 0\n"
"v_mov_b32 v237, v236\n"
"v_mov_b32 v238, v236\n"
"v_mov_b32 v239, v236\n"
"v_max_f32 v164, v164, v165\n"
"v_min_f32_e64 v240, -v164, v240\n"
"v_mov_b32 v241, v240\n"
"v_mov_b32 v242, v240\n"
"v_mov_b32 v243, v240\n"
"v_max_f32 v182, v182, v183\n"
"v_min_f32_e64 v244, -v182, v244\n"
"v_mov_b32 v245, v244\n"
"v_mov_b32 v246, v244\n"
"v_mov_b32 v247, v244\n"
"v_mfma_f32_32x32x16_bf16 a[128:143], v[132:135], v[132:135], 0\n"
"v_add_f32 v138, v138, v232\n"
"v_add_f32 v139, v139, v232\n"
"v_add_f32 v140, v140, v232\n"
"v_add_f32 v141, v141, v232\n"
"v_exp_f32 v138, v138\n"
"v_exp_f32 v139, v139\n"
"v_mfma_f32_16x16x32_bf16 v[142:145], v[200:203], v[0:3], v[232:235]\n"
"ds_read_b128 v[224:227], v252 offset:896\n"
"v_exp_f32 v140, v140\n"
"v_mfma_f32_16x16x32_bf16 v[142:145], v[204:207], v[4:7], v[142:145]\n"
"ds_read_b128 v[228:231], v252 offset:960\n"
"v_exp_f32 v141, v141\n"
"v_mfma_f32_16x16x32_bf16 v[160:163], v[200:203], v[32:35], v[236:239]\n"
"v_add_f32 v248, v248, v138\n"
"v_add_f32 v248, v248, v139\n"
"v_add_f32 v248, v248, v140\n"
"v_mfma_f32_16x16x32_bf16 v[160:163], v[204:207], v[36:39], v[160:163]\n"
"v_add_f32 v248, v248, v141\n"
"v_cvt_pk_bf16_f32 v128, v138, v139\n"
"v_cvt_pk_bf16_f32 v129, v140, v141\n"
"v_mfma_f32_16x16x32_bf16 v[178:181], v[200:203], v[64:67], v[240:243]\n"
"v_add_f32 v156, v156, v236\n"
"v_add_f32 v157, v157, v236\n"
"v_add_f32 v158, v158, v236\n"
"v_mfma_f32_16x16x32_bf16 v[178:181], v[204:207], v[68:71], v[178:181]\n"
"v_add_f32 v159, v159, v236\n"
"v_exp_f32 v156, v156\n"
"v_mfma_f32_16x16x32_bf16 v[196:199], v[200:203], v[96:99], v[244:247]\n"
"v_exp_f32 v157, v157\n"
"v_exp_f32 v158, v158\n"
"v_mfma_f32_16x16x32_bf16 v[196:199], v[204:207], v[100:103], v[196:199]\n"
"s_waitcnt lgkmcnt(2)\n"
"v_exp_f32 v159, v159\n"
"v_add_f32 v249, v249, v156\n"
"v_mfma_f32_16x16x32_bf16 v[142:145], v[208:211], v[8:11], v[142:145]\n"
"s_waitcnt vmcnt(16) lgkmcnt(0)\n"
"v_add_f32 v249, v249, v157\n"
"v_add_f32 v249, v249, v158\n"
"v_add_f32 v249, v249, v159\n"
"v_mfma_f32_16x16x32_bf16 v[142:145], v[212:215], v[12:15], v[142:145]\n"
"s_barrier\n"
"v_cvt_pk_bf16_f32 v146, v156, v157\n"
"v_cvt_pk_bf16_f32 v147, v158, v159\n"
"v_add_f32 v174, v174, v240\n"
"v_mfma_f32_16x16x32_bf16 v[160:163], v[208:211], v[40:43], v[160:163]\n"
"ds_read_b128 v[200:203], v252 offset:16896\n"
"v_add_f32 v175, v175, v240\n"
"v_add_f32 v176, v176, v240\n"
"v_mfma_f32_16x16x32_bf16 v[160:163], v[212:215], v[44:47], v[160:163]\n"
"ds_read_b128 v[204:207], v252 offset:16960\n"
"v_add_f32 v177, v177, v240\n"
"v_exp_f32 v174, v174\n"
"v_mfma_f32_16x16x32_bf16 v[178:181], v[208:211], v[72:75], v[178:181]\n"
"v_exp_f32 v175, v175\n"
"v_exp_f32 v176, v176\n"
"v_mfma_f32_16x16x32_bf16 v[178:181], v[212:215], v[76:79], v[178:181]\n"
"v_exp_f32 v177, v177\n"
"v_mfma_f32_16x16x32_bf16 v[196:199], v[208:211], v[104:107], v[196:199]\n"
"v_add_f32 v250, v250, v174\n"
"v_add_f32 v250, v250, v175\n"
"v_mfma_f32_16x16x32_bf16 v[196:199], v[212:215], v[108:111], v[196:199]\n"
"v_add_f32 v250, v250, v176\n"
"v_add_f32 v250, v250, v177\n"
"v_mfma_f32_16x16x32_bf16 v[142:145], v[216:219], v[16:19], v[142:145]\n"
"s_add_u32 m0, s83, 0\n"
"ds_read_b128 v[208:211], v252 offset:17024\n"
"v_cvt_pk_bf16_f32 v164, v174, v175\n"
"v_mfma_f32_16x16x32_bf16 v[142:145], v[220:223], v[20:23], v[142:145]\n"
"buffer_load_dwordx4 v255, s[48:51], s85 offen offset:0 nt lds\n"
"v_cvt_pk_bf16_f32 v165, v176, v177\n"
"v_mfma_f32_16x16x32_bf16 v[160:163], v[216:219], v[48:51], v[160:163]\n"
"ds_read_b128 v[212:215], v252 offset:17088\n"
"v_add_f32 v192, v192, v244\n"
"v_mfma_f32_16x16x32_bf16 v[160:163], v[220:223], v[52:55], v[160:163]\n"
"s_add_u32 m0, s83, 544\n"
"v_add_f32 v193, v193, v244\n"
"v_add_f32 v194, v194, v244\n"
"v_mfma_f32_16x16x32_bf16 v[178:181], v[216:219], v[80:83], v[178:181]\n"
"buffer_load_dwordx4 v255, s[48:51], s85 offen offset:512 nt lds\n"
"v_add_f32 v195, v195, v244\n"
"v_mfma_f32_16x16x32_bf16 v[178:181], v[220:223], v[84:87], v[178:181]\n"
"v_exp_f32 v192, v192\n"
"v_mfma_f32_16x16x32_bf16 v[196:199], v[216:219], v[112:115], v[196:199]\n"
"s_add_u32 m0, s83, 1088\n"
"v_exp_f32 v193, v193\n"
"v_mfma_f32_16x16x32_bf16 v[196:199], v[220:223], v[116:119], v[196:199]\n"
"buffer_load_dwordx4 v255, s[48:51], s85 offen offset:1024 nt lds\n"
"v_mfma_f32_16x16x32_bf16 v[142:145], v[224:227], v[24:27], v[142:145]\n"
"ds_read_b128 v[216:219], v252 offset:17152\n"
"v_mfma_f32_16x16x32_bf16 v[142:145], v[228:231], v[28:31], v[142:145]\n"
"s_add_u32 m0, s83, 1632\n"
"ds_read_b128 v[220:223], v252 offset:17216\n"
"v_mfma_f32_16x16x32_bf16 v[160:163], v[224:227], v[56:59], v[160:163]\n"
"buffer_load_dwordx4 v255, s[48:51], s85 offen offset:1536 nt lds\n"
"v_mfma_f32_16x16x32_bf16 v[160:163], v[228:231], v[60:63], v[160:163]\n"
"v_exp_f32 v194, v194\n"
"v_mfma_f32_16x16x32_bf16 v[178:181], v[224:227], v[88:91], v[178:181]\n"
"s_add_u32 s85, s85, 0x4000\n"
"v_exp_f32 v195, v195\n"
"v_mfma_f32_16x16x32_bf16 v[178:181], v[228:231], v[92:95], v[178:181]\n"
"s_cmp_lt_u32 s85, 0x80000\n"
"v_add_f32 v251, v251, v192\n"
"v_add_f32 v251, v251, v193\n"
"v_mfma_f32_16x16x32_bf16 v[196:199], v[224:227], v[120:123], v[196:199]\n"
"s_cselect_b32 s50, 0x80000, 0\n"
"v_add_f32 v251, v251, v194\n"
"v_add_f32 v251, v251, v195\n"
"v_mfma_f32_16x16x32_bf16 v[196:199], v[228:231], v[124:127], v[196:199]\n"
"s_waitcnt lgkmcnt(2)\n"
"s_add_u32 s83, s83, 16896\n"
"v_cvt_pk_bf16_f32 v182, v192, v193\n"
"v_cvt_pk_bf16_f32 v183, v194, v195\n"
"v_mfma_f32_16x16x32_bf16 v[138:141], v[200:203], v[0:3], v[232:235]\n"
"s_cmp_ge_u32 s83, s91\n"
"ds_read_b128 v[224:227], v252 offset:17280\n"
"v_exp_f32 v142, v142\n"
"v_mfma_f32_16x16x32_bf16 v[138:141], v[204:207], v[4:7], v[138:141]\n"
"s_cselect_b32 s87, 84480, 0\n"
"ds_read_b128 v[228:231], v252 offset:17344\n"
"v_exp_f32 v143, v143\n"
"v_mfma_f32_16x16x32_bf16 v[156:159], v[200:203], v[32:35], v[236:239]\n"
"s_sub_u32 s83, s83, s87\n"
"v_exp_f32 v144, v144\n"
"v_exp_f32 v145, v145\n"
"v_mfma_f32_16x16x32_bf16 v[156:159], v[204:207], v[36:39], v[156:159]\n"
"s_add_u32 m0, s82, 0\n"
"v_add_f32 v248, v248, v142\n"
"v_add_f32 v248, v248, v143\n"
"v_add_f32 v248, v248, v144\n"
"v_mfma_f32_16x16x32_bf16 v[174:177], v[200:203], v[64:67], v[240:243]\n"
"buffer_load_dwordx4 v255, s[44:47], s84 offen offset:0 lds\n"
"v_add_f32 v248, v248, v145\n"
"v_cvt_pk_bf16_f32 v130, v142, v143\n"
"v_mfma_f32_16x16x32_bf16 v[174:177], v[204:207], v[68:71], v[174:177]\n"
"v_cvt_pk_bf16_f32 v131, v144, v145\n"
"v_exp_f32 v160, v160\n"
"v_mfma_f32_16x16x32_bf16 v[192:195], v[200:203], v[96:99], v[244:247]\n"
"s_add_u32 m0, s82, 544\n"
"v_exp_f32 v161, v161\n"
"v_exp_f32 v162, v162\n"
"v_mfma_f32_16x16x32_bf16 v[192:195], v[204:207], v[100:103], v[192:195]\n"
"buffer_load_dwordx4 v255, s[44:47], s84 offen offset:512 lds\n"
"v_mfma_f32_16x16x32_bf16 v[138:141], v[208:211], v[8:11], v[138:141]\n"
"ds_read_b128 v[200:203], v252 offset:17408\n"
"v_mfma_f32_16x16x32_bf16 v[138:141], v[212:215], v[12:15], v[138:141]\n"
"s_add_u32 m0, s82, 1088\n"
"ds_read_b128 v[204:207], v252 offset:17472\n"
"v_exp_f32 v163, v163\n"
"v_mfma_f32_16x16x32_bf16 v[156:159], v[208:211], v[40:43], v[156:159]\n"
"buffer_load_dwordx4 v255, s[44:47], s84 offen offset:1024 nt lds\n"
"v_add_f32 v249, v249, v160\n"
"v_mfma_f32_16x16x32_bf16 v[156:159], v[212:215], v[44:47], v[156:159]\n"
"v_add_f32 v249, v249, v161\n"
"v_add_f32 v249, v249, v162\n"
"v_mfma_f32_16x16x32_bf16 v[174:177], v[208:211], v[72:75], v[174:177]\n"
"s_add_u32 m0, s82, 1632\n"
"v_add_f32 v249, v249, v163\n"
"v_cvt_pk_bf16_f32 v148, v160, v161\n"
"v_mfma_f32_16x16x32_bf16 v[174:177], v[212:215], v[76:79], v[174:177]\n"
"buffer_load_dwordx4 v255, s[44:47], s84 offen offset:1536 nt lds\n"
"v_cvt_pk_bf16_f32 v149, v162, v163\n"
"v_mfma_f32_16x16x32_bf16 v[192:195], v[208:211], v[104:107], v[192:195]\n"
"v_exp_f32 v178, v178\n"
"v_mfma_f32_16x16x32_bf16 v[192:195], v[212:215], v[108:111], v[192:195]\n"
"s_waitcnt lgkmcnt(2)\n"
"s_add_u32 s84, s84, 0x4000\n"
"v_exp_f32 v179, v179\n"
"v_mfma_f32_16x16x32_bf16 v[138:141], v[216:219], v[16:19], v[138:141]\n"
"s_cmp_lt_u32 s84, 0x80000\n"
"ds_read_b128 v[208:211], v252 offset:17536\n"
"v_mfma_f32_16x16x32_bf16 v[138:141], v[220:223], v[20:23], v[138:141]\n"
"s_cselect_b32 s46, 0x80000, 0\n"
"ds_read_b128 v[212:215], v252 offset:17600\n"
"v_mfma_f32_16x16x32_bf16 v[156:159], v[216:219], v[48:51], v[156:159]\n"
"v_exp_f32 v180, v180\n"
"v_mfma_f32_16x16x32_bf16 v[156:159], v[220:223], v[52:55], v[156:159]\n"
"v_exp_f32 v181, v181\n"
"v_mfma_f32_16x16x32_bf16 v[174:177], v[216:219], v[80:83], v[174:177]\n"
"v_add_f32 v250, v250, v178\n"
"v_add_f32 v250, v250, v179\n"
"v_mfma_f32_16x16x32_bf16 v[174:177], v[220:223], v[84:87], v[174:177]\n"
"v_add_f32 v250, v250, v180\n"
"v_add_f32 v250, v250, v181\n"
"v_mfma_f32_16x16x32_bf16 v[192:195], v[216:219], v[112:115], v[192:195]\n"
"v_cvt_pk_bf16_f32 v166, v178, v179\n"
"v_cvt_pk_bf16_f32 v167, v180, v181\n"
"v_mfma_f32_16x16x32_bf16 v[192:195], v[220:223], v[116:119], v[192:195]\n"
"v_exp_f32 v196, v196\n"
"v_mfma_f32_16x16x32_bf16 v[138:141], v[224:227], v[24:27], v[138:141]\n"
"ds_read_b128 v[216:219], v252 offset:17664\n"
"v_mfma_f32_16x16x32_bf16 v[138:141], v[228:231], v[28:31], v[138:141]\n"
"ds_read_b128 v[220:223], v252 offset:17728\n"
"v_exp_f32 v197, v197\n"
"v_mfma_f32_16x16x32_bf16 v[156:159], v[224:227], v[56:59], v[156:159]\n"
"v_exp_f32 v198, v198\n"
"v_mfma_f32_16x16x32_bf16 v[156:159], v[228:231], v[60:63], v[156:159]\n"
"v_exp_f32 v199, v199\n"
"v_mfma_f32_16x16x32_bf16 v[174:177], v[224:227], v[88:91], v[174:177]\n"
"v_add_f32 v251, v251, v196\n"
"v_add_f32 v251, v251, v197\n"
"v_mfma_f32_16x16x32_bf16 v[174:177], v[228:231], v[92:95], v[174:177]\n"
"v_add_f32 v251, v251, v198\n"
"v_add_f32 v251, v251, v199\n"
"v_mfma_f32_16x16x32_bf16 v[192:195], v[224:227], v[120:123], v[192:195]\n"
"v_cvt_pk_bf16_f32 v184, v196, v197\n"
"v_cvt_pk_bf16_f32 v185, v198, v199\n"
"v_mfma_f32_16x16x32_bf16 v[192:195], v[228:231], v[124:127], v[192:195]\n"
"s_waitcnt lgkmcnt(2)\n"
"v_mfma_f32_16x16x32_bf16 v[142:145], v[200:203], v[0:3], v[232:235]\n"
"ds_read_b128 v[224:227], v252 offset:17792\n"
"v_exp_f32 v138, v138\n"
"v_mfma_f32_16x16x32_bf16 v[142:145], v[204:207], v[4:7], v[142:145]\n"
"ds_read_b128 v[228:231], v252 offset:17856\n"
"v_exp_f32 v139, v139\n"
"v_mfma_f32_16x16x32_bf16 v[160:163], v[200:203], v[32:35], v[236:239]\n"
"v_exp_f32 v140, v140\n"
"v_exp_f32 v141, v141\n"
"v_mfma_f32_16x16x32_bf16 v[160:163], v[204:207], v[36:39], v[160:163]\n"
"v_add_f32 v248, v248, v138\n"
"v_add_f32 v248, v248, v139\n"
"v_mfma_f32_16x16x32_bf16 v[178:181], v[200:203], v[64:67], v[240:243]\n"
"v_add_f32 v248, v248, v140\n"
"v_add_f32 v248, v248, v141\n"
"v_mfma_f32_16x16x32_bf16 v[178:181], v[204:207], v[68:71], v[178:181]\n"
"v_cvt_pk_bf16_f32 v132, v138, v139\n"
"v_cvt_pk_bf16_f32 v133, v140, v141\n"
"v_mfma_f32_16x16x32_bf16 v[196:199], v[200:203], v[96:99], v[244:247]\n"
"v_exp_f32 v156, v156\n"
"v_mfma_f32_16x16x32_bf16 v[196:199], v[204:207], v[100:103], v[196:199]\n"
"v_exp_f32 v157, v157\n"
"v_mfma_f32_16x16x32_bf16 v[142:145], v[208:211], v[8:11], v[142:145]\n"
"v_exp_f32 v158, v158\n"
"v_mfma_f32_16x16x32_bf16 v[142:145], v[212:215], v[12:15], v[142:145]\n"
"v_exp_f32 v159, v159\n"
"v_mfma_f32_16x16x32_bf16 v[160:163], v[208:211], v[40:43], v[160:163]\n"
"v_add_f32 v249, v249, v156\n"
"v_add_f32 v249, v249, v157\n"
"v_mfma_f32_16x16x32_bf16 v[160:163], v[212:215], v[44:47], v[160:163]\n"
"s_waitcnt vmcnt(12) lgkmcnt(0)\n"
"v_add_f32 v249, v249, v158\n"
"v_add_f32 v249, v249, v159\n"
"v_mfma_f32_16x16x32_bf16 v[178:181], v[208:211], v[72:75], v[178:181]\n"
"s_barrier\n"
"v_cvt_pk_bf16_f32 v150, v156, v157\n"
"v_cvt_pk_bf16_f32 v151, v158, v159\n"
"v_mfma_f32_16x16x32_bf16 v[178:181], v[212:215], v[76:79], v[178:181]\n"
"ds_read_b128 v[200:203], v252 offset:33792\n"
"v_mfma_f32_16x16x32_bf16 v[196:199], v[208:211], v[104:107], v[196:199]\n"
"s_mov_b32 s81, 8\n"
"ds_read_b128 v[204:207], v252 offset:33856\n"
"v_mfma_f32_16x16x32_bf16 v[196:199], v[212:215], v[108:111], v[196:199]\n"
"s_cmp_gt_i32 s41, 0\n"
"v_exp_f32 v174, v174\n"
"v_mfma_f32_16x16x32_bf16 v[142:145], v[216:219], v[16:19], v[142:145]\n"
"s_cselect_b32 s88, 0, 0\n"
"ds_read_b128 v[208:211], v252 offset:33920\n"
"v_exp_f32 v175, v175\n"
"v_mfma_f32_16x16x32_bf16 v[142:145], v[220:223], v[20:23], v[142:145]\n"
"s_cselect_b32 s40, 163, 0\n"
"ds_read_b128 v[212:215], v252 offset:33984\n"
"v_mfma_f32_16x16x32_bf16 v[160:163], v[216:219], v[48:51], v[160:163]\n"
"s_cselect_b32 s42, 1280, 0\n"
"v_exp_f32 v176, v176\n"
"v_mfma_f32_16x16x32_bf16 v[160:163], v[220:223], v[52:55], v[160:163]\n"
"s_bfe_u32 s87, s35, 0x10012\n"
"v_exp_f32 v177, v177\n"
"v_mfma_f32_16x16x32_bf16 v[178:181], v[216:219], v[80:83], v[178:181]\n"
"s_lshl_b32 s33, s87, 16\n"
"v_add_f32 v250, v250, v174\n"
"v_add_f32 v250, v250, v175\n"
"v_mfma_f32_16x16x32_bf16 v[178:181], v[220:223], v[84:87], v[178:181]\n"
"s_sub_u32 s33, 0x40000, s33\n"
"v_add_f32 v250, v250, v176\n"
"v_add_f32 v250, v250, v177\n"
"v_mfma_f32_16x16x32_bf16 v[196:199], v[216:219], v[112:115], v[196:199]\n"
"s_lshl_b32 s87, s87, 17\n"
"v_cvt_pk_bf16_f32 v168, v174, v175\n"
"v_cvt_pk_bf16_f32 v169, v176, v177\n"
"v_mfma_f32_16x16x32_bf16 v[196:199], v[220:223], v[116:119], v[196:199]\n"
"s_bfe_u32 s86, s35, 0x20010\n"
"v_exp_f32 v192, v192\n"
"v_mfma_f32_16x16x32_bf16 v[142:145], v[224:227], v[24:27], v[142:145]\n"
"s_lshl_b32 s86, s86, 12\n"
"ds_read_b128 v[216:219], v252 offset:34048\n"
"v_mfma_f32_16x16x32_bf16 v[142:145], v[228:231], v[28:31], v[142:145]\n"
"s_add_u32 s87, s87, s86\n"
"ds_read_b128 v[220:223], v252 offset:34112\n"
"v_exp_f32 v193, v193\n"
"v_mfma_f32_16x16x32_bf16 v[160:163], v[224:227], v[56:59], v[160:163]\n"
"s_add_u32 s36, s52, s87\n"
"v_exp_f32 v194, v194\n"
"v_mfma_f32_16x16x32_bf16 v[160:163], v[228:231], v[60:63], v[160:163]\n"
"s_addc_u32 s37, s53, 0x80000\n"
"v_exp_f32 v195, v195\n"
"v_mfma_f32_16x16x32_bf16 v[178:181], v[224:227], v[88:91], v[178:181]\n"
"v_add_f32 v251, v251, v192\n"
"v_add_f32 v251, v251, v193\n"
"v_mfma_f32_16x16x32_bf16 v[178:181], v[228:231], v[92:95], v[178:181]\n"
"v_add_f32 v251, v251, v194\n"
"v_add_f32 v251, v251, v195\n"
"v_mfma_f32_16x16x32_bf16 v[196:199], v[224:227], v[120:123], v[196:199]\n"
"v_cvt_pk_bf16_f32 v186, v192, v193\n"
"v_cvt_pk_bf16_f32 v187, v194, v195\n"
"v_mfma_f32_16x16x32_bf16 v[196:199], v[228:231], v[124:127], v[196:199]\n"
"v_mfma_f32_32x32x16_bf16 a[144:159], a[0:3], a[0:3], 0\n"
"v_exp_f32 v142, v142\n"
"v_exp_f32 v143, v143\n"
"v_exp_f32 v144, v144\n"
"v_exp_f32 v145, v145\n"
"v_mfma_f32_32x32x16_bf16 a[160:175], a[0:3], a[0:3], 0\n"
"v_add_f32 v248, v248, v142\n"
"v_add_f32 v248, v248, v143\n"
"v_add_f32 v248, v248, v144\n"
"v_add_f32 v248, v248, v145\n"
"v_cvt_pk_bf16_f32 v134, v142, v143\n"
"v_cvt_pk_bf16_f32 v135, v144, v145\n"
"v_exp_f32 v160, v160\n"
"v_mfma_f32_32x32x16_bf16 a[176:191], a[0:3], a[0:3], 0\n"
"v_exp_f32 v161, v161\n"
"v_exp_f32 v162, v162\n"
"v_exp_f32 v163, v163\n"
"v_add_f32 v249, v249, v160\n"
"v_mfma_f32_32x32x16_bf16 a[192:207], a[0:3], a[0:3], 0\n"
"v_add_f32 v249, v249, v161\n"
"v_add_f32 v249, v249, v162\n"
"v_add_f32 v249, v249, v163\n"
"v_cvt_pk_bf16_f32 v152, v160, v161\n"
"v_cvt_pk_bf16_f32 v153, v162, v163\n"
"v_exp_f32 v178, v178\n"
"v_mfma_f32_32x32x16_bf16 a[208:223], a[0:3], a[0:3], 0\n"
"s_memrealtime vcc\n"
"v_exp_f32 v179, v179\n"
"v_exp_f32 v180, v180\n"
"v_exp_f32 v181, v181\n"
"v_add_f32 v250, v250, v178\n"
"v_mfma_f32_32x32x16_bf16 a[224:239], a[0:3], a[0:3], 0\n"
"v_add_f32 v250, v250, v179\n"
"v_add_f32 v250, v250, v180\n"
"v_add_f32 v250, v250, v181\n"
"v_cvt_pk_bf16_f32 v170, v178, v179\n"
"v_cvt_pk_bf16_f32 v171, v180, v181\n"
"v_exp_f32 v196, v196\n"
"v_mfma_f32_32x32x16_bf16 a[240:255], a[0:3], a[0:3], 0\n"
"v_exp_f32 v197, v197\n"
"v_exp_f32 v198, v198\n"
"v_exp_f32 v199, v199\n"
"v_add_f32 v251, v251, v196\n"
"v_add_f32 v251, v251, v197\n"
"v_add_f32 v251, v251, v198\n"
"v_add_f32 v251, v251, v199\n"
"v_cvt_pk_bf16_f32 v188, v196, v197\n"
"v_cvt_pk_bf16_f32 v189, v198, v199\n"
"s_waitcnt lgkmcnt(0)\n"
"s_add_u32 s40, s40, vcc_lo\n"
"s_sub_i32 s43, s25, vcc_lo\n"
"s_min_i32 s43, s43, 90\n"
"s_mul_i32 s43, s43, 13\n"
"s_ashr_i32 s43, s43, 3\n"
"s_add_i32 s41, s41, s43\n"
"s_branch Lloopb%=\n"
".p2align 6\n"
"Lloopa%=:\n"
"s_waitcnt lgkmcnt(5)\n"
"v_mfma_f32_16x16x32_bf16 v[132:135], v[200:203], v[0:3], v[232:235]\n"
"s_waitcnt lgkmcnt(4)\n"
"s_cmp_ge_u32 s89, 50688\n"
"ds_read_b128 v[224:227], v252 offset:384\n"
"v_mfma_f32_16x16x32_bf16 v[132:135], v[204:207], v[4:7], v[132:135]\n"
"s_cselect_b32 s86, s93, s92\n"
"ds_read_b128 v[228:231], v252 offset:448\n"
"v_mfma_f32_16x16x32_bf16 v[150:153], v[200:203], v[32:35], v[236:239]\n"
"s_add_u32 s89, s89, s86\n"
"v_add_u32 v253, s86, v253\n"
"v_mfma_f32_16x16x32_bf16 v[150:153], v[204:207], v[36:39], v[150:153]\n"
"s_cmp_ge_u32 s90, 50688\n"
"v_mfma_f32_16x16x32_bf16 v[168:171], v[200:203], v[64:67], v[240:243]\n"
"s_cselect_b32 s86, s93, s92\n"
"v_mfma_f32_16x16x32_bf16 v[168:171], v[204:207], v[68:71], v[168:171]\n"
"s_add_u32 s90, s90, s86\n"
"v_add_u32 v254, s86, v254\n"
"v_mfma_f32_16x16x32_bf16 v[186:189], v[200:203], v[96:99], v[244:247]\n"
"Ldll0%=:\n"
"s_sub_u32 s42, s42, 32\n"
"s_cbranch_scc0 Ldll0%=\n"
"v_mfma_f32_16x16x32_bf16 v[186:189], v[204:207], v[100:103], v[186:189]\n"
"s_waitcnt lgkmcnt(5)\n"
"v_mfma_f32_16x16x32_bf16 v[132:135], v[208:211], v[8:11], v[132:135]\n"
"s_waitcnt lgkmcnt(4)\n"
"ds_read_b64_tr_b16 v[200:201], v253 offset:0\n"
"v_mfma_f32_16x16x32_bf16 v[132:135], v[212:215], v[12:15], v[132:135]\n"
"ds_read_b64_tr_b16 v[202:203], v253 offset:512\n"
"v_mfma_f32_16x16x32_bf16 v[150:153], v[208:211], v[40:43], v[150:153]\n"
"ds_read_b64_tr_b16 v[206:207], v254 offset:0\n"
"v_mfma_f32_16x16x32_bf16 v[150:153], v[212:215], v[44:47], v[150:153]\n"
"ds_read_b64_tr_b16 v[204:205], v254 offset:512\n"
"v_mfma_f32_16x16x32_bf16 v[168:171], v[208:211], v[72:75], v[168:171]\n"
"s_add_u32 m0, s83, 0\n"
"v_mfma_f32_16x16x32_bf16 v[168:171], v[212:215], v[76:79], v[168:171]\n"
"buffer_load_dwordx4 v255, s[48:51], s85 offen offset:0 nt lds\n"
"v_mfma_f32_16x16x32_bf16 v[186:189], v[208:211], v[104:107], v[186:189]\n"
"s_add_u32 m0, s83, 544\n"
"v_mfma_f32_16x16x32_bf16 v[186:189], v[212:215], v[108:111], v[186:189]\n"
"s_waitcnt lgkmcnt(4)\n"
"buffer_load_dwordx4 v255, s[48:51], s85 offen offset:512 nt lds\n"
"v_mfma_f32_16x16x32_bf16 v[132:135], v[216:219], v[16:19], v[132:135]\n"
"ds_read_b64_tr_b16 v[208:209], v253 offset:32\n"
"v_mfma_f32_16x16x32_bf16 v[132:135], v[220:223], v[20:23], v[132:135]\n"
"ds_read_b64_tr_b16 v[210:211], v253 offset:544\n"
"v_mfma_f32_16x16x32_bf16 v[150:153], v[216:219], v[48:51], v[150:153]\n"
"ds_read_b64_tr_b16 v[214:215], v254 offset:32\n"
"v_mfma_f32_16x16x32_bf16 v[150:153], v[220:223], v[52:55], v[150:153]\n"
"ds_read_b64_tr_b16 v[212:213], v254 offset:544\n"
"v_mfma_f32_16x16x32_bf16 v[168:171], v[216:219], v[80:83], v[168:171]\n"
"s_add_u32 m0, s83, 1088\n"
"v_mfma_f32_16x16x32_bf16 v[168:171], v[220:223], v[84:87], v[168:171]\n"
"buffer_load_dwordx4 v255, s[48:51], s85 offen offset:1024 nt lds\n"
"v_mfma_f32_16x16x32_bf16 v[186:189], v[216:219], v[112:115], v[186:189]\n"
"s_add_u32 m0, s83, 1632\n"
"v_mfma_f32_16x16x32_bf16 v[186:189], v[220:223], v[116:119], v[186:189]\n"
"buffer_load_dwordx4 v255, s[48:51], s85 offen offset:1536 nt lds\n"
"v_mfma_f32_16x16x32_bf16 v[132:135], v[224:227], v[24:27], v[132:135]\n"
"s_add_u32 s85, s85, 0x4000\n"
"ds_read_b64_tr_b16 v[216:217], v253 offset:64\n"
"v_mfma_f32_16x16x32_bf16 v[132:135], v[228:231], v[28:31], v[132:135]\n"
"s_cmp_lt_u32 s85, 0x80000\n"
"ds_read_b64_tr_b16 v[218:219], v253 offset:576\n"
"v_mfma_f32_16x16x32_bf16 v[150:153], v[224:227], v[56:59], v[150:153]\n"
"s_cselect_b32 s50, 0x80000, 0\n"
"ds_read_b64_tr_b16 v[222:223], v254 offset:64\n"
"v_mfma_f32_16x16x32_bf16 v[150:153], v[228:231], v[60:63], v[150:153]\n"
"s_add_u32 s83, s83, 16896\n"
"ds_read_b64_tr_b16 v[220:221], v254 offset:576\n"
"v_mfma_f32_16x16x32_bf16 v[168:171], v[224:227], v[88:91], v[168:171]\n"
"s_cmp_ge_u32 s83, s91\n"
"v_exp_f32 v132, v132\n"
"v_mfma_f32_16x16x32_bf16 v[168:171], v[228:231], v[92:95], v[168:171]\n"
"s_cselect_b32 s87, 84480, 0\n"
"v_add_f32 v248, v248, v132\n"
"v_mfma_f32_16x16x32_bf16 v[186:189], v[224:227], v[120:123], v[186:189]\n"
"s_sub_u32 s83, s83, s87\n"
"v_exp_f32 v133, v133\n"
"v_mfma_f32_16x16x32_bf16 v[186:189], v[228:231], v[124:127], v[186:189]\n"
"s_waitcnt lgkmcnt(4)\n"
"ds_read_b64_tr_b16 v[224:225], v253 offset:96\n"
"v_add_f32 v248, v248, v133\n"
"v_mfma_f32_16x16x32_bf16 a[0:3], v[200:203], v[142:145], a[0:3]\n"
"v_exp_f32 v134, v134\n"
"v_mfma_f32_16x16x32_bf16 a[0:3], v[204:207], v[138:141], a[0:3]\n"
"ds_read_b64_tr_b16 v[226:227], v253 offset:608\n"
"v_add_f32 v248, v248, v134\n"
"v_mfma_f32_16x16x32_bf16 a[4:7], v[200:203], v[160:163], a[4:7]\n"
"v_exp_f32 v135, v135\n"
"v_mfma_f32_16x16x32_bf16 a[4:7], v[204:207], v[156:159], a[4:7]\n"
"ds_read_b64_tr_b16 v[230:231], v254 offset:96\n"
"v_add_f32 v248, v248, v135\n"
"v_mfma_f32_16x16x32_bf16 a[8:11], v[200:203], v[178:181], a[8:11]\n"
"v_exp_f32 v150, v150\n"
"v_mfma_f32_16x16x32_bf16 a[8:11], v[204:207], v[174:177], a[8:11]\n"
"ds_read_b64_tr_b16 v[228:229], v254 offset:608\n"
"v_cvt_pk_bf16_f32 v128, v132, v133\n"
"v_mfma_f32_16x16x32_bf16 a[12:15], v[200:203], v[196:199], a[12:15]\n"
"v_exp_f32 v151, v151\n"
"v_mfma_f32_16x16x32_bf16 a[12:15], v[204:207], v[192:195], a[12:15]\n"
"v_cvt_pk_bf16_f32 v129, v134, v135\n"
"v_add_f32 v249, v249, v150\n"
"v_mfma_f32_16x16x32_bf16 a[16:19], v[208:211], v[142:145], a[16:19]\n"
"v_exp_f32 v152, v152\n"
"v_mfma_f32_16x16x32_bf16 a[16:19], v[212:215], v[138:141], a[16:19]\n"
"v_add_f32 v249, v249, v151\n"
"v_add_f32 v249, v249, v152\n"
"v_mfma_f32_16x16x32_bf16 a[20:23], v[208:211], v[160:163], a[20:23]\n"
"v_exp_f32 v153, v153\n"
"v_mfma_f32_16x16x32_bf16 a[20:23], v[212:215], v[156:159], a[20:23]\n"
"v_add_f32 v249, v249, v153\n"
"v_cvt_pk_bf16_f32 v146, v150, v151\n"
"v_mfma_f32_16x16x32_bf16 a[24:27], v[208:211], v[178:181], a[24:27]\n"
"v_exp_f32 v168, v168\n"
"v_mfma_f32_16x16x32_bf16 a[24:27], v[212:215], v[174:177], a[24:27]\n"
"v_cvt_pk_bf16_f32 v147, v152, v153\n"
"v_add_f32 v250, v250, v168\n"
"v_mfma_f32_16x16x32_bf16 a[28:31], v[208:211], v[196:199], a[28:31]\n"
"v_exp_f32 v169, v169\n"
"v_mfma_f32_16x16x32_bf16 a[28:31], v[212:215], v[192:195], a[28:31]\n"
"s_waitcnt lgkmcnt(0)\n"
"ds_read_b128 v[200:203], v252 offset:512\n"
"v_add_f32 v250, v250, v169\n"
"v_mfma_f32_16x16x32_bf16 a[32:35], v[216:219], v[142:145], a[32:35]\n"
"v_exp_f32 v170, v170\n"
"v_mfma_f32_16x16x32_bf16 a[32:35], v[220:223], v[138:141], a[32:35]\n"
"ds_read_b128 v[204:207], v252 offset:576\n"
"v_add_f32 v250, v250, v170\n"
"v_mfma_f32_16x16x32_bf16 a[36:39], v[216:219], v[160:163], a[36:39]\n"
"v_exp_f32 v171, v171\n"
"v_mfma_f32_16x16x32_bf16 a[36:39], v[220:223], v[156:159], a[36:39]\n"
"ds_read_b128 v[208:211], v252 offset:640\n"
"v_add_f32 v250, v250, v171\n"
"v_mfma_f32_16x16x32_bf16 a[40:43], v[216:219], v[178:181], a[40:43]\n"
"v_exp_f32 v186, v186\n"
"v_mfma_f32_16x16x32_bf16 a[40:43], v[220:223], v[174:177], a[40:43]\n"
"ds_read_b128 v[212:215], v252 offset:704\n"
"v_cvt_pk_bf16_f32 v164, v168, v169\n"
"v_mfma_f32_16x16x32_bf16 a[44:47], v[216:219], v[196:199], a[44:47]\n"
"v_exp_f32 v187, v187\n"
"v_mfma_f32_16x16x32_bf16 a[44:47], v[220:223], v[192:195], a[44:47]\n"
"v_cvt_pk_bf16_f32 v165, v170, v171\n"
"v_add_f32 v251, v251, v186\n"
"v_mfma_f32_16x16x32_bf16 a[48:51], v[224:227], v[142:145], a[48:51]\n"
"v_exp_f32 v188, v188\n"
"v_mfma_f32_16x16x32_bf16 a[48:51], v[228:231], v[138:141], a[48:51]\n"
"ds_read_b128 v[216:219], v252 offset:768\n"
"v_add_f32 v251, v251, v187\n"
"v_mfma_f32_16x16x32_bf16 a[52:55], v[224:227], v[160:163], a[52:55]\n"
"v_exp_f32 v189, v189\n"
"v_mfma_f32_16x16x32_bf16 a[52:55], v[228:231], v[156:159], a[52:55]\n"
"ds_read_b128 v[220:223], v252 offset:832\n"
"v_add_f32 v251, v251, v188\n"
"v_mfma_f32_16x16x32_bf16 a[56:59], v[224:227], v[178:181], a[56:59]\n"
"v_add_f32 v251, v251, v189\n"
"v_cvt_pk_bf16_f32 v182, v186, v187\n"
"v_mfma_f32_16x16x32_bf16 a[56:59], v[228:231], v[174:177], a[56:59]\n"
"v_cvt_pk_bf16_f32 v183, v188, v189\n"
"v_mfma_f32_16x16x32_bf16 a[60:63], v[224:227], v[196:199], a[60:63]\n"
"v_mfma_f32_16x16x32_bf16 a[60:63], v[228:231], v[192:195], a[60:63]\n"
"s_waitcnt lgkmcnt(2)\n"
"v_mfma_f32_16x16x32_bf16 v[132:135], v[200:203], v[0:3], v[232:235]\n"
"ds_read_b128 v[224:227], v252 offset:896\n"
"v_mfma_f32_16x16x32_bf16 v[132:135], v[204:207], v[4:7], v[132:135]\n"
"ds_read_b128 v[228:231], v252 offset:960\n"
"v_mfma_f32_16x16x32_bf16 v[150:153], v[200:203], v[32:35], v[236:239]\n"
"s_add_u32 m0, s82, 50688\n"
"v_mfma_f32_16x16x32_bf16 v[150:153], v[204:207], v[36:39], v[150:153]\n"
"buffer_load_dwordx4 v255, s[44:47], s84 offen offset:0 lds\n"
"v_mfma_f32_16x16x32_bf16 v[168:171], v[200:203], v[64:67], v[240:243]\n"
"s_add_u32 m0, s82, 51232\n"
"v_mfma_f32_16x16x32_bf16 v[168:171], v[204:207], v[68:71], v[168:171]\n"
"buffer_load_dwordx4 v255, s[44:47], s84 offen offset:512 lds\n"
"v_mfma_f32_16x16x32_bf16 v[186:189], v[200:203], v[96:99], v[244:247]\n"
"v_mfma_f32_16x16x32_bf16 v[186:189], v[204:207], v[100:103], v[186:189]\n"
"v_mfma_f32_16x16x32_bf16 v[132:135], v[208:211], v[8:11], v[132:135]\n"
"ds_read_b64_tr_b16 v[200:201], v253 offset:128\n"
"v_mfma_f32_16x16x32_bf16 v[132:135], v[212:215], v[12:15], v[132:135]\n"
"ds_read_b64_tr_b16 v[202:203], v253 offset:640\n"
"v_mfma_f32_16x16x32_bf16 v[150:153], v[208:211], v[40:43], v[150:153]\n"
"ds_read_b64_tr_b16 v[206:207], v254 offset:128\n"
"v_mfma_f32_16x16x32_bf16 v[150:153], v[212:215], v[44:47], v[150:153]\n"
"ds_read_b64_tr_b16 v[204:205], v254 offset:640\n"
"v_mfma_f32_16x16x32_bf16 v[168:171], v[208:211], v[72:75], v[168:171]\n"
"s_add_u32 m0, s82, 51776\n"
"v_mfma_f32_16x16x32_bf16 v[168:171], v[212:215], v[76:79], v[168:171]\n"
"buffer_load_dwordx4 v255, s[44:47], s84 offen offset:1024 nt lds\n"
"v_mfma_f32_16x16x32_bf16 v[186:189], v[208:211], v[104:107], v[186:189]\n"
"s_add_u32 m0, s82, 52320\n"
"v_mfma_f32_16x16x32_bf16 v[186:189], v[212:215], v[108:111], v[186:189]\n"
"s_waitcnt lgkmcnt(4)\n"
"buffer_load_dwordx4 v255, s[44:47], s84 offen offset:1536 nt lds\n"
"v_mfma_f32_16x16x32_bf16 v[132:135], v[216:219], v[16:19], v[132:135]\n"
"ds_read_b64_tr_b16 v[208:209], v253 offset:160\n"
"v_mfma_f32_16x16x32_bf16 v[132:135], v[220:223], v[20:23], v[132:135]\n"
"ds_read_b64_tr_b16 v[210:211], v253 offset:672\n"
"v_mfma_f32_16x16x32_bf16 v[150:153], v[216:219], v[48:51], v[150:153]\n"
"ds_read_b64_tr_b16 v[214:215], v254 offset:160\n"
"v_mfma_f32_16x16x32_bf16 v[150:153], v[220:223], v[52:55], v[150:153]\n"
"ds_read_b64_tr_b16 v[212:213], v254 offset:672\n"
"v_mfma_f32_16x16x32_bf16 v[168:171], v[216:219], v[80:83], v[168:171]\n"
"v_mfma_f32_16x16x32_bf16 v[168:171], v[220:223], v[84:87], v[168:171]\n"
"v_mfma_f32_16x16x32_bf16 v[186:189], v[216:219], v[112:115], v[186:189]\n"
"v_mfma_f32_16x16x32_bf16 v[186:189], v[220:223], v[116:119], v[186:189]\n"
"v_mfma_f32_16x16x32_bf16 v[132:135], v[224:227], v[24:27], v[132:135]\n"
"s_add_u32 s84, s84, 0x4000\n"
"ds_read_b64_tr_b16 v[216:217], v253 offset:192\n"
"v_mfma_f32_16x16x32_bf16 v[132:135], v[228:231], v[28:31], v[132:135]\n"
"s_cmp_lt_u32 s84, 0x80000\n"
"ds_read_b64_tr_b16 v[218:219], v253 offset:704\n"
"v_mfma_f32_16x16x32_bf16 v[150:153], v[224:227], v[56:59], v[150:153]\n"
"s_cselect_b32 s46, 0x80000, 0\n"
"ds_read_b64_tr_b16 v[222:223], v254 offset:192\n"
"v_mfma_f32_16x16x32_bf16 v[150:153], v[228:231], v[60:63], v[150:153]\n"
"ds_read_b64_tr_b16 v[220:221], v254 offset:704\n"
"v_mfma_f32_16x16x32_bf16 v[168:171], v[224:227], v[88:91], v[168:171]\n"
"v_exp_f32 v132, v132\n"
"v_mfma_f32_16x16x32_bf16 v[168:171], v[228:231], v[92:95], v[168:171]\n"
"v_add_f32 v248, v248, v132\n"
"v_mfma_f32_16x16x32_bf16 v[186:189], v[224:227], v[120:123], v[186:189]\n"
"v_exp_f32 v133, v133\n"
"v_mfma_f32_16x16x32_bf16 v[186:189], v[228:231], v[124:127], v[186:189]\n"
"s_waitcnt lgkmcnt(4)\n"
"ds_read_b64_tr_b16 v[224:225], v253 offset:224\n"
"v_add_f32 v248, v248, v133\n"
"v_mfma_f32_16x16x32_bf16 a[64:67], v[200:203], v[142:145], a[64:67]\n"
"v_exp_f32 v134, v134\n"
"v_mfma_f32_16x16x32_bf16 a[64:67], v[204:207], v[138:141], a[64:67]\n"
"s_memrealtime vcc\n"
"ds_read_b64_tr_b16 v[226:227], v253 offset:736\n"
"v_add_f32 v248, v248, v134\n"
"v_mfma_f32_16x16x32_bf16 a[68:71], v[200:203], v[160:163], a[68:71]\n"
"v_exp_f32 v135, v135\n"
"v_mfma_f32_16x16x32_bf16 a[68:71], v[204:207], v[156:159], a[68:71]\n"
"ds_read_b64_tr_b16 v[230:231], v254 offset:224\n"
"v_add_f32 v248, v248, v135\n"
"v_mfma_f32_16x16x32_bf16 a[72:75], v[200:203], v[178:181], a[72:75]\n"
"v_exp_f32 v150, v150\n"
"v_mfma_f32_16x16x32_bf16 a[72:75], v[204:207], v[174:177], a[72:75]\n"
"ds_read_b64_tr_b16 v[228:229], v254 offset:736\n"
"v_cvt_pk_bf16_f32 v130, v132, v133\n"
"v_mfma_f32_16x16x32_bf16 a[76:79], v[200:203], v[196:199], a[76:79]\n"
"v_exp_f32 v151, v151\n"
"v_mfma_f32_16x16x32_bf16 a[76:79], v[204:207], v[192:195], a[76:79]\n"
"v_cvt_pk_bf16_f32 v131, v134, v135\n"
"v_add_f32 v249, v249, v150\n"
"v_mfma_f32_16x16x32_bf16 a[80:83], v[208:211], v[142:145], a[80:83]\n"
"v_exp_f32 v152, v152\n"
"v_mfma_f32_16x16x32_bf16 a[80:83], v[212:215], v[138:141], a[80:83]\n"
"v_add_f32 v249, v249, v151\n"
"v_add_f32 v249, v249, v152\n"
"v_mfma_f32_16x16x32_bf16 a[84:87], v[208:211], v[160:163], a[84:87]\n"
"v_exp_f32 v153, v153\n"
"v_mfma_f32_16x16x32_bf16 a[84:87], v[212:215], v[156:159], a[84:87]\n"
"v_add_f32 v249, v249, v153\n"
"v_cvt_pk_bf16_f32 v148, v150, v151\n"
"v_mfma_f32_16x16x32_bf16 a[88:91], v[208:211], v[178:181], a[88:91]\n"
"v_exp_f32 v168, v168\n"
"v_mfma_f32_16x16x32_bf16 a[88:91], v[212:215], v[174:177], a[88:91]\n"
"s_waitcnt vmcnt(19) lgkmcnt(0)\n"
"v_cvt_pk_bf16_f32 v149, v152, v153\n"
"v_add_f32 v250, v250, v168\n"
"v_mfma_f32_16x16x32_bf16 a[92:95], v[208:211], v[196:199], a[92:95]\n"
"s_barrier\n"
"v_exp_f32 v169, v169\n"
"v_mfma_f32_16x16x32_bf16 a[92:95], v[212:215], v[192:195], a[92:95]\n"
"ds_read_b128 v[200:203], v252 offset:16896\n"
"v_add_f32 v250, v250, v169\n"
"v_mfma_f32_16x16x32_bf16 a[96:99], v[216:219], v[142:145], a[96:99]\n"
"s_sub_i32 s42, s40, vcc_lo\n"
"v_exp_f32 v170, v170\n"
"v_mfma_f32_16x16x32_bf16 a[96:99], v[220:223], v[138:141], a[96:99]\n"
"s_max_i32 s42, s42, 0\n"
"ds_read_b128 v[204:207], v252 offset:16960\n"
"v_add_f32 v250, v250, v170\n"
"v_mfma_f32_16x16x32_bf16 a[100:103], v[216:219], v[160:163], a[100:103]\n"
"s_add_u32 s40, s42, vcc_lo\n"
"v_exp_f32 v171, v171\n"
"v_mfma_f32_16x16x32_bf16 a[100:103], v[220:223], v[156:159], a[100:103]\n"
"s_sub_i32 s41, s41, 15\n"
"ds_read_b128 v[208:211], v252 offset:17024\n"
"v_add_f32 v250, v250, v171\n"
"v_mfma_f32_16x16x32_bf16 a[104:107], v[216:219], v[178:181], a[104:107]\n"
"s_ashr_i32 s43, s41, 4\n"
"v_exp_f32 v186, v186\n"
"v_mfma_f32_16x16x32_bf16 a[104:107], v[220:223], v[174:177], a[104:107]\n"
"s_add_u32 s40, s40, s43\n"
"ds_read_b128 v[212:215], v252 offset:17088\n"
"v_cvt_pk_bf16_f32 v166, v168, v169\n"
"v_mfma_f32_16x16x32_bf16 a[108:111], v[216:219], v[196:199], a[108:111]\n"
"s_mul_i32 s42, s42, 23\n"
"v_exp_f32 v187, v187\n"
"v_mfma_f32_16x16x32_bf16 a[108:111], v[220:223], v[192:195], a[108:111]\n"
"v_cvt_pk_bf16_f32 v167, v170, v171\n"
"v_add_f32 v251, v251, v186\n"
"v_mfma_f32_16x16x32_bf16 a[112:115], v[224:227], v[142:145], a[112:115]\n"
"v_exp_f32 v188, v188\n"
"v_mfma_f32_16x16x32_bf16 a[112:115], v[228:231], v[138:141], a[112:115]\n"
"ds_read_b128 v[216:219], v252 offset:17152\n"
"v_add_f32 v251, v251, v187\n"
"v_mfma_f32_16x16x32_bf16 a[116:119], v[224:227], v[160:163], a[116:119]\n"
"v_exp_f32 v189, v189\n"
"v_mfma_f32_16x16x32_bf16 a[116:119], v[228:231], v[156:159], a[116:119]\n"
"ds_read_b128 v[220:223], v252 offset:17216\n"
"v_add_f32 v251, v251, v188\n"
"v_mfma_f32_16x16x32_bf16 a[120:123], v[224:227], v[178:181], a[120:123]\n"
"v_add_f32 v251, v251, v189\n"
"v_cvt_pk_bf16_f32 v184, v186, v187\n"
"v_mfma_f32_16x16x32_bf16 a[120:123], v[228:231], v[174:177], a[120:123]\n"
"v_cvt_pk_bf16_f32 v185, v188, v189\n"
"v_mfma_f32_16x16x32_bf16 a[124:127], v[224:227], v[196:199], a[124:127]\n"
"v_mfma_f32_16x16x32_bf16 a[124:127], v[228:231], v[192:195], a[124:127]\n"
"s_waitcnt lgkmcnt(2)\n"
"v_mfma_f32_16x16x32_bf16 v[132:135], v[200:203], v[0:3], v[232:235]\n"
"ds_read_b128 v[224:227], v252 offset:17280\n"
"v_mfma_f32_16x16x32_bf16 v[132:135], v[204:207], v[4:7], v[132:135]\n"
"ds_read_b128 v[228:231], v252 offset:17344\n"
"v_mfma_f32_16x16x32_bf16 v[150:153], v[200:203], v[32:35], v[236:239]\n"
"s_add_u32 m0, s83, 0\n"
"v_mfma_f32_16x16x32_bf16 v[150:153], v[204:207], v[36:39], v[150:153]\n"
"buffer_load_dwordx4 v255, s[48:51], s85 offen offset:0 nt lds\n"
"v_mfma_f32_16x16x32_bf16 v[168:171], v[200:203], v[64:67], v[240:243]\n"
"s_add_u32 m0, s83, 544\n"
"v_mfma_f32_16x16x32_bf16 v[168:171], v[204:207], v[68:71], v[168:171]\n"
"buffer_load_dwordx4 v255, s[48:51], s85 offen offset:512 nt lds\n"
"v_mfma_f32_16x16x32_bf16 v[186:189], v[200:203], v[96:99], v[244:247]\n"
"Ldll2%=:\n"
"s_sub_u32 s42, s42, 32\n"
"s_cbranch_scc0 Ldll2%=\n"
"v_mfma_f32_16x16x32_bf16 v[186:189], v[204:207], v[100:103], v[186:189]\n"
"v_mfma_f32_16x16x32_bf16 v[132:135], v[208:211], v[8:11], v[132:135]\n"
"ds_read_b64_tr_b16 v[200:201], v253 offset:256\n"
"v_mfma_f32_16x16x32_bf16 v[132:135], v[212:215], v[12:15], v[132:135]\n"
"ds_read_b64_tr_b16 v[202:203], v253 offset:768\n"
"v_mfma_f32_16x16x32_bf16 v[150:153], v[208:211], v[40:43], v[150:153]\n"
"ds_read_b64_tr_b16 v[206:207], v254 offset:256\n"
"v_mfma_f32_16x16x32_bf16 v[150:153], v[212:215], v[44:47], v[150:153]\n"
"ds_read_b64_tr_b16 v[204:205], v254 offset:768\n"
"v_mfma_f32_16x16x32_bf16 v[168:171], v[208:211], v[72:75], v[168:171]\n"
"s_add_u32 m0, s83, 1088\n"
"v_mfma_f32_16x16x32_bf16 v[168:171], v[212:215], v[76:79], v[168:171]\n"
"buffer_load_dwordx4 v255, s[48:51], s85 offen offset:1024 nt lds\n"
"v_mfma_f32_16x16x32_bf16 v[186:189], v[208:211], v[104:107], v[186:189]\n"
"s_add_u32 m0, s83, 1632\n"
"v_mfma_f32_16x16x32_bf16 v[186:189], v[212:215], v[108:111], v[186:189]\n"
"s_waitcnt lgkmcnt(4)\n"
"buffer_load_dwordx4 v255, s[48:51], s85 offen offset:1536 nt lds\n"
"v_mfma_f32_16x16x32_bf16 v[132:135], v[216:219], v[16:19], v[132:135]\n"
"ds_read_b64_tr_b16 v[208:209], v253 offset:288\n"
"v_mfma_f32_16x16x32_bf16 v[132:135], v[220:223], v[20:23], v[132:135]\n"
"ds_read_b64_tr_b16 v[210:211], v253 offset:800\n"
"v_mfma_f32_16x16x32_bf16 v[150:153], v[216:219], v[48:51], v[150:153]\n"
"ds_read_b64_tr_b16 v[214:215], v254 offset:288\n"
"v_mfma_f32_16x16x32_bf16 v[150:153], v[220:223], v[52:55], v[150:153]\n"
"ds_read_b64_tr_b16 v[212:213], v254 offset:800\n"
"v_mfma_f32_16x16x32_bf16 v[168:171], v[216:219], v[80:83], v[168:171]\n"
"v_mfma_f32_16x16x32_bf16 v[168:171], v[220:223], v[84:87], v[168:171]\n"
"v_mfma_f32_16x16x32_bf16 v[186:189], v[216:219], v[112:115], v[186:189]\n"
"v_mfma_f32_16x16x32_bf16 v[186:189], v[220:223], v[116:119], v[186:189]\n"
"v_mfma_f32_16x16x32_bf16 v[132:135], v[224:227], v[24:27], v[132:135]\n"
"s_add_u32 s85, s85, 0x4000\n"
"ds_read_b64_tr_b16 v[216:217], v253 offset:320\n"
"v_mfma_f32_16x16x32_bf16 v[132:135], v[228:231], v[28:31], v[132:135]\n"
"s_cmp_lt_u32 s85, 0x80000\n"
"ds_read_b64_tr_b16 v[218:219], v253 offset:832\n"
"v_mfma_f32_16x16x32_bf16 v[150:153], v[224:227], v[56:59], v[150:153]\n"
"s_cselect_b32 s50, 0x80000, 0\n"
"ds_read_b64_tr_b16 v[222:223], v254 offset:320\n"
"v_mfma_f32_16x16x32_bf16 v[150:153], v[228:231], v[60:63], v[150:153]\n"
"s_add_u32 s83, s83, 16896\n"
"ds_read_b64_tr_b16 v[220:221], v254 offset:832\n"
"v_mfma_f32_16x16x32_bf16 v[168:171], v[224:227], v[88:91], v[168:171]\n"
"s_cmp_ge_u32 s83, s91\n"
"v_exp_f32 v132, v132\n"
"v_mfma_f32_16x16x32_bf16 v[168:171], v[228:231], v[92:95], v[168:171]\n"
"s_cselect_b32 s87, 84480, 0\n"
"v_add_f32 v248, v248, v132\n"
"v_mfma_f32_16x16x32_bf16 v[186:189], v[224:227], v[120:123], v[186:189]\n"
"s_sub_u32 s83, s83, s87\n"
"v_exp_f32 v133, v133\n"
"v_mfma_f32_16x16x32_bf16 v[186:189], v[228:231], v[124:127], v[186:189]\n"
"s_waitcnt lgkmcnt(4)\n"
"ds_read_b64_tr_b16 v[224:225], v253 offset:352\n"
"v_add_f32 v248, v248, v133\n"
"v_mfma_f32_16x16x32_bf16 a[128:131], v[200:203], v[142:145], a[128:131]\n"
"v_exp_f32 v134, v134\n"
"v_mfma_f32_16x16x32_bf16 a[128:131], v[204:207], v[138:141], a[128:131]\n"
"ds_read_b64_tr_b16 v[226:227], v253 offset:864\n"
"v_add_f32 v248, v248, v134\n"
"v_mfma_f32_16x16x32_bf16 a[132:135], v[200:203], v[160:163], a[132:135]\n"
"v_exp_f32 v135, v135\n"
"v_mfma_f32_16x16x32_bf16 a[132:135], v[204:207], v[156:159], a[132:135]\n"
"ds_read_b64_tr_b16 v[230:231], v254 offset:352\n"
"v_add_f32 v248, v248, v135\n"
"v_mfma_f32_16x16x32_bf16 a[136:139], v[200:203], v[178:181], a[136:139]\n"
"v_exp_f32 v150, v150\n"
"v_mfma_f32_16x16x32_bf16 a[136:139], v[204:207], v[174:177], a[136:139]\n"
"ds_read_b64_tr_b16 v[228:229], v254 offset:864\n"
"v_cvt_pk_bf16_f32 v132, v132, v133\n"
"v_mfma_f32_16x16x32_bf16 a[140:143], v[200:203], v[196:199], a[140:143]\n"
"v_exp_f32 v151, v151\n"
"v_mfma_f32_16x16x32_bf16 a[140:143], v[204:207], v[192:195], a[140:143]\n"
"v_cvt_pk_bf16_f32 v133, v134, v135\n"
"v_add_f32 v249, v249, v150\n"
"v_mfma_f32_16x16x32_bf16 a[144:147], v[208:211], v[142:145], a[144:147]\n"
"v_exp_f32 v152, v152\n"
"v_mfma_f32_16x16x32_bf16 a[144:147], v[212:215], v[138:141], a[144:147]\n"
"v_add_f32 v249, v249, v151\n"
"v_add_f32 v249, v249, v152\n"
"v_mfma_f32_16x16x32_bf16 a[148:151], v[208:211], v[160:163], a[148:151]\n"
"v_exp_f32 v153, v153\n"
"v_mfma_f32_16x16x32_bf16 a[148:151], v[212:215], v[156:159], a[148:151]\n"
"v_add_f32 v249, v249, v153\n"
"v_cvt_pk_bf16_f32 v150, v150, v151\n"
"v_mfma_f32_16x16x32_bf16 a[152:155], v[208:211], v[178:181], a[152:155]\n"
"v_exp_f32 v168, v168\n"
"v_mfma_f32_16x16x32_bf16 a[152:155], v[212:215], v[174:177], a[152:155]\n"
"v_cvt_pk_bf16_f32 v151, v152, v153\n"
"v_add_f32 v250, v250, v168\n"
"v_mfma_f32_16x16x32_bf16 a[156:159], v[208:211], v[196:199], a[156:159]\n"
"v_exp_f32 v169, v169\n"
"v_mfma_f32_16x16x32_bf16 a[156:159], v[212:215], v[192:195], a[156:159]\n"
"s_waitcnt lgkmcnt(0)\n"
"ds_read_b128 v[200:203], v252 offset:17408\n"
"v_add_f32 v250, v250, v169\n"
"v_mfma_f32_16x16x32_bf16 a[160:163], v[216:219], v[142:145], a[160:163]\n"
"v_exp_f32 v170, v170\n"
"v_mfma_f32_16x16x32_bf16 a[160:163], v[220:223], v[138:141], a[160:163]\n"
"ds_read_b128 v[204:207], v252 offset:17472\n"
"v_add_f32 v250, v250, v170\n"
"v_mfma_f32_16x16x32_bf16 a[164:167], v[216:219], v[160:163], a[164:167]\n"
"v_exp_f32 v171, v171\n"
"v_mfma_f32_16x16x32_bf16 a[164:167], v[220:223], v[156:159], a[164:167]\n"
"ds_read_b128 v[208:211], v252 offset:17536\n"
"v_add_f32 v250, v250, v171\n"
"v_mfma_f32_16x16x32_bf16 a[168:171], v[216:219], v[178:181], a[168:171]\n"
"v_exp_f32 v186, v186\n"
"v_mfma_f32_16x16x32_bf16 a[168:171], v[220:223], v[174:177], a[168:171]\n"
"ds_read_b128 v[212:215], v252 offset:17600\n"
"v_cvt_pk_bf16_f32 v168, v168, v169\n"
"v_mfma_f32_16x16x32_bf16 a[172:175], v[216:219], v[196:199], a[172:175]\n"
"v_exp_f32 v187, v187\n"
"v_mfma_f32_16x16x32_bf16 a[172:175], v[220:223], v[192:195], a[172:175]\n"
"v_cvt_pk_bf16_f32 v169, v170, v171\n"
"v_add_f32 v251, v251, v186\n"
"v_mfma_f32_16x16x32_bf16 a[176:179], v[224:227], v[142:145], a[176:179]\n"
"v_exp_f32 v188, v188\n"
"v_mfma_f32_16x16x32_bf16 a[176:179], v[228:231], v[138:141], a[176:179]\n"
"ds_read_b128 v[216:219], v252 offset:17664\n"
"v_add_f32 v251, v251, v187\n"
"v_mfma_f32_16x16x32_bf16 a[180:183], v[224:227], v[160:163], a[180:183]\n"
"v_exp_f32 v189, v189\n"
"v_mfma_f32_16x16x32_bf16 a[180:183], v[228:231], v[156:159], a[180:183]\n"
"ds_read_b128 v[220:223], v252 offset:17728\n"
"v_add_f32 v251, v251, v188\n"
"v_mfma_f32_16x16x32_bf16 a[184:187], v[224:227], v[178:181], a[184:187]\n"
"v_add_f32 v251, v251, v189\n"
"v_cvt_pk_bf16_f32 v186, v186, v187\n"
"v_mfma_f32_16x16x32_bf16 a[184:187], v[228:231], v[174:177], a[184:187]\n"
"v_cvt_pk_bf16_f32 v187, v188, v189\n"
"v_mfma_f32_16x16x32_bf16 a[188:191], v[224:227], v[196:199], a[188:191]\n"
"v_mfma_f32_16x16x32_bf16 a[188:191], v[228:231], v[192:195], a[188:191]\n"
"s_waitcnt lgkmcnt(2)\n"
"v_mfma_f32_16x16x32_bf16 v[134:137], v[200:203], v[0:3], v[232:235]\n"
"ds_read_b128 v[224:227], v252 offset:17792\n"
"v_mfma_f32_16x16x32_bf16 v[134:137], v[204:207], v[4:7], v[134:137]\n"
"ds_read_b128 v[228:231], v252 offset:17856\n"
"v_mfma_f32_16x16x32_bf16 v[152:155], v[200:203], v[32:35], v[236:239]\n"
"s_add_u32 m0, s82, 0\n"
"v_mfma_f32_16x16x32_bf16 v[152:155], v[204:207], v[36:39], v[152:155]\n"
"buffer_load_dwordx4 v255, s[44:47], s84 offen offset:0 lds\n"
"v_mfma_f32_16x16x32_bf16 v[170:173], v[200:203], v[64:67], v[240:243]\n"
"s_add_u32 m0, s82, 544\n"
"v_mfma_f32_16x16x32_bf16 v[170:173], v[204:207], v[68:71], v[170:173]\n"
"buffer_load_dwordx4 v255, s[44:47], s84 offen offset:512 lds\n"
"v_mfma_f32_16x16x32_bf16 v[188:191], v[200:203], v[96:99], v[244:247]\n"
"v_mfma_f32_16x16x32_bf16 v[188:191], v[204:207], v[100:103], v[188:191]\n"
"v_mfma_f32_16x16x32_bf16 v[134:137], v[208:211], v[8:11], v[134:137]\n"
"ds_read_b64_tr_b16 v[200:201], v253 offset:384\n"
"v_mfma_f32_16x16x32_bf16 v[134:137], v[212:215], v[12:15], v[134:137]\n"
"ds_read_b64_tr_b16 v[202:203], v253 offset:896\n"
"v_mfma_f32_16x16x32_bf16 v[152:155], v[208:211], v[40:43], v[152:155]\n"
"ds_read_b64_tr_b16 v[206:207], v254 offset:384\n"
"v_mfma_f32_16x16x32_bf16 v[152:155], v[212:215], v[44:47], v[152:155]\n"
"ds_read_b64_tr_b16 v[204:205], v254 offset:896\n"
"v_mfma_f32_16x16x32_bf16 v[170:173], v[208:211], v[72:75], v[170:173]\n"
"s_add_u32 m0, s82, 1088\n"
"v_mfma_f32_16x16x32_bf16 v[170:173], v[212:215], v[76:79], v[170:173]\n"
"buffer_load_dwordx4 v255, s[44:47], s84 offen offset:1024 nt lds\n"
"v_mfma_f32_16x16x32_bf16 v[188:191], v[208:211], v[104:107], v[188:191]\n"
"s_add_u32 m0, s82, 1632\n"
"v_mfma_f32_16x16x32_bf16 v[188:191], v[212:215], v[108:111], v[188:191]\n"
"s_waitcnt lgkmcnt(4)\n"
"buffer_load_dwordx4 v255, s[44:47], s84 offen offset:1536 nt lds\n"
"v_mfma_f32_16x16x32_bf16 v[134:137], v[216:219], v[16:19], v[134:137]\n"
"ds_read_b64_tr_b16 v[208:209], v253 offset:416\n"
"v_mfma_f32_16x16x32_bf16 v[134:137], v[220:223], v[20:23], v[134:137]\n"
"ds_read_b64_tr_b16 v[210:211], v253 offset:928\n"
"v_mfma_f32_16x16x32_bf16 v[152:155], v[216:219], v[48:51], v[152:155]\n"
"ds_read_b64_tr_b16 v[214:215], v254 offset:416\n"
"v_mfma_f32_16x16x32_bf16 v[152:155], v[220:223], v[52:55], v[152:155]\n"
"ds_read_b64_tr_b16 v[212:213], v254 offset:928\n"
"v_mfma_f32_16x16x32_bf16 v[170:173], v[216:219], v[80:83], v[170:173]\n"
"v_mfma_f32_16x16x32_bf16 v[170:173], v[220:223], v[84:87], v[170:173]\n"
"v_mfma_f32_16x16x32_bf16 v[188:191], v[216:219], v[112:115], v[188:191]\n"
"v_mfma_f32_16x16x32_bf16 v[188:191], v[220:223], v[116:119], v[188:191]\n"
"v_mfma_f32_16x16x32_bf16 v[134:137], v[224:227], v[24:27], v[134:137]\n"
"s_add_u32 s84, s84, 0x4000\n"
"ds_read_b64_tr_b16 v[216:217], v253 offset:448\n"
"v_mfma_f32_16x16x32_bf16 v[134:137], v[228:231], v[28:31], v[134:137]\n"
"s_cmp_lt_u32 s84, 0x80000\n"
"ds_read_b64_tr_b16 v[218:219], v253 offset:960\n"
"v_mfma_f32_16x16x32_bf16 v[152:155], v[224:227], v[56:59], v[152:155]\n"
"s_cselect_b32 s46, 0x80000, 0\n"
"ds_read_b64_tr_b16 v[222:223], v254 offset:448\n"
"v_mfma_f32_16x16x32_bf16 v[152:155], v[228:231], v[60:63], v[152:155]\n"
"ds_read_b64_tr_b16 v[220:221], v254 offset:960\n"
"v_mfma_f32_16x16x32_bf16 v[170:173], v[224:227], v[88:91], v[170:173]\n"
"v_exp_f32 v134, v134\n"
"v_mfma_f32_16x16x32_bf16 v[170:173], v[228:231], v[92:95], v[170:173]\n"
"v_add_f32 v248, v248, v134\n"
"v_mfma_f32_16x16x32_bf16 v[188:191], v[224:227], v[120:123], v[188:191]\n"
"v_exp_f32 v135, v135\n"
"v_mfma_f32_16x16x32_bf16 v[188:191], v[228:231], v[124:127], v[188:191]\n"
"s_waitcnt lgkmcnt(4)\n"
"ds_read_b64_tr_b16 v[224:225], v253 offset:480\n"
"v_add_f32 v248, v248, v135\n"
"v_mfma_f32_16x16x32_bf16 a[192:195], v[200:203], v[142:145], a[192:195]\n"
"v_exp_f32 v136, v136\n"
"v_mfma_f32_16x16x32_bf16 a[192:195], v[204:207], v[138:141], a[192:195]\n"
"s_memrealtime vcc\n"
"ds_read_b64_tr_b16 v[226:227], v253 offset:992\n"
"v_add_f32 v248, v248, v136\n"
"v_mfma_f32_16x16x32_bf16 a[196:199], v[200:203], v[160:163], a[196:199]\n"
"v_exp_f32 v137, v137\n"
"v_mfma_f32_16x16x32_bf16 a[196:199], v[204:207], v[156:159], a[196:199]\n"
"ds_read_b64_tr_b16 v[230:231], v254 offset:480\n"
"v_add_f32 v248, v248, v137\n"
"v_mfma_f32_16x16x32_bf16 a[200:203], v[200:203], v[178:181], a[200:203]\n"
"v_exp_f32 v152, v152\n"
"v_mfma_f32_16x16x32_bf16 a[200:203], v[204:207], v[174:177], a[200:203]\n"
"ds_read_b64_tr_b16 v[228:229], v254 offset:992\n"
"v_cvt_pk_bf16_f32 v134, v134, v135\n"
"v_mfma_f32_16x16x32_bf16 a[204:207], v[200:203], v[196:199], a[204:207]\n"
"v_exp_f32 v153, v153\n"
"v_mfma_f32_16x16x32_bf16 a[204:207], v[204:207], v[192:195], a[204:207]\n"
"v_cvt_pk_bf16_f32 v135, v136, v137\n"
"v_add_f32 v249, v249, v152\n"
"v_mfma_f32_16x16x32_bf16 a[208:211], v[208:211], v[142:145], a[208:211]\n"
"v_exp_f32 v154, v154\n"
"v_mfma_f32_16x16x32_bf16 a[208:211], v[212:215], v[138:141], a[208:211]\n"
"v_add_f32 v249, v249, v153\n"
"v_add_f32 v249, v249, v154\n"
"v_mfma_f32_16x16x32_bf16 a[212:215], v[208:211], v[160:163], a[212:215]\n"
"v_exp_f32 v155, v155\n"
"v_mfma_f32_16x16x32_bf16 a[212:215], v[212:215], v[156:159], a[212:215]\n"
"v_add_f32 v249, v249, v155\n"
"v_cvt_pk_bf16_f32 v152, v152, v153\n"
"v_mfma_f32_16x16x32_bf16 a[216:219], v[208:211], v[178:181], a[216:219]\n"
"v_exp_f32 v170, v170\n"
"v_mfma_f32_16x16x32_bf16 a[216:219], v[212:215], v[174:177], a[216:219]\n"
"s_waitcnt vmcnt(12) lgkmcnt(0)\n"
"v_cvt_pk_bf16_f32 v153, v154, v155\n"
"v_add_f32 v250, v250, v170\n"
"v_mfma_f32_16x16x32_bf16 a[220:223], v[208:211], v[196:199], a[220:223]\n"
"s_barrier\n"
"v_exp_f32 v171, v171\n"
"v_mfma_f32_16x16x32_bf16 a[220:223], v[212:215], v[192:195], a[220:223]\n"
"ds_read_b128 v[200:203], v252 offset:33792\n"
"v_add_f32 v250, v250, v171\n"
"v_mfma_f32_16x16x32_bf16 a[224:227], v[216:219], v[142:145], a[224:227]\n"
"s_sub_i32 s42, s40, vcc_lo\n"
"v_exp_f32 v172, v172\n"
"v_mfma_f32_16x16x32_bf16 a[224:227], v[220:223], v[138:141], a[224:227]\n"
"s_max_i32 s42, s42, 0\n"
"ds_read_b128 v[204:207], v252 offset:33856\n"
"v_add_f32 v250, v250, v172\n"
"v_mfma_f32_16x16x32_bf16 a[228:231], v[216:219], v[160:163], a[228:231]\n"
"s_add_u32 s40, s42, vcc_lo\n"
"v_exp_f32 v173, v173\n"
"v_mfma_f32_16x16x32_bf16 a[228:231], v[220:223], v[156:159], a[228:231]\n"
"s_sub_i32 s41, s41, 15\n"
"ds_read_b128 v[208:211], v252 offset:33920\n"
"v_add_f32 v250, v250, v173\n"
"v_mfma_f32_16x16x32_bf16 a[232:235], v[216:219], v[178:181], a[232:235]\n"
"s_ashr_i32 s43, s41, 4\n"
"v_exp_f32 v188, v188\n"
"v_mfma_f32_16x16x32_bf16 a[232:235], v[220:223], v[174:177], a[232:235]\n"
"s_add_u32 s40, s40, s43\n"
"ds_read_b128 v[212:215], v252 offset:33984\n"
"v_cvt_pk_bf16_f32 v170, v170, v171\n"
"v_mfma_f32_16x16x32_bf16 a[236:239], v[216:219], v[196:199], a[236:239]\n"
"s_mul_i32 s42, s42, 23\n"
"v_exp_f32 v189, v189\n"
"v_mfma_f32_16x16x32_bf16 a[236:239], v[220:223], v[192:195], a[236:239]\n"
"v_cvt_pk_bf16_f32 v171, v172, v173\n"
"v_add_f32 v251, v251, v188\n"
"v_mfma_f32_16x16x32_bf16 a[240:243], v[224:227], v[142:145], a[240:243]\n"
"v_exp_f32 v190, v190\n"
"v_mfma_f32_16x16x32_bf16 a[240:243], v[228:231], v[138:141], a[240:243]\n"
"ds_read_b128 v[216:219], v252 offset:34048\n"
"v_add_f32 v251, v251, v189\n"
"v_mfma_f32_16x16x32_bf16 a[244:247], v[224:227], v[160:163], a[244:247]\n"
"v_exp_f32 v191, v191\n"
"v_mfma_f32_16x16x32_bf16 a[244:247], v[228:231], v[156:159], a[244:247]\n"
"ds_read_b128 v[220:223], v252 offset:34112\n"
"v_add_f32 v251, v251, v190\n"
"v_mfma_f32_16x16x32_bf16 a[248:251], v[224:227], v[178:181], a[248:251]\n"
"v_add_f32 v251, v251, v191\n"
"v_cvt_pk_bf16_f32 v188, v188, v189\n"
"v_mfma_f32_16x16x32_bf16 a[248:251], v[228:231], v[174:177], a[248:251]\n"
"v_cvt_pk_bf16_f32 v189, v190, v191\n"
"v_mfma_f32_16x16x32_bf16 a[252:255], v[224:227], v[196:199], a[252:255]\n"
"v_mfma_f32_16x16x32_bf16 a[252:255], v[228:231], v[192:195], a[252:255]\n"
"Lloopb%=:\n"
"s_waitcnt lgkmcnt(5)\n"
"v_mfma_f32_16x16x32_bf16 v[138:141], v[200:203], v[0:3], v[232:235]\n"
"s_waitcnt lgkmcnt(4)\n"
"s_cmp_ge_u32 s89, 50688\n"
"ds_read_b128 v[224:227], v252 offset:34176\n"
"v_mfma_f32_16x16x32_bf16 v[138:141], v[204:207], v[4:7], v[138:141]\n"
"s_cselect_b32 s86, s93, s92\n"
"ds_read_b128 v[228:231], v252 offset:34240\n"
"v_mfma_f32_16x16x32_bf16 v[156:159], v[200:203], v[32:35], v[236:239]\n"
"s_add_u32 s89, s89, s86\n"
"v_add_u32 v253, s86, v253\n"
"v_mfma_f32_16x16x32_bf16 v[156:159], v[204:207], v[36:39], v[156:159]\n"
"s_cmp_ge_u32 s90, 50688\n"
"v_mfma_f32_16x16x32_bf16 v[174:177], v[200:203], v[64:67], v[240:243]\n"
"s_cselect_b32 s86, s93, s92\n"
"v_mfma_f32_16x16x32_bf16 v[174:177], v[204:207], v[68:71], v[174:177]\n"
"s_add_u32 s90, s90, s86\n"
"v_add_u32 v254, s86, v254\n"
"v_mfma_f32_16x16x32_bf16 v[192:195], v[200:203], v[96:99], v[244:247]\n"
"Ldll4%=:\n"
"s_sub_u32 s42, s42, 32\n"
"s_cbranch_scc0 Ldll4%=\n"
"v_mfma_f32_16x16x32_bf16 v[192:195], v[204:207], v[100:103], v[192:195]\n"
"s_waitcnt lgkmcnt(5)\n"
"v_mfma_f32_16x16x32_bf16 v[138:141], v[208:211], v[8:11], v[138:141]\n"
"s_waitcnt lgkmcnt(4)\n"
"ds_read_b64_tr_b16 v[200:201], v253 offset:0\n"
"v_mfma_f32_16x16x32_bf16 v[138:141], v[212:215], v[12:15], v[138:141]\n"
"ds_read_b64_tr_b16 v[202:203], v253 offset:512\n"
"v_mfma_f32_16x16x32_bf16 v[156:159], v[208:211], v[40:43], v[156:159]\n"
"ds_read_b64_tr_b16 v[204:205], v254 offset:0\n"
"v_mfma_f32_16x16x32_bf16 v[156:159], v[212:215], v[44:47], v[156:159]\n"
"ds_read_b64_tr_b16 v[206:207], v254 offset:512\n"
"v_mfma_f32_16x16x32_bf16 v[174:177], v[208:211], v[72:75], v[174:177]\n"
"s_add_u32 m0, s83, 0\n"
"v_mfma_f32_16x16x32_bf16 v[174:177], v[212:215], v[76:79], v[174:177]\n"
"buffer_load_dwordx4 v255, s[48:51], s85 offen offset:0 nt lds\n"
"v_mfma_f32_16x16x32_bf16 v[192:195], v[208:211], v[104:107], v[192:195]\n"
"s_add_u32 m0, s83, 544\n"
"v_mfma_f32_16x16x32_bf16 v[192:195], v[212:215], v[108:111], v[192:195]\n"
"s_waitcnt lgkmcnt(4)\n"
"buffer_load_dwordx4 v255, s[48:51], s85 offen offset:512 nt lds\n"
"v_mfma_f32_16x16x32_bf16 v[138:141], v[216:219], v[16:19], v[138:141]\n"
"ds_read_b64_tr_b16 v[208:209], v253 offset:32\n"
"v_mfma_f32_16x16x32_bf16 v[138:141], v[220:223], v[20:23], v[138:141]\n"
"ds_read_b64_tr_b16 v[210:211], v253 offset:544\n"
"v_mfma_f32_16x16x32_bf16 v[156:159], v[216:219], v[48:51], v[156:159]\n"
"ds_read_b64_tr_b16 v[212:213], v254 offset:32\n"
"v_mfma_f32_16x16x32_bf16 v[156:159], v[220:223], v[52:55], v[156:159]\n"
"ds_read_b64_tr_b16 v[214:215], v254 offset:544\n"
"v_mfma_f32_16x16x32_bf16 v[174:177], v[216:219], v[80:83], v[174:177]\n"
"s_add_u32 m0, s83, 1088\n"
"v_mfma_f32_16x16x32_bf16 v[174:177], v[220:223], v[84:87], v[174:177]\n"
"buffer_load_dwordx4 v255, s[48:51], s85 offen offset:1024 nt lds\n"
"v_mfma_f32_16x16x32_bf16 v[192:195], v[216:219], v[112:115], v[192:195]\n"
"s_add_u32 m0, s83, 1632\n"
"v_mfma_f32_16x16x32_bf16 v[192:195], v[220:223], v[116:119], v[192:195]\n"
"buffer_load_dwordx4 v255, s[48:51], s85 offen offset:1536 nt lds\n"
"v_mfma_f32_16x16x32_bf16 v[138:141], v[224:227], v[24:27], v[138:141]\n"
"s_add_u32 s85, s85, 0x4000\n"
"ds_read_b64_tr_b16 v[216:217], v253 offset:64\n"
"v_mfma_f32_16x16x32_bf16 v[138:141], v[228:231], v[28:31], v[138:141]\n"
"s_cmp_lt_u32 s85, 0x80000\n"
"ds_read_b64_tr_b16 v[218:219], v253 offset:576\n"
"v_mfma_f32_16x16x32_bf16 v[156:159], v[224:227], v[56:59], v[156:159]\n"
"s_cselect_b32 s50, 0x80000, 0\n"
"ds_read_b64_tr_b16 v[220:221], v254 offset:64\n"
"v_mfma_f32_16x16x32_bf16 v[156:159], v[228:231], v[60:63], v[156:159]\n"
"s_add_u32 s83, s83, 16896\n"
"ds_read_b64_tr_b16 v[222:223], v254 offset:576\n"
"v_mfma_f32_16x16x32_bf16 v[174:177], v[224:227], v[88:91], v[174:177]\n"
"s_cmp_ge_u32 s83, s91\n"
"v_exp_f32 v138, v138\n"
"v_mfma_f32_16x16x32_bf16 v[174:177], v[228:231], v[92:95], v[174:177]\n"
"s_cselect_b32 s87, 84480, 0\n"
"v_add_f32 v248, v248, v138\n"
"v_mfma_f32_16x16x32_bf16 v[192:195], v[224:227], v[120:123], v[192:195]\n"
"s_sub_u32 s83, s83, s87\n"
"v_exp_f32 v139, v139\n"
"v_mfma_f32_16x16x32_bf16 v[192:195], v[228:231], v[124:127], v[192:195]\n"
"s_waitcnt lgkmcnt(4)\n"
"ds_read_b64_tr_b16 v[224:225], v253 offset:96\n"
"v_add_f32 v248, v248, v139\n"
"v_mfma_f32_16x16x32_bf16 a[0:3], v[200:203], v[128:131], a[0:3]\n"
"v_exp_f32 v140, v140\n"
"v_mfma_f32_16x16x32_bf16 a[0:3], v[204:207], v[132:135], a[0:3]\n"
"ds_read_b64_tr_b16 v[226:227], v253 offset:608\n"
"v_add_f32 v248, v248, v140\n"
"v_mfma_f32_16x16x32_bf16 a[4:7], v[200:203], v[146:149], a[4:7]\n"
"v_exp_f32 v141, v141\n"
"v_mfma_f32_16x16x32_bf16 a[4:7], v[204:207], v[150:153], a[4:7]\n"
"ds_read_b64_tr_b16 v[228:229], v254 offset:96\n"
"v_add_f32 v248, v248, v141\n"
"v_mfma_f32_16x16x32_bf16 a[8:11], v[200:203], v[164:167], a[8:11]\n"
"v_exp_f32 v156, v156\n"
"v_mfma_f32_16x16x32_bf16 a[8:11], v[204:207], v[168:171], a[8:11]\n"
"ds_read_b64_tr_b16 v[230:231], v254 offset:608\n"
"v_cvt_pk_bf16_f32 v142, v138, v139\n"
"v_mfma_f32_16x16x32_bf16 a[12:15], v[200:203], v[182:185], a[12:15]\n"
"v_exp_f32 v157, v157\n"
"v_mfma_f32_16x16x32_bf16 a[12:15], v[204:207], v[186:189], a[12:15]\n"
"v_cvt_pk_bf16_f32 v143, v140, v141\n"
"v_add_f32 v249, v249, v156\n"
"v_mfma_f32_16x16x32_bf16 a[16:19], v[208:211], v[128:131], a[16:19]\n"
"v_exp_f32 v158, v158\n"
"v_mfma_f32_16x16x32_bf16 a[16:19], v[212:215], v[132:135], a[16:19]\n"
"v_add_f32 v249, v249, v157\n"
"v_add_f32 v249, v249, v158\n"
"v_mfma_f32_16x16x32_bf16 a[20:23], v[208:211], v[146:149], a[20:23]\n"
"v_exp_f32 v159, v159\n"
"v_mfma_f32_16x16x32_bf16 a[20:23], v[212:215], v[150:153], a[20:23]\n"
"v_add_f32 v249, v249, v159\n"
"v_cvt_pk_bf16_f32 v160, v156, v157\n"
"v_mfma_f32_16x16x32_bf16 a[24:27], v[208:211], v[164:167], a[24:27]\n"
"v_exp_f32 v174, v174\n"
"v_mfma_f32_16x16x32_bf16 a[24:27], v[212:215], v[168:171], a[24:27]\n"
"v_cvt_pk_bf16_f32 v161, v158, v159\n"
"v_add_f32 v250, v250, v174\n"
"v_mfma_f32_16x16x32_bf16 a[28:31], v[208:211], v[182:185], a[28:31]\n"
"v_exp_f32 v175, v175\n"
"v_mfma_f32_16x16x32_bf16 a[28:31], v[212:215], v[186:189], a[28:31]\n"
"s_waitcnt lgkmcnt(0)\n"
"ds_read_b128 v[200:203], v252 offset:34304\n"
"v_add_f32 v250, v250, v175\n"
"v_mfma_f32_16x16x32_bf16 a[32:35], v[216:219], v[128:131], a[32:35]\n"
"v_exp_f32 v176, v176\n"
"v_mfma_f32_16x16x32_bf16 a[32:35], v[220:223], v[132:135], a[32:35]\n"
"ds_read_b128 v[204:207], v252 offset:34368\n"
"v_add_f32 v250, v250, v176\n"
"v_mfma_f32_16x16x32_bf16 a[36:39], v[216:219], v[146:149], a[36:39]\n"
"v_exp_f32 v177, v177\n"
"v_mfma_f32_16x16x32_bf16 a[36:39], v[220:223], v[150:153], a[36:39]\n"
"ds_read_b128 v[208:211], v252 offset:34432\n"
"v_add_f32 v250, v250, v177\n"
"v_mfma_f32_16x16x32_bf16 a[40:43], v[216:219], v[164:167], a[40:43]\n"
"v_exp_f32 v192, v192\n"
"v_mfma_f32_16x16x32_bf16 a[40:43], v[220:223], v[168:171], a[40:43]\n"
"ds_read_b128 v[212:215], v252 offset:34496\n"
"v_cvt_pk_bf16_f32 v178, v174, v175\n"
"v_mfma_f32_16x16x32_bf16 a[44:47], v[216:219], v[182:185], a[44:47]\n"
"v_exp_f32 v193, v193\n"
"v_mfma_f32_16x16x32_bf16 a[44:47], v[220:223], v[186:189], a[44:47]\n"
"v_cvt_pk_bf16_f32 v179, v176, v177\n"
"v_add_f32 v251, v251, v192\n"
"v_mfma_f32_16x16x32_bf16 a[48:51], v[224:227], v[128:131], a[48:51]\n"
"v_exp_f32 v194, v194\n"
"v_mfma_f32_16x16x32_bf16 a[48:51], v[228:231], v[132:135], a[48:51]\n"
"ds_read_b128 v[216:219], v252 offset:34560\n"
"v_add_f32 v251, v251, v193\n"
"v_mfma_f32_16x16x32_bf16 a[52:55], v[224:227], v[146:149], a[52:55]\n"
"v_exp_f32 v195, v195\n"
"v_mfma_f32_16x16x32_bf16 a[52:55], v[228:231], v[150:153], a[52:55]\n"
"ds_read_b128 v[220:223], v252 offset:34624\n"
"v_add_f32 v251, v251, v194\n"
"v_mfma_f32_16x16x32_bf16 a[56:59], v[224:227], v[164:167], a[56:59]\n"
"v_add_f32 v251, v251, v195\n"
"v_cvt_pk_bf16_f32 v196, v192, v193\n"
"v_mfma_f32_16x16x32_bf16 a[56:59], v[228:231], v[168:171], a[56:59]\n"
"v_cvt_pk_bf16_f32 v197, v194, v195\n"
"v_mfma_f32_16x16x32_bf16 a[60:63], v[224:227], v[182:185], a[60:63]\n"
"v_mfma_f32_16x16x32_bf16 a[60:63], v[228:231], v[186:189], a[60:63]\n"
"s_waitcnt lgkmcnt(2)\n"
"v_mfma_f32_16x16x32_bf16 v[138:141], v[200:203], v[0:3], v[232:235]\n"
"ds_read_b128 v[224:227], v252 offset:34688\n"
"v_mfma_f32_16x16x32_bf16 v[138:141], v[204:207], v[4:7], v[138:141]\n"
"ds_read_b128 v[228:231], v252 offset:34752\n"
"v_mfma_f32_16x16x32_bf16 v[156:159], v[200:203], v[32:35], v[236:239]\n"
"s_add_u32 m0, s82, 16896\n"
"v_mfma_f32_16x16x32_bf16 v[156:159], v[204:207], v[36:39], v[156:159]\n"
"buffer_load_dwordx4 v255, s[44:47], s84 offen offset:0 lds\n"
"v_mfma_f32_16x16x32_bf16 v[174:177], v[200:203], v[64:67], v[240:243]\n"
"s_add_u32 m0, s82, 17440\n"
"v_mfma_f32_16x16x32_bf16 v[174:177], v[204:207], v[68:71], v[174:177]\n"
"buffer_load_dwordx4 v255, s[44:47], s84 offen offset:512 lds\n"
"v_mfma_f32_16x16x32_bf16 v[192:195], v[200:203], v[96:99], v[244:247]\n"
"v_mfma_f32_16x16x32_bf16 v[192:195], v[204:207], v[100:103], v[192:195]\n"
"v_mfma_f32_16x16x32_bf16 v[138:141], v[208:211], v[8:11], v[138:141]\n"
"s_cmp_eq_u32 s81, 1\n"
"ds_read_b64_tr_b16 v[200:201], v253 offset:128\n"
"v_mfma_f32_16x16x32_bf16 v[138:141], v[212:215], v[12:15], v[138:141]\n"
"s_cselect_b32 s38, 0x80000, 0\n"
"ds_read_b64_tr_b16 v[202:203], v253 offset:640\n"
"v_mfma_f32_16x16x32_bf16 v[156:159], v[208:211], v[40:43], v[156:159]\n"
"ds_read_b64_tr_b16 v[204:205], v254 offset:128\n"
"v_mfma_f32_16x16x32_bf16 v[156:159], v[212:215], v[44:47], v[156:159]\n"
"ds_read_b64_tr_b16 v[206:207], v254 offset:640\n"
"v_mfma_f32_16x16x32_bf16 v[174:177], v[208:211], v[72:75], v[174:177]\n"
"s_add_u32 m0, s82, 17984\n"
"v_mfma_f32_16x16x32_bf16 v[174:177], v[212:215], v[76:79], v[174:177]\n"
"buffer_load_dwordx4 v255, s[44:47], s84 offen offset:1024 nt lds\n"
"v_mfma_f32_16x16x32_bf16 v[192:195], v[208:211], v[104:107], v[192:195]\n"
"s_add_u32 m0, s82, 18528\n"
"v_mfma_f32_16x16x32_bf16 v[192:195], v[212:215], v[108:111], v[192:195]\n"
"s_waitcnt lgkmcnt(4)\n"
"buffer_load_dwordx4 v255, s[44:47], s84 offen offset:1536 nt lds\n"
"v_mfma_f32_16x16x32_bf16 v[138:141], v[216:219], v[16:19], v[138:141]\n"
"s_cmp_eq_u32 s81, 1\n"
"ds_read_b64_tr_b16 v[208:209], v253 offset:160\n"
"v_mfma_f32_16x16x32_bf16 v[138:141], v[220:223], v[20:23], v[138:141]\n"
"s_cselect_b32 s30, 128, 0\n"
"ds_read_b64_tr_b16 v[210:211], v253 offset:672\n"
"v_mfma_f32_16x16x32_bf16 v[156:159], v[216:219], v[48:51], v[156:159]\n"
"ds_read_b64_tr_b16 v[212:213], v254 offset:160\n"
"v_mfma_f32_16x16x32_bf16 v[156:159], v[220:223], v[52:55], v[156:159]\n"
"ds_read_b64_tr_b16 v[214:215], v254 offset:672\n"
"v_mfma_f32_16x16x32_bf16 v[174:177], v[216:219], v[80:83], v[174:177]\n"
"s_add_u32 m0, s95, 153088\n"
"v_mfma_f32_16x16x32_bf16 v[174:177], v[220:223], v[84:87], v[174:177]\n"
"buffer_load_dword v255, s[36:39], 0 idxen lds\n"
"v_mfma_f32_16x16x32_bf16 v[192:195], v[216:219], v[112:115], v[192:195]\n"
"s_add_u32 m0, s95, 153344\n"
"v_mfma_f32_16x16x32_bf16 v[192:195], v[220:223], v[116:119], v[192:195]\n"
"buffer_load_dword v255, s[28:31], 0 idxen lds\n"
"v_mfma_f32_16x16x32_bf16 v[138:141], v[224:227], v[24:27], v[138:141]\n"
"s_add_u32 s84, s84, 0x4000\n"
"ds_read_b64_tr_b16 v[216:217], v253 offset:192\n"
"v_mfma_f32_16x16x32_bf16 v[138:141], v[228:231], v[28:31], v[138:141]\n"
"s_cmp_lt_u32 s84, 0x80000\n"
"ds_read_b64_tr_b16 v[218:219], v253 offset:704\n"
"v_mfma_f32_16x16x32_bf16 v[156:159], v[224:227], v[56:59], v[156:159]\n"
"s_cselect_b32 s46, 0x80000, 0\n"
"ds_read_b64_tr_b16 v[220:221], v254 offset:192\n"
"v_mfma_f32_16x16x32_bf16 v[156:159], v[228:231], v[60:63], v[156:159]\n"
"ds_read_b64_tr_b16 v[222:223], v254 offset:704\n"
"v_mfma_f32_16x16x32_bf16 v[174:177], v[224:227], v[88:91], v[174:177]\n"
"v_exp_f32 v138, v138\n"
"v_mfma_f32_16x16x32_bf16 v[174:177], v[228:231], v[92:95], v[174:177]\n"
"v_add_f32 v248, v248, v138\n"
"v_mfma_f32_16x16x32_bf16 v[192:195], v[224:227], v[120:123], v[192:195]\n"
"v_exp_f32 v139, v139\n"
"v_mfma_f32_16x16x32_bf16 v[192:195], v[228:231], v[124:127], v[192:195]\n"
"s_waitcnt lgkmcnt(4)\n"
"ds_read_b64_tr_b16 v[224:225], v253 offset:224\n"
"v_add_f32 v248, v248, v139\n"
"v_mfma_f32_16x16x32_bf16 a[64:67], v[200:203], v[128:131], a[64:67]\n"
"v_exp_f32 v140, v140\n"
"v_mfma_f32_16x16x32_bf16 a[64:67], v[204:207], v[132:135], a[64:67]\n"
"s_memrealtime vcc\n"
"ds_read_b64_tr_b16 v[226:227], v253 offset:736\n"
"v_add_f32 v248, v248, v140\n"
"v_mfma_f32_16x16x32_bf16 a[68:71], v[200:203], v[146:149], a[68:71]\n"
"v_exp_f32 v141, v141\n"
"v_mfma_f32_16x16x32_bf16 a[68:71], v[204:207], v[150:153], a[68:71]\n"
"ds_read_b64_tr_b16 v[228:229], v254 offset:224\n"
"v_add_f32 v248, v248, v141\n"
"v_mfma_f32_16x16x32_bf16 a[72:75], v[200:203], v[164:167], a[72:75]\n"
"v_exp_f32 v156, v156\n"
"v_mfma_f32_16x16x32_bf16 a[72:75], v[204:207], v[168:171], a[72:75]\n"
"ds_read_b64_tr_b16 v[230:231], v254 offset:736\n"
"v_cvt_pk_bf16_f32 v144, v138, v139\n"
"v_mfma_f32_16x16x32_bf16 a[76:79], v[200:203], v[182:185], a[76:79]\n"
"v_exp_f32 v157, v157\n"
"v_mfma_f32_16x16x32_bf16 a[76:79], v[204:207], v[186:189], a[76:79]\n"
"v_cvt_pk_bf16_f32 v145, v140, v141\n"
"v_add_f32 v249, v249, v156\n"
"v_mfma_f32_16x16x32_bf16 a[80:83], v[208:211], v[128:131], a[80:83]\n"
"v_exp_f32 v158, v158\n"
"v_mfma_f32_16x16x32_bf16 a[80:83], v[212:215], v[132:135], a[80:83]\n"
"v_add_f32 v249, v249, v157\n"
"v_add_f32 v249, v249, v158\n"
"v_mfma_f32_16x16x32_bf16 a[84:87], v[208:211], v[146:149], a[84:87]\n"
"v_exp_f32 v159, v159\n"
"v_mfma_f32_16x16x32_bf16 a[84:87], v[212:215], v[150:153], a[84:87]\n"
"v_add_f32 v249, v249, v159\n"
"v_cvt_pk_bf16_f32 v162, v156, v157\n"
"v_mfma_f32_16x16x32_bf16 a[88:91], v[208:211], v[164:167], a[88:91]\n"
"v_exp_f32 v174, v174\n"
"v_mfma_f32_16x16x32_bf16 a[88:91], v[212:215], v[168:171], a[88:91]\n"
"s_waitcnt vmcnt(18) lgkmcnt(0)\n"
"v_cvt_pk_bf16_f32 v163, v158, v159\n"
"v_add_f32 v250, v250, v174\n"
"v_mfma_f32_16x16x32_bf16 a[92:95], v[208:211], v[182:185], a[92:95]\n"
"s_barrier\n"
"v_exp_f32 v175, v175\n"
"v_mfma_f32_16x16x32_bf16 a[92:95], v[212:215], v[186:189], a[92:95]\n"
"ds_read_b128 v[200:203], v252 offset:50688\n"
"v_add_f32 v250, v250, v175\n"
"v_mfma_f32_16x16x32_bf16 a[96:99], v[216:219], v[128:131], a[96:99]\n"
"s_sub_i32 s42, s40, vcc_lo\n"
"v_exp_f32 v176, v176\n"
"v_mfma_f32_16x16x32_bf16 a[96:99], v[220:223], v[132:135], a[96:99]\n"
"s_max_i32 s42, s42, 0\n"
"ds_read_b128 v[204:207], v252 offset:50752\n"
"v_add_f32 v250, v250, v176\n"
"v_mfma_f32_16x16x32_bf16 a[100:103], v[216:219], v[146:149], a[100:103]\n"
"s_add_u32 s40, s42, vcc_lo\n"
"v_exp_f32 v177, v177\n"
"v_mfma_f32_16x16x32_bf16 a[100:103], v[220:223], v[150:153], a[100:103]\n"
"s_sub_i32 s41, s41, 15\n"
"ds_read_b128 v[208:211], v252 offset:50816\n"
"v_add_f32 v250, v250, v177\n"
"v_mfma_f32_16x16x32_bf16 a[104:107], v[216:219], v[164:167], a[104:107]\n"
"s_ashr_i32 s43, s41, 4\n"
"v_exp_f32 v192, v192\n"
"v_mfma_f32_16x16x32_bf16 a[104:107], v[220:223], v[168:171], a[104:107]\n"
"s_add_u32 s40, s40, s43\n"
"ds_read_b128 v[212:215], v252 offset:50880\n"
"v_cvt_pk_bf16_f32 v180, v174, v175\n"
"v_mfma_f32_16x16x32_bf16 a[108:111], v[216:219], v[182:185], a[108:111]\n"
"s_mul_i32 s42, s42, 23\n"
"v_exp_f32 v193, v193\n"
"v_mfma_f32_16x16x32_bf16 a[108:111], v[220:223], v[186:189], a[108:111]\n"
"v_cvt_pk_bf16_f32 v181, v176, v177\n"
"v_add_f32 v251, v251, v192\n"
"v_mfma_f32_16x16x32_bf16 a[112:115], v[224:227], v[128:131], a[112:115]\n"
"v_exp_f32 v194, v194\n"
"v_mfma_f32_16x16x32_bf16 a[112:115], v[228:231], v[132:135], a[112:115]\n"
"ds_read_b128 v[216:219], v252 offset:50944\n"
"v_add_f32 v251, v251, v193\n"
"v_mfma_f32_16x16x32_bf16 a[116:119], v[224:227], v[146:149], a[116:119]\n"
"v_exp_f32 v195, v195\n"
"v_mfma_f32_16x16x32_bf16 a[116:119], v[228:231], v[150:153], a[116:119]\n"
"ds_read_b128 v[220:223], v252 offset:51008\n"
"v_add_f32 v251, v251, v194\n"
"v_mfma_f32_16x16x32_bf16 a[120:123], v[224:227], v[164:167], a[120:123]\n"
"v_add_f32 v251, v251, v195\n"
"v_cvt_pk_bf16_f32 v198, v192, v193\n"
"v_mfma_f32_16x16x32_bf16 a[120:123], v[228:231], v[168:171], a[120:123]\n"
"v_cvt_pk_bf16_f32 v199, v194, v195\n"
"v_mfma_f32_16x16x32_bf16 a[124:127], v[224:227], v[182:185], a[124:127]\n"
"v_mfma_f32_16x16x32_bf16 a[124:127], v[228:231], v[186:189], a[124:127]\n"
"s_waitcnt lgkmcnt(2)\n"
"v_mfma_f32_16x16x32_bf16 v[138:141], v[200:203], v[0:3], v[232:235]\n"
"ds_read_b128 v[224:227], v252 offset:51072\n"
"v_mfma_f32_16x16x32_bf16 v[138:141], v[204:207], v[4:7], v[138:141]\n"
"ds_read_b128 v[228:231], v252 offset:51136\n"
"v_mfma_f32_16x16x32_bf16 v[156:159], v[200:203], v[32:35], v[236:239]\n"
"s_add_u32 m0, s83, 0\n"
"v_mfma_f32_16x16x32_bf16 v[156:159], v[204:207], v[36:39], v[156:159]\n"
"buffer_load_dwordx4 v255, s[48:51], s85 offen offset:0 nt lds\n"
"v_mfma_f32_16x16x32_bf16 v[174:177], v[200:203], v[64:67], v[240:243]\n"
"s_add_u32 m0, s83, 544\n"
"v_mfma_f32_16x16x32_bf16 v[174:177], v[204:207], v[68:71], v[174:177]\n"
"buffer_load_dwordx4 v255, s[48:51], s85 offen offset:512 nt lds\n"
"v_mfma_f32_16x16x32_bf16 v[192:195], v[200:203], v[96:99], v[244:247]\n"
"Ldll6%=:\n"
"s_sub_u32 s42, s42, 32\n"
"s_cbranch_scc0 Ldll6%=\n"
"v_mfma_f32_16x16x32_bf16 v[192:195], v[204:207], v[100:103], v[192:195]\n"
"v_mfma_f32_16x16x32_bf16 v[138:141], v[208:211], v[8:11], v[138:141]\n"
"ds_read_b64_tr_b16 v[200:201], v253 offset:256\n"
"v_mfma_f32_16x16x32_bf16 v[138:141], v[212:215], v[12:15], v[138:141]\n"
"ds_read_b64_tr_b16 v[202:203], v253 offset:768\n"
"v_mfma_f32_16x16x32_bf16 v[156:159], v[208:211], v[40:43], v[156:159]\n"
"ds_read_b64_tr_b16 v[204:205], v254 offset:256\n"
"v_mfma_f32_16x16x32_bf16 v[156:159], v[212:215], v[44:47], v[156:159]\n"
"ds_read_b64_tr_b16 v[206:207], v254 offset:768\n"
"v_mfma_f32_16x16x32_bf16 v[174:177], v[208:211], v[72:75], v[174:177]\n"
"s_add_u32 m0, s83, 1088\n"
"v_mfma_f32_16x16x32_bf16 v[174:177], v[212:215], v[76:79], v[174:177]\n"
"buffer_load_dwordx4 v255, s[48:51], s85 offen offset:1024 nt lds\n"
"v_mfma_f32_16x16x32_bf16 v[192:195], v[208:211], v[104:107], v[192:195]\n"
"s_add_u32 m0, s83, 1632\n"
"v_mfma_f32_16x16x32_bf16 v[192:195], v[212:215], v[108:111], v[192:195]\n"
"s_waitcnt lgkmcnt(4)\n"
"buffer_load_dwordx4 v255, s[48:51], s85 offen offset:1536 nt lds\n"
"v_mfma_f32_16x16x32_bf16 v[138:141], v[216:219], v[16:19], v[138:141]\n"
"ds_read_b64_tr_b16 v[208:209], v253 offset:288\n"
"v_mfma_f32_16x16x32_bf16 v[138:141], v[220:223], v[20:23], v[138:141]\n"
"ds_read_b64_tr_b16 v[210:211], v253 offset:800\n"
"v_mfma_f32_16x16x32_bf16 v[156:159], v[216:219], v[48:51], v[156:159]\n"
"ds_read_b64_tr_b16 v[212:213], v254 offset:288\n"
"v_mfma_f32_16x16x32_bf16 v[156:159], v[220:223], v[52:55], v[156:159]\n"
"ds_read_b64_tr_b16 v[214:215], v254 offset:800\n"
"v_mfma_f32_16x16x32_bf16 v[174:177], v[216:219], v[80:83], v[174:177]\n"
"s_add_u32 m0, s95, 153088\n"
"v_mfma_f32_16x16x32_bf16 v[174:177], v[220:223], v[84:87], v[174:177]\n"
"buffer_load_dword v255, s[36:39], s33 idxen lds\n"
"v_mfma_f32_16x16x32_bf16 v[192:195], v[216:219], v[112:115], v[192:195]\n"
"v_mfma_f32_16x16x32_bf16 v[192:195], v[220:223], v[116:119], v[192:195]\n"
"v_mfma_f32_16x16x32_bf16 v[138:141], v[224:227], v[24:27], v[138:141]\n"
"s_add_u32 s85, s85, 0x4000\n"
"ds_read_b64_tr_b16 v[216:217], v253 offset:320\n"
"v_mfma_f32_16x16x32_bf16 v[138:141], v[228:231], v[28:31], v[138:141]\n"
"s_cmp_lt_u32 s85, 0x80000\n"
"ds_read_b64_tr_b16 v[218:219], v253 offset:832\n"
"v_mfma_f32_16x16x32_bf16 v[156:159], v[224:227], v[56:59], v[156:159]\n"
"s_cselect_b32 s50, 0x80000, 0\n"
"ds_read_b64_tr_b16 v[220:221], v254 offset:320\n"
"v_mfma_f32_16x16x32_bf16 v[156:159], v[228:231], v[60:63], v[156:159]\n"
"s_add_u32 s83, s83, 16896\n"
"ds_read_b64_tr_b16 v[222:223], v254 offset:832\n"
"v_mfma_f32_16x16x32_bf16 v[174:177], v[224:227], v[88:91], v[174:177]\n"
"s_cmp_ge_u32 s83, s91\n"
"v_exp_f32 v138, v138\n"
"v_mfma_f32_16x16x32_bf16 v[174:177], v[228:231], v[92:95], v[174:177]\n"
"s_cselect_b32 s87, 84480, 0\n"
"v_add_f32 v248, v248, v138\n"
"v_mfma_f32_16x16x32_bf16 v[192:195], v[224:227], v[120:123], v[192:195]\n"
"s_sub_u32 s83, s83, s87\n"
"v_exp_f32 v139, v139\n"
"v_mfma_f32_16x16x32_bf16 v[192:195], v[228:231], v[124:127], v[192:195]\n"
"s_waitcnt lgkmcnt(4)\n"
"ds_read_b64_tr_b16 v[224:225], v253 offset:352\n"
"v_add_f32 v248, v248, v139\n"
"v_mfma_f32_16x16x32_bf16 a[128:131], v[200:203], v[128:131], a[128:131]\n"
"v_exp_f32 v140, v140\n"
"v_mfma_f32_16x16x32_bf16 a[128:131], v[204:207], v[132:135], a[128:131]\n"
"ds_read_b64_tr_b16 v[226:227], v253 offset:864\n"
"v_add_f32 v248, v248, v140\n"
"v_mfma_f32_16x16x32_bf16 a[132:135], v[200:203], v[146:149], a[132:135]\n"
"v_exp_f32 v141, v141\n"
"v_mfma_f32_16x16x32_bf16 a[132:135], v[204:207], v[150:153], a[132:135]\n"
"ds_read_b64_tr_b16 v[228:229], v254 offset:352\n"
"v_add_f32 v248, v248, v141\n"
"v_mfma_f32_16x16x32_bf16 a[136:139], v[200:203], v[164:167], a[136:139]\n"
"v_exp_f32 v156, v156\n"
"v_mfma_f32_16x16x32_bf16 a[136:139], v[204:207], v[168:171], a[136:139]\n"
"ds_read_b64_tr_b16 v[230:231], v254 offset:864\n"
"v_cvt_pk_bf16_f32 v141, v140, v141\n"
"v_mfma_f32_16x16x32_bf16 a[140:143], v[200:203], v[182:185], a[140:143]\n"
"v_exp_f32 v157, v157\n"
"v_mfma_f32_16x16x32_bf16 a[140:143], v[204:207], v[186:189], a[140:143]\n"
"v_cvt_pk_bf16_f32 v140, v138, v139\n"
"v_add_f32 v249, v249, v156\n"
"v_mfma_f32_16x16x32_bf16 a[144:147], v[208:211], v[128:131], a[144:147]\n"
"v_exp_f32 v158, v158\n"
"v_mfma_f32_16x16x32_bf16 a[144:147], v[212:215], v[132:135], a[144:147]\n"
"v_add_f32 v249, v249, v157\n"
"v_add_f32 v249, v249, v158\n"
"v_mfma_f32_16x16x32_bf16 a[148:151], v[208:211], v[146:149], a[148:151]\n"
"v_exp_f32 v159, v159\n"
"v_mfma_f32_16x16x32_bf16 a[148:151], v[212:215], v[150:153], a[148:151]\n"
"v_add_f32 v249, v249, v159\n"
"v_cvt_pk_bf16_f32 v159, v158, v159\n"
"v_mfma_f32_16x16x32_bf16 a[152:155], v[208:211], v[164:167], a[152:155]\n"
"v_exp_f32 v174, v174\n"
"v_mfma_f32_16x16x32_bf16 a[152:155], v[212:215], v[168:171], a[152:155]\n"
"v_cvt_pk_bf16_f32 v158, v156, v157\n"
"v_add_f32 v250, v250, v174\n"
"v_mfma_f32_16x16x32_bf16 a[156:159], v[208:211], v[182:185], a[156:159]\n"
"v_exp_f32 v175, v175\n"
"v_mfma_f32_16x16x32_bf16 a[156:159], v[212:215], v[186:189], a[156:159]\n"
"s_waitcnt lgkmcnt(0)\n"
"ds_read_b128 v[200:203], v252 offset:51200\n"
"v_add_f32 v250, v250, v175\n"
"v_mfma_f32_16x16x32_bf16 a[160:163], v[216:219], v[128:131], a[160:163]\n"
"v_exp_f32 v176, v176\n"
"v_mfma_f32_16x16x32_bf16 a[160:163], v[220:223], v[132:135], a[160:163]\n"
"ds_read_b128 v[204:207], v252 offset:51264\n"
"v_add_f32 v250, v250, v176\n"
"v_mfma_f32_16x16x32_bf16 a[164:167], v[216:219], v[146:149], a[164:167]\n"
"v_exp_f32 v177, v177\n"
"v_mfma_f32_16x16x32_bf16 a[164:167], v[220:223], v[150:153], a[164:167]\n"
"ds_read_b128 v[208:211], v252 offset:51328\n"
"v_add_f32 v250, v250, v177\n"
"v_mfma_f32_16x16x32_bf16 a[168:171], v[216:219], v[164:167], a[168:171]\n"
"v_exp_f32 v192, v192\n"
"v_mfma_f32_16x16x32_bf16 a[168:171], v[220:223], v[168:171], a[168:171]\n"
"ds_read_b128 v[212:215], v252 offset:51392\n"
"v_cvt_pk_bf16_f32 v177, v176, v177\n"
"v_mfma_f32_16x16x32_bf16 a[172:175], v[216:219], v[182:185], a[172:175]\n"
"v_exp_f32 v193, v193\n"
"v_mfma_f32_16x16x32_bf16 a[172:175], v[220:223], v[186:189], a[172:175]\n"
"v_cvt_pk_bf16_f32 v176, v174, v175\n"
"v_add_f32 v251, v251, v192\n"
"v_mfma_f32_16x16x32_bf16 a[176:179], v[224:227], v[128:131], a[176:179]\n"
"v_exp_f32 v194, v194\n"
"v_mfma_f32_16x16x32_bf16 a[176:179], v[228:231], v[132:135], a[176:179]\n"
"ds_read_b128 v[216:219], v252 offset:51456\n"
"v_add_f32 v251, v251, v193\n"
"v_mfma_f32_16x16x32_bf16 a[180:183], v[224:227], v[146:149], a[180:183]\n"
"v_exp_f32 v195, v195\n"
"v_mfma_f32_16x16x32_bf16 a[180:183], v[228:231], v[150:153], a[180:183]\n"
"ds_read_b128 v[220:223], v252 offset:51520\n"
"v_add_f32 v251, v251, v194\n"
"v_mfma_f32_16x16x32_bf16 a[184:187], v[224:227], v[164:167], a[184:187]\n"
"v_add_f32 v251, v251, v195\n"
"v_cvt_pk_bf16_f32 v195, v194, v195\n"
"v_mfma_f32_16x16x32_bf16 a[184:187], v[228:231], v[168:171], a[184:187]\n"
"v_cvt_pk_bf16_f32 v194, v192, v193\n"
"v_mfma_f32_16x16x32_bf16 a[188:191], v[224:227], v[182:185], a[188:191]\n"
"v_mfma_f32_16x16x32_bf16 a[188:191], v[228:231], v[186:189], a[188:191]\n"
"s_waitcnt lgkmcnt(2)\n"
"v_mfma_f32_16x16x32_bf16 v[136:139], v[200:203], v[0:3], v[232:235]\n"
"ds_read_b128 v[224:227], v252 offset:51584\n"
"v_mfma_f32_16x16x32_bf16 v[136:139], v[204:207], v[4:7], v[136:139]\n"
"ds_read_b128 v[228:231], v252 offset:51648\n"
"v_mfma_f32_16x16x32_bf16 v[154:157], v[200:203], v[32:35], v[236:239]\n"
"s_add_u32 m0, s82, 33792\n"
"v_mfma_f32_16x16x32_bf16 v[154:157], v[204:207], v[36:39], v[154:157]\n"
"buffer_load_dwordx4 v255, s[44:47], s84 offen offset:0 lds\n"
"v_mfma_f32_16x16x32_bf16 v[172:175], v[200:203], v[64:67], v[240:243]\n"
"s_add_u32 m0, s82, 34336\n"
"v_mfma_f32_16x16x32_bf16 v[172:175], v[204:207], v[68:71], v[172:175]\n"
"buffer_load_dwordx4 v255, s[44:47], s84 offen offset:512 lds\n"
"v_mfma_f32_16x16x32_bf16 v[190:193], v[200:203], v[96:99], v[244:247]\n"
"v_mfma_f32_16x16x32_bf16 v[190:193], v[204:207], v[100:103], v[190:193]\n"
"v_mfma_f32_16x16x32_bf16 v[136:139], v[208:211], v[8:11], v[136:139]\n"
"ds_read_b64_tr_b16 v[200:201], v253 offset:384\n"
"v_mfma_f32_16x16x32_bf16 v[136:139], v[212:215], v[12:15], v[136:139]\n"
"ds_read_b64_tr_b16 v[202:203], v253 offset:896\n"
"v_mfma_f32_16x16x32_bf16 v[154:157], v[208:211], v[40:43], v[154:157]\n"
"ds_read_b64_tr_b16 v[204:205], v254 offset:384\n"
"v_mfma_f32_16x16x32_bf16 v[154:157], v[212:215], v[44:47], v[154:157]\n"
"ds_read_b64_tr_b16 v[206:207], v254 offset:896\n"
"v_mfma_f32_16x16x32_bf16 v[172:175], v[208:211], v[72:75], v[172:175]\n"
"s_add_u32 m0, s82, 34880\n"
"v_mfma_f32_16x16x32_bf16 v[172:175], v[212:215], v[76:79], v[172:175]\n"
"buffer_load_dwordx4 v255, s[44:47], s84 offen offset:1024 nt lds\n"
"v_mfma_f32_16x16x32_bf16 v[190:193], v[208:211], v[104:107], v[190:193]\n"
"s_add_u32 m0, s82, 35424\n"
"v_mfma_f32_16x16x32_bf16 v[190:193], v[212:215], v[108:111], v[190:193]\n"
"s_waitcnt lgkmcnt(4)\n"
"buffer_load_dwordx4 v255, s[44:47], s84 offen offset:1536 nt lds\n"
"v_mfma_f32_16x16x32_bf16 v[136:139], v[216:219], v[16:19], v[136:139]\n"
"ds_read_b64_tr_b16 v[208:209], v253 offset:416\n"
"v_mfma_f32_16x16x32_bf16 v[136:139], v[220:223], v[20:23], v[136:139]\n"
"ds_read_b64_tr_b16 v[210:211], v253 offset:928\n"
"v_mfma_f32_16x16x32_bf16 v[154:157], v[216:219], v[48:51], v[154:157]\n"
"ds_read_b64_tr_b16 v[212:213], v254 offset:416\n"
"v_mfma_f32_16x16x32_bf16 v[154:157], v[220:223], v[52:55], v[154:157]\n"
"ds_read_b64_tr_b16 v[214:215], v254 offset:928\n"
"v_mfma_f32_16x16x32_bf16 v[172:175], v[216:219], v[80:83], v[172:175]\n"
"v_mfma_f32_16x16x32_bf16 v[172:175], v[220:223], v[84:87], v[172:175]\n"
"v_mfma_f32_16x16x32_bf16 v[190:193], v[216:219], v[112:115], v[190:193]\n"
"v_mfma_f32_16x16x32_bf16 v[190:193], v[220:223], v[116:119], v[190:193]\n"
"v_mfma_f32_16x16x32_bf16 v[136:139], v[224:227], v[24:27], v[136:139]\n"
"s_add_u32 s84, s84, 0x4000\n"
"ds_read_b64_tr_b16 v[216:217], v253 offset:448\n"
"v_mfma_f32_16x16x32_bf16 v[136:139], v[228:231], v[28:31], v[136:139]\n"
"s_cmp_lt_u32 s84, 0x80000\n"
"ds_read_b64_tr_b16 v[218:219], v253 offset:960\n"
"v_mfma_f32_16x16x32_bf16 v[154:157], v[224:227], v[56:59], v[154:157]\n"
"s_cselect_b32 s46, 0x80000, 0\n"
"ds_read_b64_tr_b16 v[220:221], v254 offset:448\n"
"v_mfma_f32_16x16x32_bf16 v[154:157], v[228:231], v[60:63], v[154:157]\n"
"ds_read_b64_tr_b16 v[222:223], v254 offset:960\n"
"v_mfma_f32_16x16x32_bf16 v[172:175], v[224:227], v[88:91], v[172:175]\n"
"v_exp_f32 v136, v136\n"
"v_mfma_f32_16x16x32_bf16 v[172:175], v[228:231], v[92:95], v[172:175]\n"
"v_add_f32 v248, v248, v136\n"
"v_mfma_f32_16x16x32_bf16 v[190:193], v[224:227], v[120:123], v[190:193]\n"
"v_exp_f32 v137, v137\n"
"v_mfma_f32_16x16x32_bf16 v[190:193], v[228:231], v[124:127], v[190:193]\n"
"s_waitcnt lgkmcnt(4)\n"
"ds_read_b64_tr_b16 v[224:225], v253 offset:480\n"
"v_add_f32 v248, v248, v137\n"
"v_mfma_f32_16x16x32_bf16 a[192:195], v[200:203], v[128:131], a[192:195]\n"
"v_exp_f32 v138, v138\n"
"v_mfma_f32_16x16x32_bf16 a[192:195], v[204:207], v[132:135], a[192:195]\n"
"s_memrealtime vcc\n"
"ds_read_b64_tr_b16 v[226:227], v253 offset:992\n"
"v_add_f32 v248, v248, v138\n"
"v_mfma_f32_16x16x32_bf16 a[196:199], v[200:203], v[146:149], a[196:199]\n"
"v_exp_f32 v139, v139\n"
"v_mfma_f32_16x16x32_bf16 a[196:199], v[204:207], v[150:153], a[196:199]\n"
"ds_read_b64_tr_b16 v[228:229], v254 offset:480\n"
"v_add_f32 v248, v248, v139\n"
"v_mfma_f32_16x16x32_bf16 a[200:203], v[200:203], v[164:167], a[200:203]\n"
"v_exp_f32 v154, v154\n"
"v_mfma_f32_16x16x32_bf16 a[200:203], v[204:207], v[168:171], a[200:203]\n"
"ds_read_b64_tr_b16 v[230:231], v254 offset:992\n"
"v_cvt_pk_bf16_f32 v139, v138, v139\n"
"v_mfma_f32_16x16x32_bf16 a[204:207], v[200:203], v[182:185], a[204:207]\n"
"v_exp_f32 v155, v155\n"
"v_mfma_f32_16x16x32_bf16 a[204:207], v[204:207], v[186:189], a[204:207]\n"
"v_cvt_pk_bf16_f32 v138, v136, v137\n"
"v_add_f32 v249, v249, v154\n"
"v_mfma_f32_16x16x32_bf16 a[208:211], v[208:211], v[128:131], a[208:211]\n"
"v_exp_f32 v156, v156\n"
"v_mfma_f32_16x16x32_bf16 a[208:211], v[212:215], v[132:135], a[208:211]\n"
"v_add_f32 v249, v249, v155\n"
"v_add_f32 v249, v249, v156\n"
"v_mfma_f32_16x16x32_bf16 a[212:215], v[208:211], v[146:149], a[212:215]\n"
"v_exp_f32 v157, v157\n"
"v_mfma_f32_16x16x32_bf16 a[212:215], v[212:215], v[150:153], a[212:215]\n"
"v_add_f32 v249, v249, v157\n"
"v_cvt_pk_bf16_f32 v157, v156, v157\n"
"v_mfma_f32_16x16x32_bf16 a[216:219], v[208:211], v[164:167], a[216:219]\n"
"v_exp_f32 v172, v172\n"
"v_mfma_f32_16x16x32_bf16 a[216:219], v[212:215], v[168:171], a[216:219]\n"
"s_waitcnt vmcnt(15) lgkmcnt(0)\n"
"v_cvt_pk_bf16_f32 v156, v154, v155\n"
"v_add_f32 v250, v250, v172\n"
"v_mfma_f32_16x16x32_bf16 a[220:223], v[208:211], v[182:185], a[220:223]\n"
"s_barrier\n"
"v_exp_f32 v173, v173\n"
"v_mfma_f32_16x16x32_bf16 a[220:223], v[212:215], v[186:189], a[220:223]\n"
"ds_read_b128 v[200:203], v252 offset:0\n"
"v_add_f32 v250, v250, v173\n"
"v_mfma_f32_16x16x32_bf16 a[224:227], v[216:219], v[128:131], a[224:227]\n"
"s_sub_i32 s42, s40, vcc_lo\n"
"v_exp_f32 v174, v174\n"
"v_mfma_f32_16x16x32_bf16 a[224:227], v[220:223], v[132:135], a[224:227]\n"
"s_max_i32 s42, s42, 0\n"
"ds_read_b128 v[204:207], v252 offset:64\n"
"v_add_f32 v250, v250, v174\n"
"v_mfma_f32_16x16x32_bf16 a[228:231], v[216:219], v[146:149], a[228:231]\n"
"s_add_u32 s40, s42, vcc_lo\n"
"v_exp_f32 v175, v175\n"
"v_mfma_f32_16x16x32_bf16 a[228:231], v[220:223], v[150:153], a[228:231]\n"
"s_sub_i32 s41, s41, 15\n"
"ds_read_b128 v[208:211], v252 offset:128\n"
"v_add_f32 v250, v250, v175\n"
"v_mfma_f32_16x16x32_bf16 a[232:235], v[216:219], v[164:167], a[232:235]\n"
"s_ashr_i32 s43, s41, 4\n"
"v_exp_f32 v190, v190\n"
"v_mfma_f32_16x16x32_bf16 a[232:235], v[220:223], v[168:171], a[232:235]\n"
"s_add_u32 s40, s40, s43\n"
"ds_read_b128 v[212:215], v252 offset:192\n"
"v_cvt_pk_bf16_f32 v175, v174, v175\n"
"v_mfma_f32_16x16x32_bf16 a[236:239], v[216:219], v[182:185], a[236:239]\n"
"s_mul_i32 s42, s42, 23\n"
"v_exp_f32 v191, v191\n"
"v_mfma_f32_16x16x32_bf16 a[236:239], v[220:223], v[186:189], a[236:239]\n"
"v_cvt_pk_bf16_f32 v174, v172, v173\n"
"v_add_f32 v251, v251, v190\n"
"v_mfma_f32_16x16x32_bf16 a[240:243], v[224:227], v[128:131], a[240:243]\n"
"v_exp_f32 v192, v192\n"
"v_mfma_f32_16x16x32_bf16 a[240:243], v[228:231], v[132:135], a[240:243]\n"
"ds_read_b128 v[216:219], v252 offset:256\n"
"v_add_f32 v251, v251, v191\n"
"v_mfma_f32_16x16x32_bf16 a[244:247], v[224:227], v[146:149], a[244:247]\n"
"v_exp_f32 v193, v193\n"
"v_mfma_f32_16x16x32_bf16 a[244:247], v[228:231], v[150:153], a[244:247]\n"
"ds_read_b128 v[220:223], v252 offset:320\n"
"v_add_f32 v251, v251, v192\n"
"v_mfma_f32_16x16x32_bf16 a[248:251], v[224:227], v[164:167], a[248:251]\n"
"v_add_f32 v251, v251, v193\n"
"v_cvt_pk_bf16_f32 v193, v192, v193\n"
"v_mfma_f32_16x16x32_bf16 a[248:251], v[228:231], v[168:171], a[248:251]\n"
"s_sub_u32 s81, s81, 1\n"
"v_cvt_pk_bf16_f32 v192, v190, v191\n"
"v_mfma_f32_16x16x32_bf16 a[252:255], v[224:227], v[182:185], a[252:255]\n"
"s_cmp_lg_u32 s81, 0\n"
"v_mfma_f32_16x16x32_bf16 a[252:255], v[228:231], v[186:189], a[252:255]\n"
"s_cbranch_scc1 Lloopa%=\n"
"s_cmp_ge_u32 s89, 50688\n"
"s_cselect_b32 s86, s93, s92\n"
"s_add_u32 s89, s89, s86\n"
"v_add_u32 v253, s86, v253\n"
"s_cmp_ge_u32 s90, 50688\n"
"s_cselect_b32 s86, s93, s92\n"
"s_add_u32 s90, s90, s86\n"
"v_add_u32 v254, s86, v254\n"
"s_waitcnt lgkmcnt(0)\n"
"ds_read_b64_tr_b16 v[200:201], v253 offset:0\n"
"ds_read_b64_tr_b16 v[202:203], v253 offset:512\n"
"ds_read_b64_tr_b16 v[206:207], v254 offset:0\n"
"ds_read_b64_tr_b16 v[204:205], v254 offset:512\n"
"ds_read_b64_tr_b16 v[208:209], v253 offset:32\n"
"ds_read_b64_tr_b16 v[210:211], v253 offset:544\n"
"ds_read_b64_tr_b16 v[214:215], v254 offset:32\n"
"ds_read_b64_tr_b16 v[212:213], v254 offset:544\n"
"ds_read_b64_tr_b16 v[216:217], v253 offset:64\n"
"ds_read_b64_tr_b16 v[218:219], v253 offset:576\n"
"ds_read_b64_tr_b16 v[222:223], v254 offset:64\n"
"ds_read_b64_tr_b16 v[220:221], v254 offset:576\n"
"v_mov_b32 v128, v248\n"
"v_mov_b32 v146, v249\n"
"v_mov_b32 v164, v250\n"
"v_mov_b32 v182, v251\n"
"v_mov_b32 v130, 152128\n"
"v_permlane32_swap_b32 v248, v128\n"
"v_permlane32_swap_b32 v249, v146\n"
"v_permlane32_swap_b32 v250, v164\n"
"v_permlane32_swap_b32 v251, v182\n"
"v_lshl_add_u32 v131, s80, 2, v130\n"
"v_add_f32 v248, v248, v128\n"
"v_add_f32 v249, v249, v146\n"
"v_add_f32 v250, v250, v164\n"
"v_add_f32 v251, v251, v182\n"
"v_mov_b32 v128, v248\n"
"v_mov_b32 v146, v249\n"
"v_mov_b32 v164, v250\n"
"v_mov_b32 v182, v251\n"
"s_nop 0\n"
"v_permlane16_swap_b32 v248, v128\n"
"v_permlane16_swap_b32 v249, v146\n"
"v_permlane16_swap_b32 v250, v164\n"
"v_permlane16_swap_b32 v251, v182\n"
"s_nop 0\n"
"v_add_f32 v248, v248, v128\n"
"v_add_f32 v249, v249, v146\n"
"v_add_f32 v250, v250, v164\n"
"v_add_f32 v251, v251, v182\n"
"s_waitcnt lgkmcnt(10)\n"
"v_mfma_f32_16x16x32_bf16 a[0:3], v[200:203], v[142:145], a[0:3]\n"
"s_waitcnt lgkmcnt(8)\n"
"ds_read_b64_tr_b16 v[224:225], v253 offset:96\n"
"v_add_f32 v128, v248, v249\n"
"v_mfma_f32_16x16x32_bf16 a[0:3], v[204:207], v[138:141], a[0:3]\n"
"v_add_f32 v146, v250, v251\n"
"v_add_f32 v128, v128, v146\n"
"v_mfma_f32_16x16x32_bf16 a[4:7], v[200:203], v[160:163], a[4:7]\n"
"ds_read_b64_tr_b16 v[226:227], v253 offset:608\n"
"v_cmp_nge_f32 vcc, 0x53800000, v128\n"
"v_mfma_f32_16x16x32_bf16 a[4:7], v[204:207], v[156:159], a[4:7]\n"
"s_cmp_lg_u64 vcc, 0\n"
"v_mbcnt_lo_u32_b32 v166, -1, 0\n"
"v_mbcnt_hi_u32_b32 v166, -1, v166\n"
"v_mfma_f32_16x16x32_bf16 a[8:11], v[200:203], v[178:181], a[8:11]\n"
"s_cselect_b32 s87, 1, 0\n"
"ds_read_b64_tr_b16 v[230:231], v254 offset:96\n"
"v_lshrrev_b32 v164, 4, v166\n"
"v_mfma_f32_16x16x32_bf16 a[8:11], v[204:207], v[174:177], a[8:11]\n"
"s_add_u32 s84, s34, 1\n"
"v_mov_b32 v129, s87\n"
"v_lshlrev_b32 v164, 1, v164\n"
"v_mfma_f32_16x16x32_bf16 a[12:15], v[200:203], v[196:199], a[12:15]\n"
"s_cmp_ge_u32 s84, 7\n"
"ds_read_b64_tr_b16 v[228:229], v254 offset:608\n"
"ds_write_b32 v131, v129\n"
"v_mfma_f32_16x16x32_bf16 a[12:15], v[204:207], v[192:195], a[12:15]\n"
"s_waitcnt lgkmcnt(11)\n"
"s_cselect_b32 s84, 0, s84\n"
"v_and_b32 v165, 7, v166\n"
"v_xor_b32 v164, v164, v165\n"
"v_mfma_f32_16x16x32_bf16 a[16:19], v[208:211], v[142:145], a[16:19]\n"
"s_waitcnt lgkmcnt(5)\n"
"s_mul_i32 s85, s84, 131072\n"
"ds_read_b64_tr_b16 v[200:201], v253 offset:128\n"
"v_lshlrev_b32 v164, 4, v164\n"
"v_mfma_f32_16x16x32_bf16 a[16:19], v[212:215], v[138:141], a[16:19]\n"
"s_lshl_b32 s84, s84, 7\n"
"v_lshrrev_b32 v165, 3, v166\n"
"v_lshl_add_u32 v165, s80, 3, v165\n"
"v_mfma_f32_16x16x32_bf16 a[20:23], v[208:211], v[160:163], a[20:23]\n"
"ds_read_b64_tr_b16 v[202:203], v253 offset:640\n"
"v_mul_u32_u24 v165, 896, v165\n"
"v_mfma_f32_16x16x32_bf16 a[20:23], v[212:215], v[156:159], a[20:23]\n"
"s_waitcnt vmcnt(0) lgkmcnt(0)\n"
"v_add3_u32 v232, v164, v165, s84\n"
"v_lshrrev_b32 v164, 5, v166\n"
"v_mfma_f32_16x16x32_bf16 a[24:27], v[208:211], v[178:181], a[24:27]\n"
"s_barrier\n"
"ds_read_b64_tr_b16 v[206:207], v254 offset:128\n"
"v_lshlrev_b32 v164, 10, v164\n"
"v_mfma_f32_16x16x32_bf16 a[24:27], v[212:215], v[174:177], a[24:27]\n"
"ds_read_b128 v[146:149], v130\n"
"v_bfe_u32 v165, v166, 2, 3\n"
"v_mfma_f32_16x16x32_bf16 a[28:31], v[208:211], v[196:199], a[28:31]\n"
"ds_read_b64_tr_b16 v[204:205], v254 offset:640\n"
"v_lshl_add_u32 v164, v165, 7, v164\n"
"v_mfma_f32_16x16x32_bf16 a[28:31], v[212:215], v[192:195], a[28:31]\n"
"v_lshrrev_b32 v165, 1, v165\n"
"v_lshl_add_u32 v164, v165, 5, v164\n"
"v_mfma_f32_16x16x32_bf16 a[32:35], v[216:219], v[142:145], a[32:35]\n"
"ds_read_b64_tr_b16 v[208:209], v253 offset:160\n"
"v_and_b32 v165, 3, v166\n"
"v_mfma_f32_16x16x32_bf16 a[32:35], v[220:223], v[138:141], a[32:35]\n"
"v_lshl_add_u32 v164, v165, 3, v164\n"
"v_xor_b32 v236, 0, v164\n"
"v_mfma_f32_16x16x32_bf16 a[36:39], v[216:219], v[160:163], a[36:39]\n"
"ds_read_b64_tr_b16 v[210:211], v253 offset:672\n"
"v_xor_b32 v237, 32, v164\n"
"v_mfma_f32_16x16x32_bf16 a[36:39], v[220:223], v[156:159], a[36:39]\n"
"s_waitcnt lgkmcnt(3)\n"
"v_or3_b32 v146, v146, v147, v148\n"
"v_or_b32 v146, v146, v149\n"
"v_mfma_f32_16x16x32_bf16 a[40:43], v[216:219], v[178:181], a[40:43]\n"
"ds_read_b64_tr_b16 v[214:215], v254 offset:160\n"
"v_readfirstlane_b32 s87, v146\n"
"v_mfma_f32_16x16x32_bf16 a[40:43], v[220:223], v[174:177], a[40:43]\n"
"s_cmp_gt_u32 s87, s94\n"
"v_xor_b32 v238, 64, v164\n"
"v_xor_b32 v239, 96, v164\n"
"v_mfma_f32_16x16x32_bf16 a[44:47], v[216:219], v[196:199], a[44:47]\n"
"s_cbranch_scc1 Lfallback%=\n"
"ds_read_b64_tr_b16 v[212:213], v254 offset:672\n"
"v_mov_b32 v233, v166\n"
"v_mfma_f32_16x16x32_bf16 a[44:47], v[220:223], v[192:195], a[44:47]\n"
"s_add_u32 m0, s95, 0\n"
"v_rcp_f32 v240, v248\n"
"v_mfma_f32_16x16x32_bf16 a[48:51], v[224:227], v[142:145], a[48:51]\n"
"buffer_load_dwordx4 v232, s[52:55], s64 offen lds\n"
"v_accvgpr_read_b32 v168, a0\n"
"v_mfma_f32_16x16x32_bf16 a[48:51], v[228:231], v[138:141], a[48:51]\n"
"ds_read_b64_tr_b16 v[216:217], v253 offset:192\n"
"v_accvgpr_read_b32 v169, a1\n"
"v_mfma_f32_16x16x32_bf16 a[52:55], v[224:227], v[160:163], a[52:55]\n"
"ds_read_b64_tr_b16 v[218:219], v253 offset:704\n"
"v_accvgpr_read_b32 v170, a2\n"
"v_mfma_f32_16x16x32_bf16 a[52:55], v[228:231], v[156:159], a[52:55]\n"
"v_rcp_f32 v241, v249\n"
"v_mfma_f32_16x16x32_bf16 a[56:59], v[224:227], v[178:181], a[56:59]\n"
"s_add_u32 m0, s95, 4096\n"
"ds_read_b64_tr_b16 v[222:223], v254 offset:192\n"
"v_accvgpr_read_b32 v171, a3\n"
"v_mfma_f32_16x16x32_bf16 a[56:59], v[228:231], v[174:177], a[56:59]\n"
"buffer_load_dwordx4 v232, s[52:55], s65 offen lds\n"
"v_cvt_pk_bf16_f32 v0, v168, v169\n"
"v_mfma_f32_16x16x32_bf16 a[60:63], v[224:227], v[196:199], a[60:63]\n"
"ds_read_b64_tr_b16 v[220:221], v254 offset:704\n"
"v_cvt_pk_bf16_f32 v1, v170, v171\n"
"v_mfma_f32_16x16x32_bf16 a[60:63], v[228:231], v[192:195], a[60:63]\n"
"v_rcp_f32 v242, v250\n"
"v_mfma_f32_16x16x32_bf16 a[64:67], v[200:203], v[142:145], a[64:67]\n"
"s_waitcnt lgkmcnt(4)\n"
"ds_read_b64_tr_b16 v[224:225], v253 offset:224\n"
"v_accvgpr_read_b32 v182, a16\n"
"v_mfma_f32_16x16x32_bf16 a[64:67], v[204:207], v[138:141], a[64:67]\n"
"s_add_u32 m0, s95, 8192\n"
"v_rcp_f32 v243, v251\n"
"v_mfma_f32_16x16x32_bf16 a[68:71], v[200:203], v[160:163], a[68:71]\n"
"buffer_load_dwordx4 v232, s[52:55], s66 offen lds\n"
"v_accvgpr_read_b32 v183, a17\n"
"v_mfma_f32_16x16x32_bf16 a[68:71], v[204:207], v[156:159], a[68:71]\n"
"ds_read_b64_tr_b16 v[226:227], v253 offset:736\n"
"v_accvgpr_read_b32 v184, a18\n"
"v_mfma_f32_16x16x32_bf16 a[72:75], v[200:203], v[178:181], a[72:75]\n"
"ds_read_b64_tr_b16 v[230:231], v254 offset:224\n"
"v_accvgpr_read_b32 v185, a19\n"
"v_mfma_f32_16x16x32_bf16 a[72:75], v[204:207], v[174:177], a[72:75]\n"
"v_cvt_pk_bf16_f32 v2, v182, v183\n"
"v_cvt_pk_bf16_f32 v3, v184, v185\n"
"v_mfma_f32_16x16x32_bf16 a[76:79], v[200:203], v[196:199], a[76:79]\n"
"s_add_u32 m0, s95, 12288\n"
"ds_read_b64_tr_b16 v[228:229], v254 offset:736\n"
"v_accvgpr_read_b32 v186, a4\n"
"v_mfma_f32_16x16x32_bf16 a[76:79], v[204:207], v[192:195], a[76:79]\n"
"buffer_load_dwordx4 v232, s[52:55], s67 offen lds\n"
"v_accvgpr_read_b32 v187, a5\n"
"v_mfma_f32_16x16x32_bf16 a[80:83], v[208:211], v[142:145], a[80:83]\n"
"ds_read_b64_tr_b16 v[200:201], v253 offset:256\n"
"v_accvgpr_read_b32 v188, a6\n"
"v_mfma_f32_16x16x32_bf16 a[80:83], v[212:215], v[138:141], a[80:83]\n"
"v_accvgpr_read_b32 v189, a7\n"
"v_cvt_pk_bf16_f32 v32, v186, v187\n"
"v_mfma_f32_16x16x32_bf16 a[84:87], v[208:211], v[160:163], a[84:87]\n"
"ds_read_b64_tr_b16 v[202:203], v253 offset:768\n"
"v_cvt_pk_bf16_f32 v33, v188, v189\n"
"v_mfma_f32_16x16x32_bf16 a[84:87], v[212:215], v[156:159], a[84:87]\n"
"s_add_u32 m0, s95, 16384\n"
"v_accvgpr_read_b32 v168, a20\n"
"v_accvgpr_read_b32 v169, a21\n"
"v_mfma_f32_16x16x32_bf16 a[88:91], v[208:211], v[178:181], a[88:91]\n"
"buffer_load_dwordx4 v232, s[52:55], s68 offen lds\n"
"v_accvgpr_read_b32 v170, a22\n"
"v_mfma_f32_16x16x32_bf16 a[88:91], v[212:215], v[174:177], a[88:91]\n"
"ds_read_b64_tr_b16 v[206:207], v254 offset:256\n"
"v_accvgpr_read_b32 v171, a23\n"
"v_mfma_f32_16x16x32_bf16 a[92:95], v[208:211], v[196:199], a[92:95]\n"
"ds_read_b64_tr_b16 v[204:205], v254 offset:768\n"
"v_cvt_pk_bf16_f32 v34, v168, v169\n"
"v_mfma_f32_16x16x32_bf16 a[92:95], v[212:215], v[192:195], a[92:95]\n"
"s_waitcnt lgkmcnt(4)\n"
"v_cvt_pk_bf16_f32 v35, v170, v171\n"
"v_accvgpr_read_b32 v182, a32\n"
"v_mfma_f32_16x16x32_bf16 a[96:99], v[216:219], v[142:145], a[96:99]\n"
"s_add_u32 m0, s95, 20480\n"
"ds_read_b64_tr_b16 v[208:209], v253 offset:288\n"
"v_accvgpr_read_b32 v183, a33\n"
"v_mfma_f32_16x16x32_bf16 a[96:99], v[220:223], v[138:141], a[96:99]\n"
"buffer_load_dwordx4 v232, s[52:55], s69 offen lds\n"
"v_accvgpr_read_b32 v184, a34\n"
"v_mfma_f32_16x16x32_bf16 a[100:103], v[216:219], v[160:163], a[100:103]\n"
"ds_read_b64_tr_b16 v[210:211], v253 offset:800\n"
"v_accvgpr_read_b32 v185, a35\n"
"v_mfma_f32_16x16x32_bf16 a[100:103], v[220:223], v[156:159], a[100:103]\n"
"v_cvt_pk_bf16_f32 v4, v182, v183\n"
"v_cvt_pk_bf16_f32 v5, v184, v185\n"
"v_mfma_f32_16x16x32_bf16 a[104:107], v[216:219], v[178:181], a[104:107]\n"
"ds_read_b64_tr_b16 v[214:215], v254 offset:288\n"
"v_accvgpr_read_b32 v186, a48\n"
"v_mfma_f32_16x16x32_bf16 a[104:107], v[220:223], v[174:177], a[104:107]\n"
"s_add_u32 m0, s95, 24576\n"
"v_accvgpr_read_b32 v187, a49\n"
"v_accvgpr_read_b32 v188, a50\n"
"v_mfma_f32_16x16x32_bf16 a[108:111], v[216:219], v[196:199], a[108:111]\n"
"buffer_load_dwordx4 v232, s[52:55], s70 offen lds\n"
"v_accvgpr_read_b32 v189, a51\n"
"v_mfma_f32_16x16x32_bf16 a[108:111], v[220:223], v[192:195], a[108:111]\n"
"ds_read_b64_tr_b16 v[212:213], v254 offset:800\n"
"v_cvt_pk_bf16_f32 v6, v186, v187\n"
"v_mfma_f32_16x16x32_bf16 a[112:115], v[224:227], v[142:145], a[112:115]\n"
"ds_read_b64_tr_b16 v[216:217], v253 offset:320\n"
"v_cvt_pk_bf16_f32 v7, v188, v189\n"
"v_mfma_f32_16x16x32_bf16 a[112:115], v[228:231], v[138:141], a[112:115]\n"
"v_accvgpr_read_b32 v168, a36\n"
"v_accvgpr_read_b32 v169, a37\n"
"v_mfma_f32_16x16x32_bf16 a[116:119], v[224:227], v[160:163], a[116:119]\n"
"s_add_u32 m0, s95, 28672\n"
"ds_read_b64_tr_b16 v[218:219], v253 offset:832\n"
"v_accvgpr_read_b32 v170, a38\n"
"v_mfma_f32_16x16x32_bf16 a[116:119], v[228:231], v[156:159], a[116:119]\n"
"buffer_load_dwordx4 v232, s[52:55], s71 offen lds\n"
"v_accvgpr_read_b32 v171, a39\n"
"v_mfma_f32_16x16x32_bf16 a[120:123], v[224:227], v[178:181], a[120:123]\n"
"ds_read_b64_tr_b16 v[222:223], v254 offset:320\n"
"v_cvt_pk_bf16_f32 v36, v168, v169\n"
"v_mfma_f32_16x16x32_bf16 a[120:123], v[228:231], v[174:177], a[120:123]\n"
"v_cvt_pk_bf16_f32 v37, v170, v171\n"
"v_accvgpr_read_b32 v182, a52\n"
"v_mfma_f32_16x16x32_bf16 a[124:127], v[224:227], v[196:199], a[124:127]\n"
"ds_read_b64_tr_b16 v[220:221], v254 offset:832\n"
"v_accvgpr_read_b32 v183, a53\n"
"v_mfma_f32_16x16x32_bf16 a[124:127], v[228:231], v[192:195], a[124:127]\n"
"s_waitcnt lgkmcnt(5)\n"
"s_add_u32 m0, s95, 32768\n"
"v_accvgpr_read_b32 v184, a54\n"
"v_accvgpr_read_b32 v185, a55\n"
"v_mfma_f32_16x16x32_bf16 a[128:131], v[200:203], v[142:145], a[128:131]\n"
"buffer_load_dwordx4 v232, s[52:55], s72 offen lds\n"
"v_cvt_pk_bf16_f32 v38, v182, v183\n"
"v_mfma_f32_16x16x32_bf16 a[128:131], v[204:207], v[138:141], a[128:131]\n"
"ds_read_b64_tr_b16 v[224:225], v253 offset:352\n"
"v_cvt_pk_bf16_f32 v39, v184, v185\n"
"v_mfma_f32_16x16x32_bf16 a[132:135], v[200:203], v[160:163], a[132:135]\n"
"ds_read_b64_tr_b16 v[226:227], v253 offset:864\n"
"v_accvgpr_read_b32 v186, a64\n"
"v_mfma_f32_16x16x32_bf16 a[132:135], v[204:207], v[156:159], a[132:135]\n"
"v_accvgpr_read_b32 v187, a65\n"
"v_accvgpr_read_b32 v188, a66\n"
"v_mfma_f32_16x16x32_bf16 a[136:139], v[200:203], v[178:181], a[136:139]\n"
"s_add_u32 m0, s95, 36864\n"
"ds_read_b64_tr_b16 v[230:231], v254 offset:352\n"
"v_accvgpr_read_b32 v189, a67\n"
"v_mfma_f32_16x16x32_bf16 a[136:139], v[204:207], v[174:177], a[136:139]\n"
"buffer_load_dwordx4 v232, s[52:55], s73 offen lds\n"
"v_cvt_pk_bf16_f32 v8, v186, v187\n"
"v_mfma_f32_16x16x32_bf16 a[140:143], v[200:203], v[196:199], a[140:143]\n"
"ds_read_b64_tr_b16 v[228:229], v254 offset:864\n"
"v_cvt_pk_bf16_f32 v9, v188, v189\n"
"v_mfma_f32_16x16x32_bf16 a[140:143], v[204:207], v[192:195], a[140:143]\n"
"v_accvgpr_read_b32 v168, a80\n"
"v_accvgpr_read_b32 v169, a81\n"
"v_mfma_f32_16x16x32_bf16 a[144:147], v[208:211], v[142:145], a[144:147]\n"
"s_waitcnt lgkmcnt(4)\n"
"ds_read_b64_tr_b16 v[200:201], v253 offset:384\n"
"v_accvgpr_read_b32 v170, a82\n"
"v_mfma_f32_16x16x32_bf16 a[144:147], v[212:215], v[138:141], a[144:147]\n"
"s_add_u32 m0, s95, 40960\n"
"v_accvgpr_read_b32 v171, a83\n"
"v_cvt_pk_bf16_f32 v10, v168, v169\n"
"v_mfma_f32_16x16x32_bf16 a[148:151], v[208:211], v[160:163], a[148:151]\n"
"buffer_load_dwordx4 v232, s[52:55], s74 offen lds\n"
"v_cvt_pk_bf16_f32 v11, v170, v171\n"
"v_mfma_f32_16x16x32_bf16 a[148:151], v[212:215], v[156:159], a[148:151]\n"
"ds_read_b64_tr_b16 v[202:203], v253 offset:896\n"
"v_accvgpr_read_b32 v182, a68\n"
"v_mfma_f32_16x16x32_bf16 a[152:155], v[208:211], v[178:181], a[152:155]\n"
"ds_read_b64_tr_b16 v[206:207], v254 offset:384\n"
"v_accvgpr_read_b32 v183, a69\n"
"v_mfma_f32_16x16x32_bf16 a[152:155], v[212:215], v[174:177], a[152:155]\n"
"v_accvgpr_read_b32 v184, a70\n"
"v_accvgpr_read_b32 v185, a71\n"
"v_mfma_f32_16x16x32_bf16 a[156:159], v[208:211], v[196:199], a[156:159]\n"
"s_add_u32 m0, s95, 45056\n"
"ds_read_b64_tr_b16 v[204:205], v254 offset:896\n"
"v_cvt_pk_bf16_f32 v40, v182, v183\n"
"v_mfma_f32_16x16x32_bf16 a[156:159], v[212:215], v[192:195], a[156:159]\n"
"buffer_load_dwordx4 v232, s[52:55], s75 offen lds\n"
"v_cvt_pk_bf16_f32 v41, v184, v185\n"
"v_mfma_f32_16x16x32_bf16 a[160:163], v[216:219], v[142:145], a[160:163]\n"
"ds_read_b64_tr_b16 v[208:209], v253 offset:416\n"
"v_accvgpr_read_b32 v186, a84\n"
"v_mfma_f32_16x16x32_bf16 a[160:163], v[220:223], v[138:141], a[160:163]\n"
"v_accvgpr_read_b32 v187, a85\n"
"v_accvgpr_read_b32 v188, a86\n"
"v_mfma_f32_16x16x32_bf16 a[164:167], v[216:219], v[160:163], a[164:167]\n"
"ds_read_b64_tr_b16 v[210:211], v253 offset:928\n"
"v_accvgpr_read_b32 v189, a87\n"
"v_mfma_f32_16x16x32_bf16 a[164:167], v[220:223], v[156:159], a[164:167]\n"
"s_add_u32 m0, s95, 49152\n"
"v_cvt_pk_bf16_f32 v42, v186, v187\n"
"v_cvt_pk_bf16_f32 v43, v188, v189\n"
"v_mfma_f32_16x16x32_bf16 a[168:171], v[216:219], v[178:181], a[168:171]\n"
"buffer_load_dwordx4 v232, s[52:55], s76 offen lds\n"
"v_accvgpr_read_b32 v168, a96\n"
"v_mfma_f32_16x16x32_bf16 a[168:171], v[220:223], v[174:177], a[168:171]\n"
"ds_read_b64_tr_b16 v[214:215], v254 offset:416\n"
"v_accvgpr_read_b32 v169, a97\n"
"v_mfma_f32_16x16x32_bf16 a[172:175], v[216:219], v[196:199], a[172:175]\n"
"ds_read_b64_tr_b16 v[212:213], v254 offset:928\n"
"v_accvgpr_read_b32 v170, a98\n"
"v_mfma_f32_16x16x32_bf16 a[172:175], v[220:223], v[192:195], a[172:175]\n"
"s_waitcnt lgkmcnt(4)\n"
"v_accvgpr_read_b32 v171, a99\n"
"v_cvt_pk_bf16_f32 v12, v168, v169\n"
"v_mfma_f32_16x16x32_bf16 a[176:179], v[224:227], v[142:145], a[176:179]\n"
"s_add_u32 m0, s95, 53248\n"
"ds_read_b64_tr_b16 v[216:217], v253 offset:448\n"
"v_cvt_pk_bf16_f32 v13, v170, v171\n"
"v_mfma_f32_16x16x32_bf16 a[176:179], v[228:231], v[138:141], a[176:179]\n"
"buffer_load_dwordx4 v232, s[52:55], s77 offen lds\n"
"v_accvgpr_read_b32 v182, a112\n"
"v_mfma_f32_16x16x32_bf16 a[180:183], v[224:227], v[160:163], a[180:183]\n"
"ds_read_b64_tr_b16 v[218:219], v253 offset:960\n"
"v_accvgpr_read_b32 v183, a113\n"
"v_mfma_f32_16x16x32_bf16 a[180:183], v[228:231], v[156:159], a[180:183]\n"
"v_accvgpr_read_b32 v184, a114\n"
"v_accvgpr_read_b32 v185, a115\n"
"v_mfma_f32_16x16x32_bf16 a[184:187], v[224:227], v[178:181], a[184:187]\n"
"ds_read_b64_tr_b16 v[222:223], v254 offset:448\n"
"v_cvt_pk_bf16_f32 v14, v182, v183\n"
"v_mfma_f32_16x16x32_bf16 a[184:187], v[228:231], v[174:177], a[184:187]\n"
"s_add_u32 m0, s95, 57344\n"
"v_cvt_pk_bf16_f32 v15, v184, v185\n"
"v_accvgpr_read_b32 v186, a100\n"
"v_mfma_f32_16x16x32_bf16 a[188:191], v[224:227], v[196:199], a[188:191]\n"
"buffer_load_dwordx4 v232, s[52:55], s78 offen lds\n"
"v_accvgpr_read_b32 v187, a101\n"
"v_mfma_f32_16x16x32_bf16 a[188:191], v[228:231], v[192:195], a[188:191]\n"
"ds_read_b64_tr_b16 v[220:221], v254 offset:960\n"
"v_accvgpr_read_b32 v188, a102\n"
"v_mfma_f32_16x16x32_bf16 a[192:195], v[200:203], v[142:145], a[192:195]\n"
"ds_read_b64_tr_b16 v[224:225], v253 offset:480\n"
"v_accvgpr_read_b32 v189, a103\n"
"v_mfma_f32_16x16x32_bf16 a[192:195], v[204:207], v[138:141], a[192:195]\n"
"v_cvt_pk_bf16_f32 v44, v186, v187\n"
"v_cvt_pk_bf16_f32 v45, v188, v189\n"
"v_mfma_f32_16x16x32_bf16 a[196:199], v[200:203], v[160:163], a[196:199]\n"
"s_add_u32 m0, s95, 61440\n"
"ds_read_b64_tr_b16 v[226:227], v253 offset:992\n"
"v_accvgpr_read_b32 v168, a116\n"
"v_mfma_f32_16x16x32_bf16 a[196:199], v[204:207], v[156:159], a[196:199]\n"
"buffer_load_dwordx4 v232, s[52:55], s79 offen lds\n"
"v_accvgpr_read_b32 v169, a117\n"
"v_mfma_f32_16x16x32_bf16 a[200:203], v[200:203], v[178:181], a[200:203]\n"
"ds_read_b64_tr_b16 v[230:231], v254 offset:480\n"
"v_accvgpr_read_b32 v170, a118\n"
"v_mfma_f32_16x16x32_bf16 a[200:203], v[204:207], v[174:177], a[200:203]\n"
"v_accvgpr_read_b32 v171, a119\n"
"v_cvt_pk_bf16_f32 v46, v168, v169\n"
"v_mfma_f32_16x16x32_bf16 a[204:207], v[200:203], v[196:199], a[204:207]\n"
"s_waitcnt vmcnt(8)\n"
"ds_read_b64_tr_b16 v[228:229], v254 offset:992\n"
"v_cvt_pk_bf16_f32 v47, v170, v171\n"
"v_mfma_f32_16x16x32_bf16 a[204:207], v[204:207], v[192:195], a[204:207]\n"
"s_barrier\n"
"v_accvgpr_read_b32 v182, a128\n"
"v_accvgpr_read_b32 v183, a129\n"
"s_waitcnt lgkmcnt(5)\n"
"v_mfma_f32_16x16x32_bf16 a[208:211], v[208:211], v[142:145], a[208:211]\n"
"s_cmp_ge_u32 s84, 768\n"
"ds_read_b64_tr_b16 a[0:1], v236 offset:0\n"
"v_accvgpr_read_b32 v184, a130\n"
"v_mfma_f32_16x16x32_bf16 a[208:211], v[212:215], v[138:141], a[208:211]\n"
"s_cselect_b32 s86, -896, 0\n"
"ds_read_b64_tr_b16 a[2:3], v236 offset:2048\n"
"v_accvgpr_read_b32 v185, a131\n"
"v_mfma_f32_16x16x32_bf16 a[212:215], v[208:211], v[160:163], a[212:215]\n"
"s_add_u32 s86, s86, 128\n"
"ds_read_b64_tr_b16 a[4:5], v236 offset:4096\n"
"v_cvt_pk_bf16_f32 v16, v182, v183\n"
"v_mfma_f32_16x16x32_bf16 a[212:215], v[212:215], v[156:159], a[212:215]\n"
"s_add_u32 s84, s84, s86\n"
"ds_read_b64_tr_b16 a[6:7], v236 offset:6144\n"
"v_add_u32 v232, s86, v232\n"
"v_mfma_f32_16x16x32_bf16 a[216:219], v[208:211], v[178:181], a[216:219]\n"
"s_add_u32 m0, s95, 98304\n"
"ds_read_b64_tr_b16 a[16:17], v236 offset:8192\n"
"v_cvt_pk_bf16_f32 v17, v184, v185\n"
"v_mfma_f32_16x16x32_bf16 a[216:219], v[212:215], v[174:177], a[216:219]\n"
"buffer_load_dwordx4 v232, s[52:55], s64 offen lds\n"
"v_accvgpr_read_b32 v186, a144\n"
"v_mfma_f32_16x16x32_bf16 a[220:223], v[208:211], v[196:199], a[220:223]\n"
"ds_read_b64_tr_b16 a[18:19], v236 offset:10240\n"
"ds_read_b64_tr_b16 a[20:21], v236 offset:12288\n"
"v_mfma_f32_16x16x32_bf16 a[220:223], v[212:215], v[192:195], a[220:223]\n"
"ds_read_b64_tr_b16 a[22:23], v236 offset:14336\n"
"v_accvgpr_read_b32 v187, a145\n"
"v_mfma_f32_16x16x32_bf16 a[224:227], v[216:219], v[142:145], a[224:227]\n"
"s_waitcnt lgkmcnt(8)\n"
"ds_read_b64_tr_b16 a[32:33], v236 offset:16384\n"
"v_accvgpr_read_b32 v188, a146\n"
"v_mfma_f32_16x16x32_bf16 a[224:227], v[220:223], v[138:141], a[224:227]\n"
"s_add_u32 m0, s95, 102400\n"
"ds_read_b64_tr_b16 a[34:35], v236 offset:18432\n"
"v_accvgpr_read_b32 v189, a147\n"
"v_mfma_f32_16x16x32_bf16 a[228:231], v[216:219], v[160:163], a[228:231]\n"
"buffer_load_dwordx4 v232, s[52:55], s65 offen lds\n"
"v_cvt_pk_bf16_f32 v18, v186, v187\n"
"v_mfma_f32_16x16x32_bf16 a[228:231], v[220:223], v[156:159], a[228:231]\n"
"ds_read_b64_tr_b16 a[36:37], v236 offset:20480\n"
"ds_read_b64_tr_b16 a[38:39], v236 offset:22528\n"
"v_mfma_f32_16x16x32_bf16 a[232:235], v[216:219], v[178:181], a[232:235]\n"
"ds_read_b64_tr_b16 a[48:49], v236 offset:24576\n"
"v_cvt_pk_bf16_f32 v19, v188, v189\n"
"v_mfma_f32_16x16x32_bf16 a[232:235], v[220:223], v[174:177], a[232:235]\n"
"ds_read_b64_tr_b16 a[50:51], v236 offset:26624\n"
"v_accvgpr_read_b32 v168, a132\n"
"v_mfma_f32_16x16x32_bf16 a[236:239], v[216:219], v[196:199], a[236:239]\n"
"s_add_u32 m0, s95, 106496\n"
"ds_read_b64_tr_b16 a[52:53], v236 offset:28672\n"
"v_accvgpr_read_b32 v169, a133\n"
"v_mfma_f32_16x16x32_bf16 a[236:239], v[220:223], v[192:195], a[236:239]\n"
"buffer_load_dwordx4 v232, s[52:55], s66 offen lds\n"
"v_accvgpr_read_b32 v170, a134\n"
"v_mfma_f32_16x16x32_bf16 a[240:243], v[224:227], v[142:145], a[240:243]\n"
"ds_read_b64_tr_b16 a[54:55], v236 offset:30720\n"
"ds_read_b64_tr_b16 a[64:65], v237 offset:0\n"
"v_mfma_f32_16x16x32_bf16 a[240:243], v[228:231], v[138:141], a[240:243]\n"
"ds_read_b64_tr_b16 a[66:67], v237 offset:2048\n"
"v_accvgpr_read_b32 v171, a135\n"
"v_mfma_f32_16x16x32_bf16 a[244:247], v[224:227], v[160:163], a[244:247]\n"
"ds_read_b64_tr_b16 a[68:69], v237 offset:4096\n"
"v_cvt_pk_bf16_f32 v48, v168, v169\n"
"v_mfma_f32_16x16x32_bf16 a[244:247], v[228:231], v[156:159], a[244:247]\n"
"s_add_u32 m0, s95, 110592\n"
"ds_read_b64_tr_b16 a[70:71], v237 offset:6144\n"
"v_cvt_pk_bf16_f32 v49, v170, v171\n"
"v_mfma_f32_16x16x32_bf16 a[248:251], v[224:227], v[178:181], a[248:251]\n"
"buffer_load_dwordx4 v232, s[52:55], s67 offen lds\n"
"v_accvgpr_read_b32 v182, a148\n"
"v_mfma_f32_16x16x32_bf16 a[248:251], v[228:231], v[174:177], a[248:251]\n"
"ds_read_b64_tr_b16 a[80:81], v237 offset:8192\n"
"ds_read_b64_tr_b16 a[82:83], v237 offset:10240\n"
"v_mfma_f32_16x16x32_bf16 a[252:255], v[224:227], v[196:199], a[252:255]\n"
"ds_read_b64_tr_b16 a[84:85], v237 offset:12288\n"
"v_accvgpr_read_b32 v183, a149\n"
"v_mfma_f32_16x16x32_bf16 a[252:255], v[228:231], v[192:195], a[252:255]\n"
"s_waitcnt lgkmcnt(8)\n"
"ds_read_b64_tr_b16 a[86:87], v237 offset:14336\n"
"v_accvgpr_read_b32 v184, a150\n"
"v_mfma_f32_16x16x32_bf16 v[128:131], a[0:3], v[0:3], 0\n"
"s_add_u32 m0, s95, 114688\n"
"ds_read_b64_tr_b16 a[96:97], v237 offset:16384\n"
"v_accvgpr_read_b32 v185, a151\n"
"v_mfma_f32_16x16x32_bf16 v[128:131], a[4:7], v[4:7], v[128:131]\n"
"buffer_load_dwordx4 v232, s[52:55], s68 offen lds\n"
"v_cvt_pk_bf16_f32 v50, v182, v183\n"
"v_mfma_f32_16x16x32_bf16 v[128:131], a[16:19], v[8:11], v[128:131]\n"
"ds_read_b64_tr_b16 a[98:99], v237 offset:18432\n"
"ds_read_b64_tr_b16 a[100:101], v237 offset:20480\n"
"v_cvt_pk_bf16_f32 v51, v184, v185\n"
"v_mfma_f32_16x16x32_bf16 v[128:131], a[20:23], v[12:15], v[128:131]\n"
"ds_read_b64_tr_b16 a[102:103], v237 offset:22528\n"
"v_accvgpr_read_b32 v186, a160\n"
"v_accvgpr_read_b32 v187, a161\n"
"v_accvgpr_read_b32 v188, a162\n"
"v_accvgpr_read_b32 v189, a163\n"
"v_cvt_pk_bf16_f32 v20, v186, v187\n"
"v_cvt_pk_bf16_f32 v21, v188, v189\n"
"v_accvgpr_read_b32 v168, a176\n"
"v_accvgpr_read_b32 v169, a177\n"
"v_accvgpr_read_b32 v170, a178\n"
"v_accvgpr_read_b32 v171, a179\n"
"v_cvt_pk_bf16_f32 v22, v168, v169\n"
"v_cvt_pk_bf16_f32 v23, v170, v171\n"
"v_accvgpr_read_b32 v182, a164\n"
"v_accvgpr_read_b32 v183, a165\n"
"v_accvgpr_read_b32 v184, a166\n"
"v_accvgpr_read_b32 v185, a167\n"
"v_cvt_pk_bf16_f32 v52, v182, v183\n"
"v_cvt_pk_bf16_f32 v53, v184, v185\n"
"v_accvgpr_read_b32 v186, a180\n"
"v_accvgpr_read_b32 v187, a181\n"
"v_accvgpr_read_b32 v188, a182\n"
"v_accvgpr_read_b32 v189, a183\n"
"v_cvt_pk_bf16_f32 v54, v186, v187\n"
"v_cvt_pk_bf16_f32 v55, v188, v189\n"
"v_mfma_f32_16x16x32_bf16 v[128:131], a[32:35], v[16:19], v[128:131]\n"
"ds_read_b64_tr_b16 a[112:113], v237 offset:24576\n"
"v_accvgpr_read_b32 v168, a192\n"
"v_accvgpr_read_b32 v169, a193\n"
"v_accvgpr_read_b32 v170, a194\n"
"v_accvgpr_read_b32 v171, a195\n"
"v_cvt_pk_bf16_f32 v24, v168, v169\n"
"v_cvt_pk_bf16_f32 v25, v170, v171\n"
"v_accvgpr_read_b32 v182, a208\n"
"v_accvgpr_read_b32 v183, a209\n"
"v_accvgpr_read_b32 v184, a210\n"
"v_accvgpr_read_b32 v185, a211\n"
"v_cvt_pk_bf16_f32 v26, v182, v183\n"
"v_cvt_pk_bf16_f32 v27, v184, v185\n"
"v_accvgpr_read_b32 v186, a196\n"
"v_accvgpr_read_b32 v187, a197\n"
"v_accvgpr_read_b32 v188, a198\n"
"v_accvgpr_read_b32 v189, a199\n"
"v_cvt_pk_bf16_f32 v56, v186, v187\n"
"v_cvt_pk_bf16_f32 v57, v188, v189\n"
"v_accvgpr_read_b32 v168, a212\n"
"v_accvgpr_read_b32 v169, a213\n"
"v_accvgpr_read_b32 v170, a214\n"
"v_accvgpr_read_b32 v171, a215\n"
"v_cvt_pk_bf16_f32 v58, v168, v169\n"
"v_cvt_pk_bf16_f32 v59, v170, v171\n"
"v_mfma_f32_16x16x32_bf16 v[128:131], a[36:39], v[20:23], v[128:131]\n"
"s_add_u32 m0, s95, 118784\n"
"ds_read_b64_tr_b16 a[114:115], v237 offset:26624\n"
"v_accvgpr_read_b32 v182, a224\n"
"v_accvgpr_read_b32 v183, a225\n"
"v_accvgpr_read_b32 v184, a226\n"
"v_accvgpr_read_b32 v185, a227\n"
"v_cvt_pk_bf16_f32 v28, v182, v183\n"
"v_cvt_pk_bf16_f32 v29, v184, v185\n"
"v_accvgpr_read_b32 v186, a240\n"
"v_accvgpr_read_b32 v187, a241\n"
"v_accvgpr_read_b32 v188, a242\n"
"v_accvgpr_read_b32 v189, a243\n"
"v_cvt_pk_bf16_f32 v30, v186, v187\n"
"v_cvt_pk_bf16_f32 v31, v188, v189\n"
"v_accvgpr_read_b32 v168, a228\n"
"v_accvgpr_read_b32 v169, a229\n"
"v_accvgpr_read_b32 v170, a230\n"
"v_accvgpr_read_b32 v171, a231\n"
"v_cvt_pk_bf16_f32 v60, v168, v169\n"
"v_cvt_pk_bf16_f32 v61, v170, v171\n"
"v_accvgpr_read_b32 v182, a244\n"
"v_accvgpr_read_b32 v183, a245\n"
"v_accvgpr_read_b32 v184, a246\n"
"v_accvgpr_read_b32 v185, a247\n"
"v_cvt_pk_bf16_f32 v62, v182, v183\n"
"v_cvt_pk_bf16_f32 v63, v184, v185\n"
"v_mfma_f32_16x16x32_bf16 v[128:131], a[48:51], v[24:27], v[128:131]\n"
"s_waitcnt lgkmcnt(8)\n"
"buffer_load_dwordx4 v232, s[52:55], s69 offen lds\n"
"v_accvgpr_read_b32 v224, a8\n"
"v_mfma_f32_16x16x32_bf16 v[128:131], a[52:55], v[28:31], v[128:131]\n"
"ds_read_b64_tr_b16 a[116:117], v237 offset:28672\n"
"ds_read_b64_tr_b16 a[118:119], v237 offset:30720\n"
"v_mfma_f32_16x16x32_bf16 v[132:135], a[0:3], v[32:35], 0\n"
"ds_read_b64_tr_b16 a[128:129], v238 offset:0\n"
"v_accvgpr_read_b32 v225, a9\n"
"v_mfma_f32_16x16x32_bf16 v[132:135], a[4:7], v[36:39], v[132:135]\n"
"ds_read_b64_tr_b16 a[130:131], v238 offset:2048\n"
"v_accvgpr_read_b32 v226, a10\n"
"v_mfma_f32_16x16x32_bf16 v[132:135], a[16:19], v[40:43], v[132:135]\n"
"s_add_u32 m0, s95, 122880\n"
"ds_read_b64_tr_b16 a[132:133], v238 offset:4096\n"
"v_accvgpr_read_b32 v227, a11\n"
"v_mfma_f32_16x16x32_bf16 v[132:135], a[20:23], v[44:47], v[132:135]\n"
"buffer_load_dwordx4 v232, s[52:55], s70 offen lds\n"
"v_mul_f32 v224, v242, v224\n"
"v_mfma_f32_16x16x32_bf16 v[132:135], a[32:35], v[48:51], v[132:135]\n"
"ds_read_b64_tr_b16 a[134:135], v238 offset:6144\n"
"ds_read_b64_tr_b16 a[144:145], v238 offset:8192\n"
"v_mfma_f32_16x16x32_bf16 v[132:135], a[36:39], v[52:55], v[132:135]\n"
"ds_read_b64_tr_b16 a[146:147], v238 offset:10240\n"
"v_mul_f32 v225, v242, v225\n"
"v_mfma_f32_16x16x32_bf16 v[132:135], a[48:51], v[56:59], v[132:135]\n"
"ds_read_b64_tr_b16 a[148:149], v238 offset:12288\n"
"v_mul_f32 v226, v242, v226\n"
"v_mfma_f32_16x16x32_bf16 v[132:135], a[52:55], v[60:63], v[132:135]\n"
"s_add_u32 m0, s95, 126976\n"
"ds_read_b64_tr_b16 a[150:151], v238 offset:14336\n"
"v_mul_f32 v227, v242, v227\n"
"v_mfma_f32_16x16x32_bf16 v[136:139], a[64:67], v[0:3], 0\n"
"buffer_load_dwordx4 v232, s[52:55], s71 offen lds\n"
"v_cvt_pk_bf16_f32 v64, v224, v225\n"
"v_mfma_f32_16x16x32_bf16 v[136:139], a[68:71], v[4:7], v[136:139]\n"
"ds_read_b64_tr_b16 a[160:161], v238 offset:16384\n"
"ds_read_b64_tr_b16 a[162:163], v238 offset:18432\n"
"v_mfma_f32_16x16x32_bf16 v[136:139], a[80:83], v[8:11], v[136:139]\n"
"s_waitcnt lgkmcnt(8)\n"
"ds_read_b64_tr_b16 a[164:165], v238 offset:20480\n"
"v_cvt_pk_bf16_f32 v65, v226, v227\n"
"v_mfma_f32_16x16x32_bf16 v[136:139], a[84:87], v[12:15], v[136:139]\n"
"ds_read_b64_tr_b16 a[166:167], v238 offset:22528\n"
"v_accvgpr_read_b32 v228, a24\n"
"v_mfma_f32_16x16x32_bf16 v[136:139], a[96:99], v[16:19], v[136:139]\n"
"ds_read_b64_tr_b16 a[176:177], v238 offset:24576\n"
"v_accvgpr_read_b32 v229, a25\n"
"v_mfma_f32_16x16x32_bf16 v[136:139], a[100:103], v[20:23], v[136:139]\n"
"ds_read_b64_tr_b16 a[178:179], v238 offset:26624\n"
"v_accvgpr_read_b32 v230, a26\n"
"v_mfma_f32_16x16x32_bf16 v[136:139], a[112:115], v[24:27], v[136:139]\n"
"ds_read_b64_tr_b16 a[180:181], v238 offset:28672\n"
"v_accvgpr_read_b32 v231, a27\n"
"v_mfma_f32_16x16x32_bf16 v[136:139], a[116:119], v[28:31], v[136:139]\n"
"ds_read_b64_tr_b16 a[182:183], v238 offset:30720\n"
"v_mul_f32 v228, v242, v228\n"
"v_mfma_f32_16x16x32_bf16 v[140:143], a[64:67], v[32:35], 0\n"
"ds_read_b64_tr_b16 a[192:193], v239 offset:0\n"
"v_mul_f32 v229, v242, v229\n"
"v_mfma_f32_16x16x32_bf16 v[140:143], a[68:71], v[36:39], v[140:143]\n"
"ds_read_b64_tr_b16 a[194:195], v239 offset:2048\n"
"v_mul_f32 v230, v242, v230\n"
"v_mfma_f32_16x16x32_bf16 v[140:143], a[80:83], v[40:43], v[140:143]\n"
"ds_read_b64_tr_b16 a[196:197], v239 offset:4096\n"
"v_mul_f32 v231, v242, v231\n"
"v_mfma_f32_16x16x32_bf16 v[140:143], a[84:87], v[44:47], v[140:143]\n"
"ds_read_b64_tr_b16 a[198:199], v239 offset:6144\n"
"v_cvt_pk_bf16_f32 v66, v228, v229\n"
"v_mfma_f32_16x16x32_bf16 v[140:143], a[96:99], v[48:51], v[140:143]\n"
"ds_read_b64_tr_b16 a[208:209], v239 offset:8192\n"
"v_cvt_pk_bf16_f32 v67, v230, v231\n"
"v_mfma_f32_16x16x32_bf16 v[140:143], a[100:103], v[52:55], v[140:143]\n"
"ds_read_b64_tr_b16 a[210:211], v239 offset:10240\n"
"v_accvgpr_read_b32 v224, a12\n"
"v_mfma_f32_16x16x32_bf16 v[140:143], a[112:115], v[56:59], v[140:143]\n"
"ds_read_b64_tr_b16 a[212:213], v239 offset:12288\n"
"v_accvgpr_read_b32 v225, a13\n"
"v_mfma_f32_16x16x32_bf16 v[140:143], a[116:119], v[60:63], v[140:143]\n"
"ds_read_b64_tr_b16 a[214:215], v239 offset:14336\n"
"v_accvgpr_read_b32 v226, a14\n"
"v_mfma_f32_16x16x32_bf16 v[144:147], a[128:131], v[0:3], 0\n"
"s_waitcnt lgkmcnt(8)\n"
"ds_read_b64_tr_b16 a[224:225], v239 offset:16384\n"
"v_accvgpr_read_b32 v227, a15\n"
"v_mfma_f32_16x16x32_bf16 v[144:147], a[132:135], v[4:7], v[144:147]\n"
"ds_read_b64_tr_b16 a[226:227], v239 offset:18432\n"
"v_mul_f32 v224, v243, v224\n"
"v_mfma_f32_16x16x32_bf16 v[144:147], a[144:147], v[8:11], v[144:147]\n"
"ds_read_b64_tr_b16 a[228:229], v239 offset:20480\n"
"v_mul_f32 v225, v243, v225\n"
"v_mfma_f32_16x16x32_bf16 v[144:147], a[148:151], v[12:15], v[144:147]\n"
"ds_read_b64_tr_b16 a[230:231], v239 offset:22528\n"
"v_mul_f32 v226, v243, v226\n"
"v_mfma_f32_16x16x32_bf16 v[144:147], a[160:163], v[16:19], v[144:147]\n"
"ds_read_b64_tr_b16 a[240:241], v239 offset:24576\n"
"v_mul_f32 v227, v243, v227\n"
"v_mfma_f32_16x16x32_bf16 v[144:147], a[164:167], v[20:23], v[144:147]\n"
"ds_read_b64_tr_b16 a[242:243], v239 offset:26624\n"
"v_cvt_pk_bf16_f32 v96, v224, v225\n"
"v_mfma_f32_16x16x32_bf16 v[144:147], a[176:179], v[24:27], v[144:147]\n"
"s_waitcnt vmcnt(8)\n"
"ds_read_b64_tr_b16 a[244:245], v239 offset:28672\n"
"v_cvt_pk_bf16_f32 v97, v226, v227\n"
"v_mfma_f32_16x16x32_bf16 v[144:147], a[180:183], v[28:31], v[144:147]\n"
"s_barrier\n"
"ds_read_b64_tr_b16 a[246:247], v239 offset:30720\n"
"v_accvgpr_read_b32 v228, a28\n"
"v_mfma_f32_16x16x32_bf16 v[148:151], a[128:131], v[32:35], 0\n"
"s_add_u32 m0, s95, 65536\n"
"ds_read_b64_tr_b16 a[0:1], v236 offset:32768\n"
"v_accvgpr_read_b32 v229, a29\n"
"v_mfma_f32_16x16x32_bf16 v[148:151], a[132:135], v[36:39], v[148:151]\n"
"buffer_load_dwordx4 v232, s[52:55], s72 offen lds\n"
"v_accvgpr_read_b32 v230, a30\n"
"v_mfma_f32_16x16x32_bf16 v[148:151], a[144:147], v[40:43], v[148:151]\n"
"ds_read_b64_tr_b16 a[2:3], v236 offset:34816\n"
"ds_read_b64_tr_b16 a[4:5], v236 offset:36864\n"
"v_mfma_f32_16x16x32_bf16 v[148:151], a[148:151], v[44:47], v[148:151]\n"
"ds_read_b64_tr_b16 a[6:7], v236 offset:38912\n"
"v_accvgpr_read_b32 v231, a31\n"
"v_mfma_f32_16x16x32_bf16 v[148:151], a[160:163], v[48:51], v[148:151]\n"
"s_add_u32 m0, s95, 69632\n"
"ds_read_b64_tr_b16 a[16:17], v236 offset:40960\n"
"v_mul_f32 v228, v243, v228\n"
"v_mfma_f32_16x16x32_bf16 v[148:151], a[164:167], v[52:55], v[148:151]\n"
"buffer_load_dwordx4 v232, s[52:55], s73 offen lds\n"
"v_mul_f32 v229, v243, v229\n"
"v_mfma_f32_16x16x32_bf16 v[148:151], a[176:179], v[56:59], v[148:151]\n"
"ds_read_b64_tr_b16 a[18:19], v236 offset:43008\n"
"ds_read_b64_tr_b16 a[20:21], v236 offset:45056\n"
"v_mfma_f32_16x16x32_bf16 v[148:151], a[180:183], v[60:63], v[148:151]\n"
"s_waitcnt lgkmcnt(8)\n"
"ds_read_b64_tr_b16 a[22:23], v236 offset:47104\n"
"v_mul_f32 v230, v243, v230\n"
"v_mfma_f32_16x16x32_bf16 v[152:155], a[192:195], v[0:3], 0\n"
"s_add_u32 m0, s95, 73728\n"
"ds_read_b64_tr_b16 a[32:33], v236 offset:49152\n"
"v_mul_f32 v231, v243, v231\n"
"v_mfma_f32_16x16x32_bf16 v[152:155], a[196:199], v[4:7], v[152:155]\n"
"buffer_load_dwordx4 v232, s[52:55], s74 offen lds\n"
"v_cvt_pk_bf16_f32 v98, v228, v229\n"
"v_mfma_f32_16x16x32_bf16 v[152:155], a[208:211], v[8:11], v[152:155]\n"
"ds_read_b64_tr_b16 a[34:35], v236 offset:51200\n"
"ds_read_b64_tr_b16 a[36:37], v236 offset:53248\n"
"v_mfma_f32_16x16x32_bf16 v[152:155], a[212:215], v[12:15], v[152:155]\n"
"ds_read_b64_tr_b16 a[38:39], v236 offset:55296\n"
"v_cvt_pk_bf16_f32 v99, v230, v231\n"
"v_mfma_f32_16x16x32_bf16 v[152:155], a[224:227], v[16:19], v[152:155]\n"
"s_add_u32 m0, s95, 77824\n"
"ds_read_b64_tr_b16 a[48:49], v236 offset:57344\n"
"v_accvgpr_read_b32 v224, a40\n"
"v_mfma_f32_16x16x32_bf16 v[152:155], a[228:231], v[20:23], v[152:155]\n"
"buffer_load_dwordx4 v232, s[52:55], s75 offen lds\n"
"v_accvgpr_read_b32 v225, a41\n"
"v_mfma_f32_16x16x32_bf16 v[152:155], a[240:243], v[24:27], v[152:155]\n"
"s_waitcnt lgkmcnt(8)\n"
"ds_read_b64_tr_b16 a[50:51], v236 offset:59392\n"
"ds_read_b64_tr_b16 a[52:53], v236 offset:61440\n"
"v_mfma_f32_16x16x32_bf16 v[152:155], a[244:247], v[28:31], v[152:155]\n"
"ds_read_b64_tr_b16 a[54:55], v236 offset:63488\n"
"v_mul_f32 v128, v240, v128\n"
"v_mfma_f32_16x16x32_bf16 v[156:159], a[192:195], v[32:35], 0\n"
"s_add_u32 m0, s95, 81920\n"
"ds_read_b64_tr_b16 a[64:65], v237 offset:32768\n"
"v_add_u32 v236, 65536, v236\n"
"v_mfma_f32_16x16x32_bf16 v[156:159], a[196:199], v[36:39], v[156:159]\n"
"buffer_load_dwordx4 v232, s[52:55], s76 offen lds\n"
"v_mul_f32 v129, v240, v129\n"
"v_mfma_f32_16x16x32_bf16 v[156:159], a[208:211], v[40:43], v[156:159]\n"
"ds_read_b64_tr_b16 a[66:67], v237 offset:34816\n"
"ds_read_b64_tr_b16 a[68:69], v237 offset:36864\n"
"v_mfma_f32_16x16x32_bf16 v[156:159], a[212:215], v[44:47], v[156:159]\n"
"ds_read_b64_tr_b16 a[70:71], v237 offset:38912\n"
"v_mul_f32 v130, v240, v130\n"
"v_mfma_f32_16x16x32_bf16 v[156:159], a[224:227], v[48:51], v[156:159]\n"
"s_add_u32 m0, s95, 86016\n"
"ds_read_b64_tr_b16 a[80:81], v237 offset:40960\n"
"v_mul_f32 v131, v240, v131\n"
"v_mfma_f32_16x16x32_bf16 v[156:159], a[228:231], v[52:55], v[156:159]\n"
"buffer_load_dwordx4 v232, s[52:55], s77 offen lds\n"
"v_accvgpr_read_b32 v226, a42\n"
"v_mfma_f32_16x16x32_bf16 v[156:159], a[240:243], v[56:59], v[156:159]\n"
"ds_read_b64_tr_b16 a[82:83], v237 offset:43008\n"
"ds_read_b64_tr_b16 a[84:85], v237 offset:45056\n"
"v_mfma_f32_16x16x32_bf16 v[156:159], a[244:247], v[60:63], v[156:159]\n"
"ds_read_b64_tr_b16 a[86:87], v237 offset:47104\n"
"v_mul_f32 v132, v241, v132\n"
"v_accvgpr_read_b32 v227, a43\n"
"v_mul_f32 v224, v242, v224\n"
"v_mul_f32 v225, v242, v225\n"
"v_mul_f32 v226, v242, v226\n"
"v_mul_f32 v227, v242, v227\n"
"v_cvt_pk_bf16_f32 v68, v224, v225\n"
"v_cvt_pk_bf16_f32 v69, v226, v227\n"
"v_accvgpr_read_b32 v228, a56\n"
"v_accvgpr_read_b32 v229, a57\n"
"v_accvgpr_read_b32 v230, a58\n"
"v_accvgpr_read_b32 v231, a59\n"
"v_mul_f32 v228, v242, v228\n"
"v_mul_f32 v229, v242, v229\n"
"v_mul_f32 v230, v242, v230\n"
"v_mul_f32 v231, v242, v231\n"
"v_cvt_pk_bf16_f32 v70, v228, v229\n"
"v_cvt_pk_bf16_f32 v71, v230, v231\n"
"v_accvgpr_read_b32 v224, a44\n"
"v_accvgpr_read_b32 v225, a45\n"
"v_accvgpr_read_b32 v226, a46\n"
"v_accvgpr_read_b32 v227, a47\n"
"v_mul_f32 v224, v243, v224\n"
"v_mul_f32 v225, v243, v225\n"
"v_mul_f32 v226, v243, v226\n"
"v_mul_f32 v227, v243, v227\n"
"v_cvt_pk_bf16_f32 v100, v224, v225\n"
"v_cvt_pk_bf16_f32 v101, v226, v227\n"
"v_accvgpr_read_b32 v228, a60\n"
"v_accvgpr_read_b32 v229, a61\n"
"v_accvgpr_read_b32 v230, a62\n"
"v_accvgpr_read_b32 v231, a63\n"
"v_mul_f32 v228, v243, v228\n"
"v_mul_f32 v229, v243, v229\n"
"v_mul_f32 v230, v243, v230\n"
"v_mul_f32 v231, v243, v231\n"
"v_cvt_pk_bf16_f32 v102, v228, v229\n"
"v_cvt_pk_bf16_f32 v103, v230, v231\n"
"v_mfma_f32_16x16x32_bf16 v[128:131], a[0:3], v[64:67], v[128:131]\n"
"s_add_u32 m0, s95, 90112\n"
"ds_read_b64_tr_b16 a[96:97], v237 offset:49152\n"
"v_mul_f32 v133, v241, v133\n"
"v_accvgpr_read_b32 v224, a72\n"
"v_accvgpr_read_b32 v225, a73\n"
"v_accvgpr_read_b32 v226, a74\n"
"v_accvgpr_read_b32 v227, a75\n"
"v_pk_mul_f32 v[224:225], v[224:225], v[242:243] op_sel:[0,0] op_sel_hi:[1,0]\n"
"v_pk_mul_f32 v[226:227], v[226:227], v[242:243] op_sel:[0,0] op_sel_hi:[1,0]\n"
"v_cvt_pk_bf16_f32 v72, v224, v225\n"
"v_cvt_pk_bf16_f32 v73, v226, v227\n"
"v_accvgpr_read_b32 v228, a88\n"
"v_accvgpr_read_b32 v229, a89\n"
"v_accvgpr_read_b32 v230, a90\n"
"v_accvgpr_read_b32 v231, a91\n"
"v_pk_mul_f32 v[228:229], v[228:229], v[242:243] op_sel:[0,0] op_sel_hi:[1,0]\n"
"v_pk_mul_f32 v[230:231], v[230:231], v[242:243] op_sel:[0,0] op_sel_hi:[1,0]\n"
"v_cvt_pk_bf16_f32 v74, v228, v229\n"
"v_cvt_pk_bf16_f32 v75, v230, v231\n"
"v_accvgpr_read_b32 v224, a76\n"
"v_accvgpr_read_b32 v225, a77\n"
"v_accvgpr_read_b32 v226, a78\n"
"v_accvgpr_read_b32 v227, a79\n"
"v_pk_mul_f32 v[224:225], v[224:225], v[242:243] op_sel:[0,1] op_sel_hi:[1,1]\n"
"v_pk_mul_f32 v[226:227], v[226:227], v[242:243] op_sel:[0,1] op_sel_hi:[1,1]\n"
"v_cvt_pk_bf16_f32 v104, v224, v225\n"
"v_cvt_pk_bf16_f32 v105, v226, v227\n"
"v_accvgpr_read_b32 v228, a92\n"
"v_accvgpr_read_b32 v229, a93\n"
"v_accvgpr_read_b32 v230, a94\n"
"v_accvgpr_read_b32 v231, a95\n"
"v_pk_mul_f32 v[228:229], v[228:229], v[242:243] op_sel:[0,1] op_sel_hi:[1,1]\n"
"v_pk_mul_f32 v[230:231], v[230:231], v[242:243] op_sel:[0,1] op_sel_hi:[1,1]\n"
"v_cvt_pk_bf16_f32 v106, v228, v229\n"
"v_cvt_pk_bf16_f32 v107, v230, v231\n"
"v_mfma_f32_16x16x32_bf16 v[128:131], a[4:7], v[68:71], v[128:131]\n"
"s_waitcnt lgkmcnt(8)\n"
"buffer_load_dwordx4 v232, s[52:55], s78 offen lds\n"
"v_mul_f32 v134, v241, v134\n"
"v_accvgpr_read_b32 v224, a104\n"
"v_accvgpr_read_b32 v225, a105\n"
"v_accvgpr_read_b32 v226, a106\n"
"v_accvgpr_read_b32 v227, a107\n"
"v_pk_mul_f32 v[224:225], v[224:225], v[242:243] op_sel:[0,0] op_sel_hi:[1,0]\n"
"v_pk_mul_f32 v[226:227], v[226:227], v[242:243] op_sel:[0,0] op_sel_hi:[1,0]\n"
"v_cvt_pk_bf16_f32 v76, v224, v225\n"
"v_cvt_pk_bf16_f32 v77, v226, v227\n"
"v_accvgpr_read_b32 v228, a120\n"
"v_accvgpr_read_b32 v229, a121\n"
"v_accvgpr_read_b32 v230, a122\n"
"v_accvgpr_read_b32 v231, a123\n"
"v_pk_mul_f32 v[228:229], v[228:229], v[242:243] op_sel:[0,0] op_sel_hi:[1,0]\n"
"v_pk_mul_f32 v[230:231], v[230:231], v[242:243] op_sel:[0,0] op_sel_hi:[1,0]\n"
"v_cvt_pk_bf16_f32 v78, v228, v229\n"
"v_cvt_pk_bf16_f32 v79, v230, v231\n"
"v_accvgpr_read_b32 v224, a108\n"
"v_accvgpr_read_b32 v225, a109\n"
"v_accvgpr_read_b32 v226, a110\n"
"v_accvgpr_read_b32 v227, a111\n"
"v_pk_mul_f32 v[224:225], v[224:225], v[242:243] op_sel:[0,1] op_sel_hi:[1,1]\n"
"v_pk_mul_f32 v[226:227], v[226:227], v[242:243] op_sel:[0,1] op_sel_hi:[1,1]\n"
"v_cvt_pk_bf16_f32 v108, v224, v225\n"
"v_cvt_pk_bf16_f32 v109, v226, v227\n"
"v_accvgpr_read_b32 v228, a124\n"
"v_accvgpr_read_b32 v229, a125\n"
"v_accvgpr_read_b32 v230, a126\n"
"v_accvgpr_read_b32 v231, a127\n"
"v_pk_mul_f32 v[228:229], v[228:229], v[242:243] op_sel:[0,1] op_sel_hi:[1,1]\n"
"v_pk_mul_f32 v[230:231], v[230:231], v[242:243] op_sel:[0,1] op_sel_hi:[1,1]\n"
"v_cvt_pk_bf16_f32 v110, v228, v229\n"
"v_cvt_pk_bf16_f32 v111, v230, v231\n"
"v_mfma_f32_16x16x32_bf16 v[128:131], a[16:19], v[72:75], v[128:131]\n"
"ds_read_b64_tr_b16 a[98:99], v237 offset:51200\n"
"ds_read_b64_tr_b16 a[100:101], v237 offset:53248\n"
"v_accvgpr_read_b32 v224, a136\n"
"v_accvgpr_read_b32 v225, a137\n"
"v_accvgpr_read_b32 v226, a138\n"
"v_accvgpr_read_b32 v227, a139\n"
"v_pk_mul_f32 v[224:225], v[224:225], v[242:243] op_sel:[0,0] op_sel_hi:[1,0]\n"
"v_pk_mul_f32 v[226:227], v[226:227], v[242:243] op_sel:[0,0] op_sel_hi:[1,0]\n"
"v_cvt_pk_bf16_f32 v80, v224, v225\n"
"v_cvt_pk_bf16_f32 v81, v226, v227\n"
"v_accvgpr_read_b32 v228, a152\n"
"v_accvgpr_read_b32 v229, a153\n"
"v_accvgpr_read_b32 v230, a154\n"
"v_accvgpr_read_b32 v231, a155\n"
"v_pk_mul_f32 v[228:229], v[228:229], v[242:243] op_sel:[0,0] op_sel_hi:[1,0]\n"
"v_pk_mul_f32 v[230:231], v[230:231], v[242:243] op_sel:[0,0] op_sel_hi:[1,0]\n"
"v_cvt_pk_bf16_f32 v82, v228, v229\n"
"v_cvt_pk_bf16_f32 v83, v230, v231\n"
"v_accvgpr_read_b32 v224, a140\n"
"v_accvgpr_read_b32 v225, a141\n"
"v_accvgpr_read_b32 v226, a142\n"
"v_accvgpr_read_b32 v227, a143\n"
"v_pk_mul_f32 v[224:225], v[224:225], v[242:243] op_sel:[0,1] op_sel_hi:[1,1]\n"
"v_pk_mul_f32 v[226:227], v[226:227], v[242:243] op_sel:[0,1] op_sel_hi:[1,1]\n"
"v_cvt_pk_bf16_f32 v112, v224, v225\n"
"v_cvt_pk_bf16_f32 v113, v226, v227\n"
"v_accvgpr_read_b32 v228, a156\n"
"v_accvgpr_read_b32 v229, a157\n"
"v_accvgpr_read_b32 v230, a158\n"
"v_accvgpr_read_b32 v231, a159\n"
"v_pk_mul_f32 v[228:229], v[228:229], v[242:243] op_sel:[0,1] op_sel_hi:[1,1]\n"
"v_pk_mul_f32 v[230:231], v[230:231], v[242:243] op_sel:[0,1] op_sel_hi:[1,1]\n"
"v_cvt_pk_bf16_f32 v114, v228, v229\n"
"v_cvt_pk_bf16_f32 v115, v230, v231\n"
"v_mfma_f32_16x16x32_bf16 v[128:131], a[20:23], v[76:79], v[128:131]\n"
"ds_read_b64_tr_b16 a[102:103], v237 offset:55296\n"
"v_mul_f32 v135, v241, v135\n"
"v_accvgpr_read_b32 v224, a168\n"
"v_accvgpr_read_b32 v225, a169\n"
"v_accvgpr_read_b32 v226, a170\n"
"v_accvgpr_read_b32 v227, a171\n"
"v_pk_mul_f32 v[224:225], v[224:225], v[242:243] op_sel:[0,0] op_sel_hi:[1,0]\n"
"v_pk_mul_f32 v[226:227], v[226:227], v[242:243] op_sel:[0,0] op_sel_hi:[1,0]\n"
"v_cvt_pk_bf16_f32 v84, v224, v225\n"
"v_cvt_pk_bf16_f32 v85, v226, v227\n"
"v_accvgpr_read_b32 v228, a184\n"
"v_accvgpr_read_b32 v229, a185\n"
"v_accvgpr_read_b32 v230, a186\n"
"v_accvgpr_read_b32 v231, a187\n"
"v_pk_mul_f32 v[228:229], v[228:229], v[242:243] op_sel:[0,0] op_sel_hi:[1,0]\n"
"v_pk_mul_f32 v[230:231], v[230:231], v[242:243] op_sel:[0,0] op_sel_hi:[1,0]\n"
"v_cvt_pk_bf16_f32 v86, v228, v229\n"
"v_cvt_pk_bf16_f32 v87, v230, v231\n"
"v_accvgpr_read_b32 v224, a172\n"
"v_accvgpr_read_b32 v225, a173\n"
"v_accvgpr_read_b32 v226, a174\n"
"v_accvgpr_read_b32 v227, a175\n"
"v_pk_mul_f32 v[224:225], v[224:225], v[242:243] op_sel:[0,1] op_sel_hi:[1,1]\n"
"v_pk_mul_f32 v[226:227], v[226:227], v[242:243] op_sel:[0,1] op_sel_hi:[1,1]\n"
"v_cvt_pk_bf16_f32 v116, v224, v225\n"
"v_cvt_pk_bf16_f32 v117, v226, v227\n"
"v_accvgpr_read_b32 v228, a188\n"
"v_accvgpr_read_b32 v229, a189\n"
"v_accvgpr_read_b32 v230, a190\n"
"v_accvgpr_read_b32 v231, a191\n"
"v_pk_mul_f32 v[228:229], v[228:229], v[242:243] op_sel:[0,1] op_sel_hi:[1,1]\n"
"v_pk_mul_f32 v[230:231], v[230:231], v[242:243] op_sel:[0,1] op_sel_hi:[1,1]\n"
"v_cvt_pk_bf16_f32 v118, v228, v229\n"
"v_cvt_pk_bf16_f32 v119, v230, v231\n"
"v_mfma_f32_16x16x32_bf16 v[128:131], a[32:35], v[80:83], v[128:131]\n"
"s_add_u32 m0, s95, 94208\n"
"ds_read_b64_tr_b16 a[112:113], v237 offset:57344\n"
"v_accvgpr_read_b32 v224, a200\n"
"v_accvgpr_read_b32 v225, a201\n"
"v_accvgpr_read_b32 v226, a202\n"
"v_accvgpr_read_b32 v227, a203\n"
"v_pk_mul_f32 v[224:225], v[224:225], v[242:243] op_sel:[0,0] op_sel_hi:[1,0]\n"
"v_pk_mul_f32 v[226:227], v[226:227], v[242:243] op_sel:[0,0] op_sel_hi:[1,0]\n"
"v_cvt_pk_bf16_f32 v88, v224, v225\n"
"v_cvt_pk_bf16_f32 v89, v226, v227\n"
"v_accvgpr_read_b32 v228, a216\n"
"v_accvgpr_read_b32 v229, a217\n"
"v_accvgpr_read_b32 v230, a218\n"
"v_accvgpr_read_b32 v231, a219\n"
"v_pk_mul_f32 v[228:229], v[228:229], v[242:243] op_sel:[0,0] op_sel_hi:[1,0]\n"
"v_pk_mul_f32 v[230:231], v[230:231], v[242:243] op_sel:[0,0] op_sel_hi:[1,0]\n"
"v_cvt_pk_bf16_f32 v90, v228, v229\n"
"v_cvt_pk_bf16_f32 v91, v230, v231\n"
"v_accvgpr_read_b32 v224, a204\n"
"v_accvgpr_read_b32 v225, a205\n"
"v_accvgpr_read_b32 v226, a206\n"
"v_accvgpr_read_b32 v227, a207\n"
"v_pk_mul_f32 v[224:225], v[224:225], v[242:243] op_sel:[0,1] op_sel_hi:[1,1]\n"
"v_pk_mul_f32 v[226:227], v[226:227], v[242:243] op_sel:[0,1] op_sel_hi:[1,1]\n"
"v_cvt_pk_bf16_f32 v120, v224, v225\n"
"v_cvt_pk_bf16_f32 v121, v226, v227\n"
"v_accvgpr_read_b32 v228, a220\n"
"v_accvgpr_read_b32 v229, a221\n"
"v_accvgpr_read_b32 v230, a222\n"
"v_accvgpr_read_b32 v231, a223\n"
"v_pk_mul_f32 v[228:229], v[228:229], v[242:243] op_sel:[0,1] op_sel_hi:[1,1]\n"
"v_pk_mul_f32 v[230:231], v[230:231], v[242:243] op_sel:[0,1] op_sel_hi:[1,1]\n"
"v_cvt_pk_bf16_f32 v122, v228, v229\n"
"v_cvt_pk_bf16_f32 v123, v230, v231\n"
"v_mfma_f32_16x16x32_bf16 v[128:131], a[36:39], v[84:87], v[128:131]\n"
"buffer_load_dwordx4 v232, s[52:55], s79 offen lds\n"
"v_accvgpr_read_b32 v224, a232\n"
"v_accvgpr_read_b32 v225, a233\n"
"v_accvgpr_read_b32 v226, a234\n"
"v_accvgpr_read_b32 v227, a235\n"
"v_pk_mul_f32 v[224:225], v[224:225], v[242:243] op_sel:[0,0] op_sel_hi:[1,0]\n"
"v_pk_mul_f32 v[226:227], v[226:227], v[242:243] op_sel:[0,0] op_sel_hi:[1,0]\n"
"v_cvt_pk_bf16_f32 v92, v224, v225\n"
"v_cvt_pk_bf16_f32 v93, v226, v227\n"
"v_accvgpr_read_b32 v228, a248\n"
"v_accvgpr_read_b32 v229, a249\n"
"v_accvgpr_read_b32 v230, a250\n"
"v_accvgpr_read_b32 v231, a251\n"
"v_pk_mul_f32 v[228:229], v[228:229], v[242:243] op_sel:[0,0] op_sel_hi:[1,0]\n"
"v_pk_mul_f32 v[230:231], v[230:231], v[242:243] op_sel:[0,0] op_sel_hi:[1,0]\n"
"v_cvt_pk_bf16_f32 v94, v228, v229\n"
"v_cvt_pk_bf16_f32 v95, v230, v231\n"
"v_accvgpr_read_b32 v224, a236\n"
"v_accvgpr_read_b32 v225, a237\n"
"v_accvgpr_read_b32 v226, a238\n"
"v_accvgpr_read_b32 v227, a239\n"
"v_pk_mul_f32 v[224:225], v[224:225], v[242:243] op_sel:[0,1] op_sel_hi:[1,1]\n"
"v_pk_mul_f32 v[226:227], v[226:227], v[242:243] op_sel:[0,1] op_sel_hi:[1,1]\n"
"v_cvt_pk_bf16_f32 v124, v224, v225\n"
"v_cvt_pk_bf16_f32 v125, v226, v227\n"
"v_accvgpr_read_b32 v228, a252\n"
"v_accvgpr_read_b32 v229, a253\n"
"v_accvgpr_read_b32 v230, a254\n"
"v_accvgpr_read_b32 v231, a255\n"
"v_pk_mul_f32 v[228:229], v[228:229], v[242:243] op_sel:[0,1] op_sel_hi:[1,1]\n"
"v_pk_mul_f32 v[230:231], v[230:231], v[242:243] op_sel:[0,1] op_sel_hi:[1,1]\n"
"v_cvt_pk_bf16_f32 v126, v228, v229\n"
"v_cvt_pk_bf16_f32 v127, v230, v231\n"
"v_mfma_f32_16x16x32_bf16 v[128:131], a[48:51], v[88:91], v[128:131]\n"
"ds_read_b64_tr_b16 a[114:115], v237 offset:59392\n"
"ds_read_b64_tr_b16 a[116:117], v237 offset:61440\n"
"v_mfma_f32_16x16x32_bf16 v[128:131], a[52:55], v[92:95], v[128:131]\n"
"ds_read_b64_tr_b16 a[118:119], v237 offset:63488\n"
"v_mul_f32 v136, v240, v136\n"
"v_mfma_f32_16x16x32_bf16 v[132:135], a[0:3], v[96:99], v[132:135]\n"
"s_cmp_ge_u32 s84, 768\n"
"ds_read_b64_tr_b16 a[128:129], v238 offset:32768\n"
"v_add_u32 v237, 65536, v237\n"
"v_mfma_f32_16x16x32_bf16 v[132:135], a[4:7], v[100:103], v[132:135]\n"
"s_cselect_b32 s86, -896, 0\n"
"ds_read_b64_tr_b16 a[130:131], v238 offset:34816\n"
"v_mul_f32 v137, v240, v137\n"
"v_mfma_f32_16x16x32_bf16 v[132:135], a[16:19], v[104:107], v[132:135]\n"
"s_add_u32 s86, s86, 128\n"
"ds_read_b64_tr_b16 a[132:133], v238 offset:36864\n"
"v_mul_f32 v138, v240, v138\n"
"v_mfma_f32_16x16x32_bf16 v[132:135], a[20:23], v[108:111], v[132:135]\n"
"s_add_u32 s84, s84, s86\n"
"ds_read_b64_tr_b16 a[134:135], v238 offset:38912\n"
"v_add_u32 v232, s86, v232\n"
"v_mfma_f32_16x16x32_bf16 v[132:135], a[32:35], v[112:115], v[132:135]\n"
"s_add_u32 m0, s95, 131072\n"
"ds_read_b64_tr_b16 a[144:145], v238 offset:40960\n"
"v_mul_f32 v139, v240, v139\n"
"v_mfma_f32_16x16x32_bf16 v[132:135], a[36:39], v[116:119], v[132:135]\n"
"buffer_load_dwordx4 v232, s[52:55], s64 offen lds\n"
"v_cvt_pk_bf16_f32 v128, v128, v129\n"
"v_mfma_f32_16x16x32_bf16 v[132:135], a[48:51], v[120:123], v[132:135]\n"
"ds_read_b64_tr_b16 a[146:147], v238 offset:43008\n"
"ds_read_b64_tr_b16 a[148:149], v238 offset:45056\n"
"v_mfma_f32_16x16x32_bf16 v[132:135], a[52:55], v[124:127], v[132:135]\n"
"s_waitcnt lgkmcnt(8)\n"
"ds_read_b64_tr_b16 a[150:151], v238 offset:47104\n"
"v_mul_f32 v140, v241, v140\n"
"v_mfma_f32_16x16x32_bf16 v[136:139], a[64:67], v[64:67], v[136:139]\n"
"s_add_u32 m0, s95, 135168\n"
"ds_read_b64_tr_b16 a[160:161], v238 offset:49152\n"
"v_mul_f32 v141, v241, v141\n"
"v_mfma_f32_16x16x32_bf16 v[136:139], a[68:71], v[68:71], v[136:139]\n"
"buffer_load_dwordx4 v232, s[52:55], s65 offen lds\n"
"v_mul_f32 v142, v241, v142\n"
"v_mfma_f32_16x16x32_bf16 v[136:139], a[80:83], v[72:75], v[136:139]\n"
"ds_read_b64_tr_b16 a[162:163], v238 offset:51200\n"
"ds_read_b64_tr_b16 a[164:165], v238 offset:53248\n"
"v_mfma_f32_16x16x32_bf16 v[136:139], a[84:87], v[76:79], v[136:139]\n"
"ds_read_b64_tr_b16 a[166:167], v238 offset:55296\n"
"v_mul_f32 v143, v241, v143\n"
"v_mfma_f32_16x16x32_bf16 v[136:139], a[96:99], v[80:83], v[136:139]\n"
"s_add_u32 m0, s95, 139264\n"
"ds_read_b64_tr_b16 a[176:177], v238 offset:57344\n"
"v_cvt_pk_bf16_f32 v129, v130, v131\n"
"v_mfma_f32_16x16x32_bf16 v[136:139], a[100:103], v[84:87], v[136:139]\n"
"buffer_load_dwordx4 v232, s[52:55], s66 offen lds\n"
"v_cvt_pk_bf16_f32 v132, v132, v133\n"
"v_mfma_f32_16x16x32_bf16 v[136:139], a[112:115], v[88:91], v[136:139]\n"
"s_waitcnt lgkmcnt(8)\n"
"ds_read_b64_tr_b16 a[178:179], v238 offset:59392\n"
"ds_read_b64_tr_b16 a[180:181], v238 offset:61440\n"
"v_mfma_f32_16x16x32_bf16 v[136:139], a[116:119], v[92:95], v[136:139]\n"
"ds_read_b64_tr_b16 a[182:183], v238 offset:63488\n"
"v_mul_f32 v144, v240, v144\n"
"v_mfma_f32_16x16x32_bf16 v[140:143], a[64:67], v[96:99], v[140:143]\n"
"s_add_u32 m0, s95, 143360\n"
"ds_read_b64_tr_b16 a[192:193], v239 offset:32768\n"
"v_add_u32 v238, 65536, v238\n"
"v_mfma_f32_16x16x32_bf16 v[140:143], a[68:71], v[100:103], v[140:143]\n"
"buffer_load_dwordx4 v232, s[52:55], s67 offen lds\n"
"v_mul_f32 v145, v240, v145\n"
"v_mfma_f32_16x16x32_bf16 v[140:143], a[80:83], v[104:107], v[140:143]\n"
"ds_read_b64_tr_b16 a[194:195], v239 offset:34816\n"
"ds_read_b64_tr_b16 a[196:197], v239 offset:36864\n"
"v_mfma_f32_16x16x32_bf16 v[140:143], a[84:87], v[108:111], v[140:143]\n"
"ds_read_b64_tr_b16 a[198:199], v239 offset:38912\n"
"v_mul_f32 v146, v240, v146\n"
"v_mfma_f32_16x16x32_bf16 v[140:143], a[96:99], v[112:115], v[140:143]\n"
"s_add_u32 m0, s95, 147456\n"
"ds_read_b64_tr_b16 a[208:209], v239 offset:40960\n"
"v_mul_f32 v147, v240, v147\n"
"v_mfma_f32_16x16x32_bf16 v[140:143], a[100:103], v[116:119], v[140:143]\n"
"buffer_load_dwordx4 v232, s[52:55], s68 offen lds\n"
"v_cvt_pk_bf16_f32 v133, v134, v135\n"
"v_mfma_f32_16x16x32_bf16 v[140:143], a[112:115], v[120:123], v[140:143]\n"
"ds_read_b64_tr_b16 a[210:211], v239 offset:43008\n"
"ds_read_b64_tr_b16 a[212:213], v239 offset:45056\n"
"v_mfma_f32_16x16x32_bf16 v[140:143], a[116:119], v[124:127], v[140:143]\n"
"ds_read_b64_tr_b16 a[214:215], v239 offset:47104\n"
"v_mul_f32 v148, v241, v148\n"
"v_mfma_f32_16x16x32_bf16 v[144:147], a[128:131], v[64:67], v[144:147]\n"
"s_add_u32 m0, s95, 151552\n"
"ds_read_b64_tr_b16 a[224:225], v239 offset:49152\n"
"v_mul_f32 v149, v241, v149\n"
"v_mfma_f32_16x16x32_bf16 v[144:147], a[132:135], v[68:71], v[144:147]\n"
"s_waitcnt lgkmcnt(8)\n"
"buffer_load_dwordx4 v232, s[52:55], s69 offen lds\n"
"v_mul_f32 v150, v241, v150\n"
"v_mfma_f32_16x16x32_bf16 v[144:147], a[144:147], v[72:75], v[144:147]\n"
"ds_read_b64_tr_b16 a[226:227], v239 offset:51200\n"
"ds_read_b64_tr_b16 a[228:229], v239 offset:53248\n"
"v_mfma_f32_16x16x32_bf16 v[144:147], a[148:151], v[76:79], v[144:147]\n"
"ds_read_b64_tr_b16 a[230:231], v239 offset:55296\n"
"v_mul_f32 v151, v241, v151\n"
"v_mfma_f32_16x16x32_bf16 v[144:147], a[160:163], v[80:83], v[144:147]\n"
"s_add_u32 m0, s95, 155648\n"
"ds_read_b64_tr_b16 a[240:241], v239 offset:57344\n"
"v_cvt_pk_bf16_f32 v136, v136, v137\n"
"v_mfma_f32_16x16x32_bf16 v[144:147], a[164:167], v[84:87], v[144:147]\n"
"buffer_load_dwordx4 v232, s[52:55], s70 offen lds\n"
"v_cvt_pk_bf16_f32 v137, v138, v139\n"
"v_mfma_f32_16x16x32_bf16 v[144:147], a[176:179], v[88:91], v[144:147]\n"
"s_waitcnt vmcnt(15)\n"
"ds_read_b64_tr_b16 a[242:243], v239 offset:59392\n"
"ds_read_b64_tr_b16 a[244:245], v239 offset:61440\n"
"v_mfma_f32_16x16x32_bf16 v[144:147], a[180:183], v[92:95], v[144:147]\n"
"s_barrier\n"
"ds_read_b64_tr_b16 a[246:247], v239 offset:63488\n"
"v_mul_f32 v152, v240, v152\n"
"v_mfma_f32_16x16x32_bf16 v[148:151], a[128:131], v[96:99], v[148:151]\n"
"s_add_u32 m0, s95, 159744\n"
"ds_read_b64_tr_b16 a[0:1], v236 offset:32768\n"
"v_add_u32 v239, 65536, v239\n"
"v_mfma_f32_16x16x32_bf16 v[148:151], a[132:135], v[100:103], v[148:151]\n"
"s_lshl_b32 s87, s34, 2\n"
"buffer_load_dwordx4 v232, s[52:55], s71 offen lds\n"
"v_mul_f32 v153, v240, v153\n"
"v_mfma_f32_16x16x32_bf16 v[148:151], a[144:147], v[104:107], v[148:151]\n"
"s_add_u32 s87, s87, s80\n"
"ds_read_b64_tr_b16 a[2:3], v236 offset:34816\n"
"ds_read_b64_tr_b16 a[4:5], v236 offset:36864\n"
"v_mfma_f32_16x16x32_bf16 v[148:151], a[148:151], v[108:111], v[148:151]\n"
"s_lshl_b32 s90, s87, 10\n"
"ds_read_b64_tr_b16 a[6:7], v236 offset:38912\n"
"v_mul_f32 v154, v240, v154\n"
"v_mfma_f32_16x16x32_bf16 v[148:151], a[160:163], v[112:115], v[148:151]\n"
"s_add_u32 s90, s90, 917504\n"
"ds_read_b64_tr_b16 a[16:17], v236 offset:40960\n"
"v_mul_f32 v155, v240, v155\n"
"v_mfma_f32_16x16x32_bf16 v[148:151], a[164:167], v[116:119], v[148:151]\n"
"s_lshl_b32 s87, s87, 12\n"
"ds_read_b64_tr_b16 a[18:19], v236 offset:43008\n"
"v_cvt_pk_bf16_f32 v140, v140, v141\n"
"v_mfma_f32_16x16x32_bf16 v[148:151], a[176:179], v[120:123], v[148:151]\n"
"ds_read_b64_tr_b16 a[20:21], v236 offset:45056\n"
"v_lshrrev_b32 v234, 4, v233\n"
"v_mfma_f32_16x16x32_bf16 v[148:151], a[180:183], v[124:127], v[148:151]\n"
"s_waitcnt lgkmcnt(8)\n"
"ds_read_b64_tr_b16 a[22:23], v236 offset:47104\n"
"v_mul_f32 v156, v241, v156\n"
"v_mfma_f32_16x16x32_bf16 v[152:155], a[192:195], v[64:67], v[152:155]\n"
"ds_read_b64_tr_b16 a[32:33], v236 offset:49152\n"
"v_mul_f32 v157, v241, v157\n"
"v_mfma_f32_16x16x32_bf16 v[152:155], a[196:199], v[68:71], v[152:155]\n"
"ds_read_b64_tr_b16 a[34:35], v236 offset:51200\n"
"v_mul_f32 v158, v241, v158\n"
"v_mfma_f32_16x16x32_bf16 v[152:155], a[208:211], v[72:75], v[152:155]\n"
"ds_read_b64_tr_b16 a[36:37], v236 offset:53248\n"
"v_mul_f32 v159, v241, v159\n"
"v_mfma_f32_16x16x32_bf16 v[152:155], a[212:215], v[76:79], v[152:155]\n"
"ds_read_b64_tr_b16 a[38:39], v236 offset:55296\n"
"v_lshl_add_u32 v234, v234, 21, v233\n"
"v_mfma_f32_16x16x32_bf16 v[152:155], a[224:227], v[80:83], v[152:155]\n"
"ds_read_b64_tr_b16 a[48:49], v236 offset:57344\n"
"v_lshl_add_u32 v234, v234, 3, s90\n"
"v_mfma_f32_16x16x32_bf16 v[152:155], a[228:231], v[84:87], v[152:155]\n"
"ds_read_b64_tr_b16 a[50:51], v236 offset:59392\n"
"v_min_u32 v235, 6, v233\n"
"v_mfma_f32_16x16x32_bf16 v[152:155], a[240:243], v[88:91], v[152:155]\n"
"s_waitcnt lgkmcnt(8)\n"
"ds_read_b64_tr_b16 a[52:53], v236 offset:61440\n"
"v_lshl_add_u32 v233, v233, 3, s87\n"
"v_mfma_f32_16x16x32_bf16 v[152:155], a[244:247], v[92:95], v[152:155]\n"
"ds_read_b64_tr_b16 a[54:55], v236 offset:63488\n"
"v_mov_b32 v244, s96\n"
"v_mfma_f32_16x16x32_bf16 v[156:159], a[192:195], v[96:99], v[156:159]\n"
"buffer_store_dwordx2 v[128:129], v233, s[56:59], s85 offen offset:0 sc1\n"
"ds_read_b64_tr_b16 a[64:65], v237 offset:32768\n"
"v_mov_b32 v245, s97\n"
"v_mfma_f32_16x16x32_bf16 v[156:159], a[196:199], v[100:103], v[156:159]\n"
"ds_read_b64_tr_b16 a[66:67], v237 offset:34816\n"
"v_cvt_pk_bf16_f32 v141, v142, v143\n"
"v_mfma_f32_16x16x32_bf16 v[156:159], a[208:211], v[104:107], v[156:159]\n"
"ds_read_b64_tr_b16 a[68:69], v237 offset:36864\n"
"v_cvt_pk_bf16_f32 v144, v144, v145\n"
"v_mfma_f32_16x16x32_bf16 v[156:159], a[212:215], v[108:111], v[156:159]\n"
"ds_read_b64_tr_b16 a[70:71], v237 offset:38912\n"
"v_cvt_pk_bf16_f32 v145, v146, v147\n"
"v_mfma_f32_16x16x32_bf16 v[156:159], a[224:227], v[112:115], v[156:159]\n"
"ds_read_b64_tr_b16 a[80:81], v237 offset:40960\n"
"v_cvt_pk_bf16_f32 v148, v148, v149\n"
"v_mfma_f32_16x16x32_bf16 v[156:159], a[228:231], v[116:119], v[156:159]\n"
"ds_read_b64_tr_b16 a[82:83], v237 offset:43008\n"
"v_cvt_pk_bf16_f32 v149, v150, v151\n"
"v_mfma_f32_16x16x32_bf16 v[156:159], a[240:243], v[120:123], v[156:159]\n"
"ds_read_b64_tr_b16 a[84:85], v237 offset:45056\n"
"v_cvt_pk_bf16_f32 v152, v152, v153\n"
"v_mfma_f32_16x16x32_bf16 v[156:159], a[244:247], v[124:127], v[156:159]\n"
"ds_read_b64_tr_b16 a[86:87], v237 offset:47104\n"
"v_cvt_pk_bf16_f32 v153, v154, v155\n"
"v_mfma_f32_16x16x32_bf16 v[160:163], a[0:3], v[0:3], 0\n"
"s_min_u32 s89, s34, 6\n"
"buffer_store_dwordx2 v[132:133], v233, s[56:59], s85 offen offset:512 sc1\n"
"ds_read_b64_tr_b16 a[96:97], v237 offset:49152\n"
"v_mfma_f32_16x16x32_bf16 v[160:163], a[4:7], v[4:7], v[160:163]\n"
"s_add_u32 s87, s89, 6\n"
"ds_read_b64_tr_b16 a[98:99], v237 offset:51200\n"
"v_mfma_f32_16x16x32_bf16 v[160:163], a[16:19], v[8:11], v[160:163]\n"
"s_waitcnt lgkmcnt(8)\n"
"ds_read_b64_tr_b16 a[100:101], v237 offset:53248\n"
"v_sub_u32 v246, s87, v235\n"
"v_mfma_f32_16x16x32_bf16 v[160:163], a[20:23], v[12:15], v[160:163]\n"
"ds_read_b64_tr_b16 a[102:103], v237 offset:55296\n"
"v_subrev_u32 v247, 7, v246\n"
"v_mfma_f32_16x16x32_bf16 v[160:163], a[32:35], v[16:19], v[160:163]\n"
"ds_read_b64_tr_b16 a[112:113], v237 offset:57344\n"
"v_min_u32 v246, v246, v247\n"
"v_mfma_f32_16x16x32_bf16 v[160:163], a[36:39], v[20:23], v[160:163]\n"
"ds_read_b64_tr_b16 a[114:115], v237 offset:59392\n"
"v_cmp_eq_u32 vcc, 6, v235\n"
"v_mfma_f32_16x16x32_bf16 v[160:163], a[48:51], v[24:27], v[160:163]\n"
"ds_read_b64_tr_b16 a[116:117], v237 offset:61440\n"
"v_lshl_add_u32 v235, v246, 5, v235\n"
"v_mfma_f32_16x16x32_bf16 v[160:163], a[52:55], v[28:31], v[160:163]\n"
"ds_read_b64_tr_b16 a[118:119], v237 offset:63488\n"
"v_add_u32 v246, 224, v246\n"
"v_mfma_f32_16x16x32_bf16 v[164:167], a[0:3], v[32:35], 0\n"
"s_lshl_b32 s87, s80, 10\n"
"buffer_store_dwordx2 v[136:137], v233, s[56:59], s85 offen offset:1024 sc1\n"
"ds_read_b64_tr_b16 a[128:129], v238 offset:32768\n"
"v_cndmask_b32 v235, v235, v246, vcc\n"
"v_mfma_f32_16x16x32_bf16 v[164:167], a[4:7], v[36:39], v[164:167]\n"
"s_add_u32 s87, s87, 917504\n"
"ds_read_b64_tr_b16 a[130:131], v238 offset:34816\n"
"v_cvt_pk_bf16_f32 v156, v156, v157\n"
"v_mfma_f32_16x16x32_bf16 v[164:167], a[16:19], v[40:43], v[164:167]\n"
"ds_read_b64_tr_b16 a[132:133], v238 offset:36864\n"
"v_mul_f32 v160, v240, v160\n"
"v_mfma_f32_16x16x32_bf16 v[164:167], a[20:23], v[44:47], v[164:167]\n"
"ds_read_b64_tr_b16 a[134:135], v238 offset:38912\n"
"v_mul_f32 v161, v240, v161\n"
"v_mfma_f32_16x16x32_bf16 v[164:167], a[32:35], v[48:51], v[164:167]\n"
"ds_read_b64_tr_b16 a[144:145], v238 offset:40960\n"
"v_mul_f32 v162, v240, v162\n"
"v_mfma_f32_16x16x32_bf16 v[164:167], a[36:39], v[52:55], v[164:167]\n"
"ds_read_b64_tr_b16 a[146:147], v238 offset:43008\n"
"v_mul_f32 v163, v240, v163\n"
"v_mfma_f32_16x16x32_bf16 v[164:167], a[48:51], v[56:59], v[164:167]\n"
"ds_read_b64_tr_b16 a[148:149], v238 offset:45056\n"
"v_lshl_add_u32 v235, v235, 7, s87\n"
"v_mfma_f32_16x16x32_bf16 v[164:167], a[52:55], v[60:63], v[164:167]\n"
"s_mul_i32 s87, s89, 114688\n"
"ds_read_b64_tr_b16 a[150:151], v238 offset:47104\n"
"v_cvt_pk_bf16_f32 v157, v158, v159\n"
"v_mfma_f32_16x16x32_bf16 v[168:171], a[64:67], v[0:3], 0\n"
"s_waitcnt lgkmcnt(8)\n"
"s_add_u32 s42, s87, 114688\n"
"buffer_store_dwordx2 v[140:141], v233, s[56:59], s85 offen offset:1536 sc1\n"
"ds_read_b64_tr_b16 a[160:161], v238 offset:49152\n"
"v_mfma_f32_16x16x32_bf16 v[168:171], a[68:71], v[4:7], v[168:171]\n"
"s_lshl_b32 s43, s89, 14\n"
"ds_read_b64_tr_b16 a[162:163], v238 offset:51200\n"
"v_mfma_f32_16x16x32_bf16 v[168:171], a[80:83], v[8:11], v[168:171]\n"
"s_cmp_eq_u32 s43, 0\n"
"ds_read_b64_tr_b16 a[164:165], v238 offset:53248\n"
"v_mul_f32 v164, v241, v164\n"
"v_mfma_f32_16x16x32_bf16 v[168:171], a[84:87], v[12:15], v[168:171]\n"
"s_cselect_b32 s43, 114688, s43\n"
"ds_read_b64_tr_b16 a[166:167], v238 offset:55296\n"
"v_mul_f32 v165, v241, v165\n"
"v_mfma_f32_16x16x32_bf16 v[168:171], a[96:99], v[16:19], v[168:171]\n"
"s_sub_u32 s43, s43, 16384\n"
"ds_read_b64_tr_b16 a[176:177], v238 offset:57344\n"
"v_mul_f32 v166, v241, v166\n"
"v_mfma_f32_16x16x32_bf16 v[168:171], a[100:103], v[20:23], v[168:171]\n"
"s_add_u32 m0, s95, 0\n"
"ds_read_b64_tr_b16 a[178:179], v238 offset:59392\n"
"v_mul_f32 v167, v241, v167\n"
"v_mfma_f32_16x16x32_bf16 v[168:171], a[112:115], v[24:27], v[168:171]\n"
"s_add_u32 s36, s43, s87\n"
"buffer_load_dwordx4 v232, s[52:55], s72 offen lds\n"
"v_mfma_f32_16x16x32_bf16 v[168:171], a[116:119], v[28:31], v[168:171]\n"
"s_cmp_eq_u32 s43, 0\n"
"ds_read_b64_tr_b16 a[180:181], v238 offset:61440\n"
"ds_read_b64_tr_b16 a[182:183], v238 offset:63488\n"
"v_mfma_f32_16x16x32_bf16 v[172:175], a[64:67], v[32:35], 0\n"
"s_cselect_b32 s43, 114688, s43\n"
"buffer_store_dwordx2 v[144:145], v233, s[56:59], s85 offen offset:2048 sc1\n"
"ds_read_b64_tr_b16 a[192:193], v239 offset:32768\n"
"v_mfma_f32_16x16x32_bf16 v[172:175], a[68:71], v[36:39], v[172:175]\n"
"s_sub_u32 s43, s43, 16384\n"
"ds_read_b64_tr_b16 a[194:195], v239 offset:34816\n"
"v_mfma_f32_16x16x32_bf16 v[172:175], a[80:83], v[40:43], v[172:175]\n"
"s_add_u32 m0, s95, 4096\n"
"ds_read_b64_tr_b16 a[196:197], v239 offset:36864\n"
"v_mul_f32 v168, v240, v168\n"
"v_mfma_f32_16x16x32_bf16 v[172:175], a[84:87], v[44:47], v[172:175]\n"
"s_add_u32 s37, s43, s87\n"
"buffer_load_dwordx4 v232, s[52:55], s73 offen lds\n"
"v_mul_f32 v169, v240, v169\n"
"v_mfma_f32_16x16x32_bf16 v[172:175], a[96:99], v[48:51], v[172:175]\n"
"s_cmp_eq_u32 s43, 0\n"
"ds_read_b64_tr_b16 a[198:199], v239 offset:38912\n"
"ds_read_b64_tr_b16 a[208:209], v239 offset:40960\n"
"v_mfma_f32_16x16x32_bf16 v[172:175], a[100:103], v[52:55], v[172:175]\n"
"s_cselect_b32 s43, 114688, s43\n"
"ds_read_b64_tr_b16 a[210:211], v239 offset:43008\n"
"v_mul_f32 v170, v240, v170\n"
"v_mfma_f32_16x16x32_bf16 v[172:175], a[112:115], v[56:59], v[172:175]\n"
"s_sub_u32 s43, s43, 16384\n"
"ds_read_b64_tr_b16 a[212:213], v239 offset:45056\n"
"v_mul_f32 v171, v240, v171\n"
"v_mfma_f32_16x16x32_bf16 v[172:175], a[116:119], v[60:63], v[172:175]\n"
"s_waitcnt lgkmcnt(9)\n"
"s_add_u32 m0, s95, 8192\n"
"ds_read_b64_tr_b16 a[214:215], v239 offset:47104\n"
"v_mfma_f32_16x16x32_bf16 v[176:179], a[128:131], v[0:3], 0\n"
"s_add_u32 s38, s43, s87\n"
"buffer_load_dwordx4 v232, s[52:55], s74 offen lds\n"
"v_mfma_f32_16x16x32_bf16 v[176:179], a[132:135], v[4:7], v[176:179]\n"
"s_cmp_eq_u32 s43, 0\n"
"buffer_store_dwordx2 v[148:149], v233, s[56:59], s85 offen offset:2560 sc1\n"
"ds_read_b64_tr_b16 a[224:225], v239 offset:49152\n"
"ds_read_b64_tr_b16 a[226:227], v239 offset:51200\n"
"v_mfma_f32_16x16x32_bf16 v[176:179], a[144:147], v[8:11], v[176:179]\n"
"s_cselect_b32 s43, 114688, s43\n"
"ds_read_b64_tr_b16 a[228:229], v239 offset:53248\n"
"v_mul_f32 v172, v241, v172\n"
"v_mfma_f32_16x16x32_bf16 v[176:179], a[148:151], v[12:15], v[176:179]\n"
"s_sub_u32 s43, s43, 16384\n"
"ds_read_b64_tr_b16 a[230:231], v239 offset:55296\n"
"v_mul_f32 v173, v241, v173\n"
"v_mfma_f32_16x16x32_bf16 v[176:179], a[160:163], v[16:19], v[176:179]\n"
"s_add_u32 m0, s95, 12288\n"
"ds_read_b64_tr_b16 a[240:241], v239 offset:57344\n"
"v_mul_f32 v174, v241, v174\n"
"v_mfma_f32_16x16x32_bf16 v[176:179], a[164:167], v[20:23], v[176:179]\n"
"s_add_u32 s39, s43, s87\n"
"buffer_load_dwordx4 v232, s[52:55], s75 offen lds\n"
"v_mul_f32 v175, v241, v175\n"
"v_mfma_f32_16x16x32_bf16 v[176:179], a[176:179], v[24:27], v[176:179]\n"
"s_waitcnt vmcnt(18) lgkmcnt(7)\n"
"ds_read_b64_tr_b16 a[242:243], v239 offset:59392\n"
"ds_read_b64_tr_b16 a[244:245], v239 offset:61440\n"
"v_mfma_f32_16x16x32_bf16 v[176:179], a[180:183], v[28:31], v[176:179]\n"
"s_barrier\n"
"ds_read_b64_tr_b16 a[246:247], v239 offset:63488\n"
"v_mfma_f32_16x16x32_bf16 v[180:183], a[128:131], v[32:35], 0\n"
"ds_read_b64_tr_b16 a[0:1], v236 offset:0\n"
"v_mfma_f32_16x16x32_bf16 v[180:183], a[132:135], v[36:39], v[180:183]\n"
"s_add_u32 m0, s95, 16384\n"
"buffer_store_dwordx2 v[152:153], v233, s[56:59], s85 offen offset:3072 sc1\n"
"ds_read_b64_tr_b16 a[2:3], v236 offset:2048\n"
"v_mfma_f32_16x16x32_bf16 v[180:183], a[144:147], v[40:43], v[180:183]\n"
"s_cmp_eq_u32 s43, 0\n"
"buffer_load_dwordx4 v232, s[52:55], s76 offen lds\n"
"v_mul_f32 v176, v240, v176\n"
"v_mfma_f32_16x16x32_bf16 v[180:183], a[148:151], v[44:47], v[180:183]\n"
"s_cselect_b32 s43, 114688, s43\n"
"ds_read_b64_tr_b16 a[4:5], v236 offset:4096\n"
"ds_read_b64_tr_b16 a[6:7], v236 offset:6144\n"
"v_mfma_f32_16x16x32_bf16 v[180:183], a[160:163], v[48:51], v[180:183]\n"
"s_sub_u32 s43, s43, 16384\n"
"ds_read_b64_tr_b16 a[16:17], v236 offset:8192\n"
"v_mul_f32 v177, v240, v177\n"
"v_mfma_f32_16x16x32_bf16 v[180:183], a[164:167], v[52:55], v[180:183]\n"
"s_add_u32 s40, s43, s87\n"
"ds_read_b64_tr_b16 a[18:19], v236 offset:10240\n"
"v_mul_f32 v178, v240, v178\n"
"v_mfma_f32_16x16x32_bf16 v[180:183], a[176:179], v[56:59], v[180:183]\n"
"s_add_u32 m0, s95, 20480\n"
"ds_read_b64_tr_b16 a[20:21], v236 offset:12288\n"
"v_mul_f32 v179, v240, v179\n"
"v_mfma_f32_16x16x32_bf16 v[180:183], a[180:183], v[60:63], v[180:183]\n"
"s_cmp_eq_u32 s43, 0\n"
"buffer_load_dwordx4 v232, s[52:55], s77 offen lds\n"
"v_mfma_f32_16x16x32_bf16 v[184:187], a[192:195], v[0:3], 0\n"
"s_cselect_b32 s43, 114688, s43\n"
"ds_read_b64_tr_b16 a[22:23], v236 offset:14336\n"
"ds_read_b64_tr_b16 a[32:33], v236 offset:16384\n"
"v_mfma_f32_16x16x32_bf16 v[184:187], a[196:199], v[4:7], v[184:187]\n"
"s_sub_u32 s43, s43, 16384\n"
"buffer_store_dwordx2 v[156:157], v233, s[56:59], s85 offen offset:3584 sc1\n"
"ds_read_b64_tr_b16 a[34:35], v236 offset:18432\n"
"v_mfma_f32_16x16x32_bf16 v[184:187], a[208:211], v[8:11], v[184:187]\n"
"s_waitcnt lgkmcnt(8)\n"
"s_add_u32 s41, s43, s87\n"
"ds_read_b64_tr_b16 a[36:37], v236 offset:20480\n"
"v_mul_f32 v180, v241, v180\n"
"v_mfma_f32_16x16x32_bf16 v[184:187], a[212:215], v[12:15], v[184:187]\n"
"s_add_u32 m0, s95, 24576\n"
"ds_read_b64_tr_b16 a[38:39], v236 offset:22528\n"
"v_mul_f32 v181, v241, v181\n"
"v_mfma_f32_16x16x32_bf16 v[184:187], a[224:227], v[16:19], v[184:187]\n"
"s_mov_b64 s[44:45], s[56:57]\n"
"buffer_load_dwordx4 v232, s[52:55], s78 offen lds\n"
"v_mul_f32 v182, v241, v182\n"
"v_mfma_f32_16x16x32_bf16 v[184:187], a[228:231], v[20:23], v[184:187]\n"
"s_cmp_eq_u32 s34, 7\n"
"ds_read_b64_tr_b16 a[48:49], v236 offset:24576\n"
"ds_read_b64_tr_b16 a[50:51], v236 offset:26624\n"
"v_mfma_f32_16x16x32_bf16 v[184:187], a[240:243], v[24:27], v[184:187]\n"
"s_cselect_b32 s43, 0, s58\n"
"ds_read_b64_tr_b16 a[52:53], v236 offset:28672\n"
"v_mul_f32 v183, v241, v183\n"
"v_mfma_f32_16x16x32_bf16 v[184:187], a[244:247], v[28:31], v[184:187]\n"
"s_cselect_b32 s50, s58, 0\n"
"ds_read_b64_tr_b16 a[54:55], v236 offset:30720\n"
"v_mfma_f32_16x16x32_bf16 v[188:191], a[192:195], v[32:35], 0\n"
"s_add_u32 m0, s95, 28672\n"
"ds_read_b64_tr_b16 a[64:65], v237 offset:0\n"
"v_add_u32 v236, 65536, v236\n"
"v_mfma_f32_16x16x32_bf16 v[188:191], a[196:199], v[36:39], v[188:191]\n"
"s_mov_b64 s[48:49], s[56:57]\n"
"buffer_load_dwordx4 v232, s[52:55], s79 offen lds\n"
"v_mfma_f32_16x16x32_bf16 v[188:191], a[208:211], v[40:43], v[188:191]\n"
"s_add_u32 s85, s85, 131072\n"
"ds_read_b64_tr_b16 a[66:67], v237 offset:2048\n"
"ds_read_b64_tr_b16 a[68:69], v237 offset:4096\n"
"v_mfma_f32_16x16x32_bf16 v[188:191], a[212:215], v[44:47], v[188:191]\n"
"s_cmp_eq_u32 s85, 917504\n"
"ds_read_b64_tr_b16 a[70:71], v237 offset:6144\n"
"v_mul_f32 v184, v240, v184\n"
"v_mfma_f32_16x16x32_bf16 v[188:191], a[224:227], v[48:51], v[188:191]\n"
"s_cselect_b32 s85, 0, s85\n"
"ds_read_b64_tr_b16 a[80:81], v237 offset:8192\n"
"v_mul_f32 v185, v240, v185\n"
"v_mfma_f32_16x16x32_bf16 v[188:191], a[228:231], v[52:55], v[188:191]\n"
"s_cmp_ge_u32 s84, 768\n"
"ds_read_b64_tr_b16 a[82:83], v237 offset:10240\n"
"v_mul_f32 v186, v240, v186\n"
"v_mfma_f32_16x16x32_bf16 v[188:191], a[240:243], v[56:59], v[188:191]\n"
"s_cselect_b32 s86, -896, 0\n"
"ds_read_b64_tr_b16 a[84:85], v237 offset:12288\n"
"v_mul_f32 v187, v240, v187\n"
"v_mfma_f32_16x16x32_bf16 v[188:191], a[244:247], v[60:63], v[188:191]\n"
"s_add_u32 s86, s86, 128\n"
"ds_read_b64_tr_b16 a[86:87], v237 offset:14336\n"
"v_mbcnt_lo_u32_b32 v246, -1, 0\n"
"v_mfma_f32_16x16x32_bf16 v[160:163], a[0:3], v[64:67], v[160:163]\n"
"s_waitcnt lgkmcnt(8)\n"
"s_add_u32 s84, s84, s86\n"
"ds_read_b64_tr_b16 a[96:97], v237 offset:16384\n"
"v_add_u32 v232, s86, v232\n"
"v_mfma_f32_16x16x32_bf16 v[160:163], a[4:7], v[68:71], v[160:163]\n"
"s_add_u32 m0, s95, 32768\n"
"ds_read_b64_tr_b16 a[98:99], v237 offset:18432\n"
"v_mbcnt_hi_u32_b32 v246, -1, v246\n"
"v_mfma_f32_16x16x32_bf16 v[160:163], a[16:19], v[72:75], v[160:163]\n"
"buffer_load_dwordx4 v232, s[52:55], s64 offen lds\n"
"v_mul_f32 v188, v241, v188\n"
"v_mfma_f32_16x16x32_bf16 v[160:163], a[20:23], v[76:79], v[160:163]\n"
"ds_read_b64_tr_b16 a[100:101], v237 offset:20480\n"
"ds_read_b64_tr_b16 a[102:103], v237 offset:22528\n"
"v_mfma_f32_16x16x32_bf16 v[160:163], a[32:35], v[80:83], v[160:163]\n"
"ds_read_b64_tr_b16 a[112:113], v237 offset:24576\n"
"v_mul_f32 v189, v241, v189\n"
"v_mfma_f32_16x16x32_bf16 v[160:163], a[36:39], v[84:87], v[160:163]\n"
"ds_read_b64_tr_b16 a[114:115], v237 offset:26624\n"
"v_mul_f32 v190, v241, v190\n"
"v_mfma_f32_16x16x32_bf16 v[160:163], a[48:51], v[88:91], v[160:163]\n"
"s_add_u32 m0, s95, 36864\n"
"ds_read_b64_tr_b16 a[116:117], v237 offset:28672\n"
"v_mul_f32 v191, v241, v191\n"
"v_mfma_f32_16x16x32_bf16 v[160:163], a[52:55], v[92:95], v[160:163]\n"
"ds_read_b64_tr_b16 a[118:119], v237 offset:30720\n"
"buffer_load_dwordx4 v232, s[52:55], s65 offen lds\n"
"v_mfma_f32_16x16x32_bf16 v[164:167], a[0:3], v[96:99], v[164:167]\n"
"ds_read_b64_tr_b16 a[128:129], v238 offset:0\n"
"v_add_u32 v237, 65536, v237\n"
"v_mfma_f32_16x16x32_bf16 v[164:167], a[4:7], v[100:103], v[164:167]\n"
"ds_read_b64_tr_b16 a[130:131], v238 offset:2048\n"
"v_lshrrev_b32 v247, 2, v246\n"
"v_mfma_f32_16x16x32_bf16 v[164:167], a[16:19], v[104:107], v[164:167]\n"
"ds_read_b64_tr_b16 a[132:133], v238 offset:4096\n"
"v_cvt_pk_bf16_f32 v160, v160, v161\n"
"v_mfma_f32_16x16x32_bf16 v[164:167], a[20:23], v[108:111], v[164:167]\n"
"s_add_u32 m0, s95, 40960\n"
"ds_read_b64_tr_b16 a[134:135], v238 offset:6144\n"
"v_cvt_pk_bf16_f32 v161, v162, v163\n"
"v_mfma_f32_16x16x32_bf16 v[164:167], a[32:35], v[112:115], v[164:167]\n"
"buffer_load_dwordx4 v232, s[52:55], s66 offen lds\n"
"v_lshrrev_b32 v224, 4, v246\n"
"v_mfma_f32_16x16x32_bf16 v[164:167], a[36:39], v[116:119], v[164:167]\n"
"buffer_store_dwordx2 v[160:161], v233, s[56:59], s85 offen offset:0 sc1\n"
"ds_read_b64_tr_b16 a[144:145], v238 offset:8192\n"
"ds_read_b64_tr_b16 a[146:147], v238 offset:10240\n"
"v_mfma_f32_16x16x32_bf16 v[164:167], a[48:51], v[120:123], v[164:167]\n"
"ds_read_b64_tr_b16 a[148:149], v238 offset:12288\n"
"v_xor_b32 v247, v247, v224\n"
"v_mfma_f32_16x16x32_bf16 v[164:167], a[52:55], v[124:127], v[164:167]\n"
"s_waitcnt lgkmcnt(8)\n"
"ds_read_b64_tr_b16 a[150:151], v238 offset:14336\n"
"v_and_b32 v247, 3, v247\n"
"v_mfma_f32_16x16x32_bf16 v[168:171], a[64:67], v[64:67], v[168:171]\n"
"s_add_u32 m0, s95, 45056\n"
"ds_read_b64_tr_b16 a[160:161], v238 offset:16384\n"
"v_lshlrev_b32 v247, 2, v247\n"
"v_mfma_f32_16x16x32_bf16 v[168:171], a[68:71], v[68:71], v[168:171]\n"
"buffer_load_dwordx4 v232, s[52:55], s67 offen lds\n"
"v_and_b32 v246, 3, v246\n"
"v_mfma_f32_16x16x32_bf16 v[168:171], a[80:83], v[72:75], v[168:171]\n"
"ds_read_b64_tr_b16 a[162:163], v238 offset:18432\n"
"ds_read_b64_tr_b16 a[164:165], v238 offset:20480\n"
"v_mfma_f32_16x16x32_bf16 v[168:171], a[84:87], v[76:79], v[168:171]\n"
"ds_read_b64_tr_b16 a[166:167], v238 offset:22528\n"
"v_cvt_pk_bf16_f32 v164, v164, v165\n"
"v_mfma_f32_16x16x32_bf16 v[168:171], a[96:99], v[80:83], v[168:171]\n"
"ds_read_b64_tr_b16 a[176:177], v238 offset:24576\n"
"v_cvt_pk_bf16_f32 v165, v166, v167\n"
"v_mfma_f32_16x16x32_bf16 v[168:171], a[100:103], v[84:87], v[168:171]\n"
"s_add_u32 m0, s95, 49152\n"
"buffer_store_dwordx2 v[164:165], v233, s[56:59], s85 offen offset:512 sc1\n"
"ds_read_b64_tr_b16 a[178:179], v238 offset:26624\n"
"v_mfma_f32_16x16x32_bf16 v[168:171], a[112:115], v[88:91], v[168:171]\n"
"s_waitcnt lgkmcnt(8)\n"
"buffer_load_dwordx4 v232, s[52:55], s68 offen lds\n"
"v_or_b32 v246, v246, v247\n"
"v_mfma_f32_16x16x32_bf16 v[168:171], a[116:119], v[92:95], v[168:171]\n"
"ds_read_b64_tr_b16 a[180:181], v238 offset:28672\n"
"ds_read_b64_tr_b16 a[182:183], v238 offset:30720\n"
"v_mfma_f32_16x16x32_bf16 v[172:175], a[64:67], v[96:99], v[172:175]\n"
"ds_read_b64_tr_b16 a[192:193], v239 offset:0\n"
"v_add_u32 v238, 65536, v238\n"
"v_mfma_f32_16x16x32_bf16 v[172:175], a[68:71], v[100:103], v[172:175]\n"
"ds_read_b64_tr_b16 a[194:195], v239 offset:2048\n"
"v_min_u32 v246, 4, v246\n"
"v_mfma_f32_16x16x32_bf16 v[172:175], a[80:83], v[104:107], v[172:175]\n"
"s_add_u32 m0, s95, 53248\n"
"ds_read_b64_tr_b16 a[196:197], v239 offset:4096\n"
"v_cvt_pk_bf16_f32 v168, v168, v169\n"
"v_mfma_f32_16x16x32_bf16 v[172:175], a[84:87], v[108:111], v[172:175]\n"
"buffer_load_dwordx4 v232, s[52:55], s69 offen lds\n"
"v_cvt_pk_bf16_f32 v169, v170, v171\n"
"v_mfma_f32_16x16x32_bf16 v[172:175], a[96:99], v[112:115], v[172:175]\n"
"ds_read_b64_tr_b16 a[198:199], v239 offset:6144\n"
"ds_read_b64_tr_b16 a[208:209], v239 offset:8192\n"
"v_mfma_f32_16x16x32_bf16 v[172:175], a[100:103], v[116:119], v[172:175]\n"
"buffer_store_dwordx2 v[168:169], v233, s[56:59], s85 offen offset:1024 sc1\n"
"ds_read_b64_tr_b16 a[210:211], v239 offset:10240\n"
"v_mfma_f32_16x16x32_bf16 v[172:175], a[112:115], v[120:123], v[172:175]\n"
"ds_read_b64_tr_b16 a[212:213], v239 offset:12288\n"
"v_lshlrev_b32 v246, 4, v246\n"
"v_mfma_f32_16x16x32_bf16 v[172:175], a[116:119], v[124:127], v[172:175]\n"
"s_add_u32 m0, s95, 57344\n"
"ds_read_b64_tr_b16 a[214:215], v239 offset:14336\n"
"v_mov_b32 v226, 0x3f80\n"
"v_mfma_f32_16x16x32_bf16 v[176:179], a[128:131], v[64:67], v[176:179]\n"
"buffer_load_dwordx4 v232, s[52:55], s70 offen lds\n"
"v_mov_b32 v227, 0\n"
"v_mfma_f32_16x16x32_bf16 v[176:179], a[132:135], v[68:71], v[176:179]\n"
"ds_read_b64_tr_b16 a[224:225], v239 offset:16384\n"
"ds_read_b64_tr_b16 a[226:227], v239 offset:18432\n"
"v_mfma_f32_16x16x32_bf16 v[176:179], a[144:147], v[72:75], v[176:179]\n"
"s_waitcnt lgkmcnt(8)\n"
"ds_read_b64_tr_b16 a[228:229], v239 offset:20480\n"
"v_cvt_pk_bf16_f32 v172, v172, v173\n"
"v_mfma_f32_16x16x32_bf16 v[176:179], a[148:151], v[76:79], v[176:179]\n"
"ds_read_b64_tr_b16 a[230:231], v239 offset:22528\n"
"v_cvt_pk_bf16_f32 v173, v174, v175\n"
"v_mfma_f32_16x16x32_bf16 v[176:179], a[160:163], v[80:83], v[176:179]\n"
"s_add_u32 m0, s95, 61440\n"
"ds_read_b64_tr_b16 a[240:241], v239 offset:24576\n"
"v_cmp_gt_u32 vcc, 64, v246\n"
"v_mfma_f32_16x16x32_bf16 v[176:179], a[164:167], v[84:87], v[176:179]\n"
"buffer_load_dwordx4 v232, s[52:55], s71 offen lds\n"
"v_lshlrev_b64 v[226:227], v246, v[226:227]\n"
"v_mfma_f32_16x16x32_bf16 v[176:179], a[176:179], v[88:91], v[176:179]\n"
"s_waitcnt vmcnt(27)\n"
"buffer_store_dwordx2 v[172:173], v233, s[56:59], s85 offen offset:1536 sc1\n"
"ds_read_b64_tr_b16 a[242:243], v239 offset:26624\n"
"ds_read_b64_tr_b16 a[244:245], v239 offset:28672\n"
"v_mfma_f32_16x16x32_bf16 v[176:179], a[180:183], v[92:95], v[176:179]\n"
"s_barrier\n"
"ds_read_b64_tr_b16 a[246:247], v239 offset:30720\n"
"v_mov_b32 v224, 0\n"
"v_mfma_f32_16x16x32_bf16 v[180:183], a[128:131], v[96:99], v[180:183]\n"
"ds_read_b64_tr_b16 a[0:1], v236 offset:0\n"
"v_add_u32 v239, 65536, v239\n"
"v_mfma_f32_16x16x32_bf16 v[180:183], a[132:135], v[100:103], v[180:183]\n"
"ds_read_b64_tr_b16 a[2:3], v236 offset:2048\n"
"v_mov_b32 v225, 0\n"
"v_mfma_f32_16x16x32_bf16 v[180:183], a[144:147], v[104:107], v[180:183]\n"
"ds_read_b64_tr_b16 a[4:5], v236 offset:4096\n"
"v_cvt_pk_bf16_f32 v176, v176, v177\n"
"v_mfma_f32_16x16x32_bf16 v[180:183], a[148:151], v[108:111], v[180:183]\n"
"ds_read_b64_tr_b16 a[6:7], v236 offset:6144\n"
"v_cvt_pk_bf16_f32 v177, v178, v179\n"
"v_mfma_f32_16x16x32_bf16 v[180:183], a[160:163], v[112:115], v[180:183]\n"
"ds_read_b64_tr_b16 a[16:17], v236 offset:8192\n"
"v_cndmask_b32 v226, 0, v226, vcc\n"
"v_mfma_f32_16x16x32_bf16 v[180:183], a[164:167], v[116:119], v[180:183]\n"
"ds_read_b64_tr_b16 a[18:19], v236 offset:10240\n"
"v_cndmask_b32 v227, 0, v227, vcc\n"
"v_mfma_f32_16x16x32_bf16 v[180:183], a[176:179], v[120:123], v[180:183]\n"
"buffer_store_dwordx2 v[176:177], v233, s[56:59], s85 offen offset:2048 sc1\n"
"ds_read_b64_tr_b16 a[20:21], v236 offset:12288\n"
"v_mfma_f32_16x16x32_bf16 v[180:183], a[180:183], v[124:127], v[180:183]\n"
"ds_read_b64_tr_b16 a[22:23], v236 offset:14336\n"
"v_mov_b32 v228, v226\n"
"v_mfma_f32_16x16x32_bf16 v[184:187], a[192:195], v[64:67], v[184:187]\n"
"s_waitcnt lgkmcnt(8)\n"
"ds_read_b64_tr_b16 a[32:33], v236 offset:16384\n"
"v_mov_b32 v229, v227\n"
"v_mfma_f32_16x16x32_bf16 v[184:187], a[196:199], v[68:71], v[184:187]\n"
"s_waitcnt vmcnt(15)\n"
"ds_read_b64_tr_b16 a[34:35], v236 offset:18432\n"
"buffer_store_dwordx2 v[244:245], v234, s[56:59], 0 offen offset:0 sc1\n"
"v_mfma_f32_16x16x32_bf16 v[184:187], a[208:211], v[72:75], v[184:187]\n"
"s_lshl_b32 s87, s80, 13\n"
"ds_read_b64_tr_b16 a[36:37], v236 offset:20480\n"
"v_cvt_pk_bf16_f32 v180, v180, v181\n"
"v_mfma_f32_16x16x32_bf16 v[184:187], a[212:215], v[76:79], v[184:187]\n"
"ds_read_b64_tr_b16 a[38:39], v236 offset:22528\n"
"v_cvt_pk_bf16_f32 v181, v182, v183\n"
"v_mfma_f32_16x16x32_bf16 v[184:187], a[224:227], v[80:83], v[184:187]\n"
"ds_read_b64_tr_b16 a[48:49], v236 offset:24576\n"
"v_mbcnt_lo_u32_b32 v246, -1, 0\n"
"v_mfma_f32_16x16x32_bf16 v[184:187], a[228:231], v[84:87], v[184:187]\n"
"ds_read_b64_tr_b16 a[50:51], v236 offset:26624\n"
"v_mbcnt_hi_u32_b32 v246, -1, v246\n"
"v_mfma_f32_16x16x32_bf16 v[184:187], a[240:243], v[88:91], v[184:187]\n"
"buffer_store_dwordx2 v[180:181], v233, s[56:59], s85 offen offset:2560 sc1\n"
"ds_read_b64_tr_b16 a[52:53], v236 offset:28672\n"
"v_mfma_f32_16x16x32_bf16 v[184:187], a[244:247], v[92:95], v[184:187]\n"
"ds_read_b64_tr_b16 a[54:55], v236 offset:30720\n"
"v_and_b32 v247, 15, v246\n"
"v_mfma_f32_16x16x32_bf16 v[188:191], a[192:195], v[96:99], v[188:191]\n"
"ds_read_b64_tr_b16 a[64:65], v237 offset:0\n"
"v_add_u32 v236, -131072, v236\n"
"v_mfma_f32_16x16x32_bf16 v[188:191], a[196:199], v[100:103], v[188:191]\n"
"ds_read_b64_tr_b16 a[66:67], v237 offset:2048\n"
"v_mul_u32_u24 v247, 144, v247\n"
"v_mfma_f32_16x16x32_bf16 v[188:191], a[208:211], v[104:107], v[188:191]\n"
"ds_read_b64_tr_b16 a[68:69], v237 offset:4096\n"
"v_cvt_pk_bf16_f32 v184, v184, v185\n"
"v_mfma_f32_16x16x32_bf16 v[188:191], a[212:215], v[108:111], v[188:191]\n"
"ds_read_b64_tr_b16 a[70:71], v237 offset:6144\n"
"v_cvt_pk_bf16_f32 v185, v186, v187\n"
"v_mfma_f32_16x16x32_bf16 v[188:191], a[224:227], v[112:115], v[188:191]\n"
"ds_read_b64_tr_b16 a[80:81], v237 offset:8192\n"
"v_lshrrev_b32 v248, 4, v246\n"
"v_mfma_f32_16x16x32_bf16 v[188:191], a[228:231], v[116:119], v[188:191]\n"
"ds_read_b64_tr_b16 a[82:83], v237 offset:10240\n"
"v_lshl_add_u32 v248, v248, 3, v247\n"
"v_mfma_f32_16x16x32_bf16 v[188:191], a[240:243], v[120:123], v[188:191]\n"
"buffer_store_dwordx2 v[184:185], v233, s[56:59], s85 offen offset:3072 sc1\n"
"ds_read_b64_tr_b16 a[84:85], v237 offset:12288\n"
"v_mfma_f32_16x16x32_bf16 v[188:191], a[244:247], v[124:127], v[188:191]\n"
"s_waitcnt lgkmcnt(8)\n"
"ds_read_b64_tr_b16 a[86:87], v237 offset:14336\n"
"v_add_u32 v248, s87, v248\n"
"v_mfma_f32_16x16x32_bf16 v[192:195], a[0:3], v[0:3], 0\n"
"ds_read_b64_tr_b16 a[96:97], v237 offset:16384\n"
"v_lshrrev_b32 v247, 3, v246\n"
"v_mfma_f32_16x16x32_bf16 v[192:195], a[4:7], v[4:7], v[192:195]\n"
"ds_read_b64_tr_b16 a[98:99], v237 offset:18432\n"
"v_and_b32 v246, 7, v246\n"
"v_mfma_f32_16x16x32_bf16 v[192:195], a[16:19], v[8:11], v[192:195]\n"
"ds_read_b64_tr_b16 a[100:101], v237 offset:20480\n"
"v_cvt_pk_bf16_f32 v188, v188, v189\n"
"v_mfma_f32_16x16x32_bf16 v[192:195], a[20:23], v[12:15], v[192:195]\n"
"ds_read_b64_tr_b16 a[102:103], v237 offset:22528\n"
"v_cvt_pk_bf16_f32 v189, v190, v191\n"
"v_mfma_f32_16x16x32_bf16 v[192:195], a[32:35], v[16:19], v[192:195]\n"
"ds_read_b64_tr_b16 a[112:113], v237 offset:24576\n"
"v_lshlrev_b32 v246, 4, v246\n"
"v_mfma_f32_16x16x32_bf16 v[192:195], a[36:39], v[20:23], v[192:195]\n"
"ds_read_b64_tr_b16 a[114:115], v237 offset:26624\n"
"v_mul_u32_u24 v249, 144, v247\n"
"v_mfma_f32_16x16x32_bf16 v[192:195], a[48:51], v[24:27], v[192:195]\n"
"s_waitcnt lgkmcnt(8)\n"
"buffer_store_dwordx2 v[188:189], v233, s[56:59], s85 offen offset:3584 sc1\n"
"ds_read_b64_tr_b16 a[116:117], v237 offset:28672\n"
"v_mfma_f32_16x16x32_bf16 v[192:195], a[52:55], v[28:31], v[192:195]\n"
"s_add_u32 s85, s85, 131072\n"
"ds_read_b64_tr_b16 a[118:119], v237 offset:30720\n"
"v_add3_u32 v249, v249, v246, s87\n"
"v_mfma_f32_16x16x32_bf16 v[196:199], a[0:3], v[32:35], 0\n"
"s_cmp_eq_u32 s85, 917504\n"
"ds_read_b64_tr_b16 a[128:129], v238 offset:0\n"
"v_add_u32 v237, -131072, v237\n"
"v_mfma_f32_16x16x32_bf16 v[196:199], a[4:7], v[36:39], v[196:199]\n"
"s_cselect_b32 s85, 0, s85\n"
"ds_read_b64_tr_b16 a[130:131], v238 offset:2048\n"
"v_lshl_add_u32 v247, s80, 5, v247\n"
"v_mfma_f32_16x16x32_bf16 v[196:199], a[16:19], v[40:43], v[196:199]\n"
"ds_read_b64_tr_b16 a[132:133], v238 offset:4096\n"
"v_mul_f32 v192, v240, v192\n"
"v_mfma_f32_16x16x32_bf16 v[196:199], a[20:23], v[44:47], v[196:199]\n"
"ds_read_b64_tr_b16 a[134:135], v238 offset:6144\n"
"v_mul_f32 v193, v240, v193\n"
"v_mfma_f32_16x16x32_bf16 v[196:199], a[32:35], v[48:51], v[196:199]\n"
"ds_read_b64_tr_b16 a[144:145], v238 offset:8192\n"
"v_mul_f32 v194, v240, v194\n"
"v_mfma_f32_16x16x32_bf16 v[196:199], a[36:39], v[52:55], v[196:199]\n"
"ds_read_b64_tr_b16 a[146:147], v238 offset:10240\n"
"v_mul_f32 v195, v240, v195\n"
"v_mfma_f32_16x16x32_bf16 v[196:199], a[48:51], v[56:59], v[196:199]\n"
"ds_read_b64_tr_b16 a[148:149], v238 offset:12288\n"
"v_mul_u32_u24 v247, 1792, v247\n"
"v_mfma_f32_16x16x32_bf16 v[196:199], a[52:55], v[60:63], v[196:199]\n"
"ds_read_b64_tr_b16 a[150:151], v238 offset:14336\n"
"v_add_u32 v250, v246, v247\n"
"v_mfma_f32_16x16x32_bf16 v[200:203], a[64:67], v[0:3], 0\n"
"s_mov_b32 s90, 0\n"
"ds_read_b64_tr_b16 a[160:161], v238 offset:16384\n"
"v_mfma_f32_16x16x32_bf16 v[200:203], a[68:71], v[4:7], v[200:203]\n"
"ds_read_b64_tr_b16 a[162:163], v238 offset:18432\n"
"v_mfma_f32_16x16x32_bf16 v[200:203], a[80:83], v[8:11], v[200:203]\n"
"s_waitcnt lgkmcnt(8)\n"
"ds_read_b64_tr_b16 a[164:165], v238 offset:20480\n"
"v_mul_f32 v196, v241, v196\n"
"v_mfma_f32_16x16x32_bf16 v[200:203], a[84:87], v[12:15], v[200:203]\n"
"ds_read_b64_tr_b16 a[166:167], v238 offset:22528\n"
"v_mul_f32 v197, v241, v197\n"
"v_mfma_f32_16x16x32_bf16 v[200:203], a[96:99], v[16:19], v[200:203]\n"
"ds_read_b64_tr_b16 a[176:177], v238 offset:24576\n"
"v_mul_f32 v198, v241, v198\n"
"v_mfma_f32_16x16x32_bf16 v[200:203], a[100:103], v[20:23], v[200:203]\n"
"s_add_u32 m0, s95, 98304\n"
"ds_read_b64_tr_b16 a[178:179], v238 offset:26624\n"
"v_mul_f32 v199, v241, v199\n"
"v_mfma_f32_16x16x32_bf16 v[200:203], a[112:115], v[24:27], v[200:203]\n"
"buffer_load_dwordx4 v232, s[52:55], s72 offen lds\n"
"v_mfma_f32_16x16x32_bf16 v[200:203], a[116:119], v[28:31], v[200:203]\n"
"ds_read_b64_tr_b16 a[180:181], v238 offset:28672\n"
"ds_read_b64_tr_b16 a[182:183], v238 offset:30720\n"
"v_mfma_f32_16x16x32_bf16 v[204:207], a[64:67], v[32:35], 0\n"
"ds_read_b64_tr_b16 a[192:193], v239 offset:0\n"
"v_add_u32 v238, -131072, v238\n"
"v_mfma_f32_16x16x32_bf16 v[204:207], a[68:71], v[36:39], v[204:207]\n"
"ds_read_b64_tr_b16 a[194:195], v239 offset:2048\n"
"v_mfma_f32_16x16x32_bf16 v[204:207], a[80:83], v[40:43], v[204:207]\n"
"s_add_u32 m0, s95, 102400\n"
"ds_read_b64_tr_b16 a[196:197], v239 offset:4096\n"
"v_mul_f32 v200, v240, v200\n"
"v_mfma_f32_16x16x32_bf16 v[204:207], a[84:87], v[44:47], v[204:207]\n"
"buffer_load_dwordx4 v232, s[52:55], s73 offen lds\n"
"v_mul_f32 v201, v240, v201\n"
"v_mfma_f32_16x16x32_bf16 v[204:207], a[96:99], v[48:51], v[204:207]\n"
"ds_read_b64_tr_b16 a[198:199], v239 offset:6144\n"
"ds_read_b64_tr_b16 a[208:209], v239 offset:8192\n"
"v_mfma_f32_16x16x32_bf16 v[204:207], a[100:103], v[52:55], v[204:207]\n"
"ds_read_b64_tr_b16 a[210:211], v239 offset:10240\n"
"v_mul_f32 v202, v240, v202\n"
"v_mfma_f32_16x16x32_bf16 v[204:207], a[112:115], v[56:59], v[204:207]\n"
"ds_read_b64_tr_b16 a[212:213], v239 offset:12288\n"
"v_mul_f32 v203, v240, v203\n"
"v_mfma_f32_16x16x32_bf16 v[204:207], a[116:119], v[60:63], v[204:207]\n"
"s_add_u32 m0, s95, 106496\n"
"ds_read_b64_tr_b16 a[214:215], v239 offset:14336\n"
"v_mfma_f32_16x16x32_bf16 v[208:211], a[128:131], v[0:3], 0\n"
"s_waitcnt lgkmcnt(8)\n"
"buffer_load_dwordx4 v232, s[52:55], s74 offen lds\n"
"v_mfma_f32_16x16x32_bf16 v[208:211], a[132:135], v[4:7], v[208:211]\n"
"ds_read_b64_tr_b16 a[224:225], v239 offset:16384\n"
"ds_read_b64_tr_b16 a[226:227], v239 offset:18432\n"
"v_mfma_f32_16x16x32_bf16 v[208:211], a[144:147], v[8:11], v[208:211]\n"
"ds_read_b64_tr_b16 a[228:229], v239 offset:20480\n"
"v_mul_f32 v204, v241, v204\n"
"v_mfma_f32_16x16x32_bf16 v[208:211], a[148:151], v[12:15], v[208:211]\n"
"ds_read_b64_tr_b16 a[230:231], v239 offset:22528\n"
"v_mul_f32 v205, v241, v205\n"
"v_mfma_f32_16x16x32_bf16 v[208:211], a[160:163], v[16:19], v[208:211]\n"
"s_add_u32 m0, s95, 110592\n"
"ds_read_b64_tr_b16 a[240:241], v239 offset:24576\n"
"v_mul_f32 v206, v241, v206\n"
"v_mfma_f32_16x16x32_bf16 v[208:211], a[164:167], v[20:23], v[208:211]\n"
"buffer_load_dwordx4 v232, s[52:55], s75 offen lds\n"
"v_mul_f32 v207, v241, v207\n"
"v_mfma_f32_16x16x32_bf16 v[208:211], a[176:179], v[24:27], v[208:211]\n"
"s_waitcnt vmcnt(21)\n"
"ds_read_b64_tr_b16 a[242:243], v239 offset:26624\n"
"ds_read_b64_tr_b16 a[244:245], v239 offset:28672\n"
"v_mfma_f32_16x16x32_bf16 v[208:211], a[180:183], v[28:31], v[208:211]\n"
"s_barrier\n"
"ds_read_b64_tr_b16 a[246:247], v239 offset:30720\n"
"v_mfma_f32_16x16x32_bf16 v[212:215], a[128:131], v[32:35], 0\n"
"ds_read_b64_tr_b16 a[0:1], v236 offset:0\n"
"v_add_u32 v239, -131072, v239\n"
"v_mfma_f32_16x16x32_bf16 v[212:215], a[132:135], v[36:39], v[212:215]\n"
"s_add_u32 m0, s95, 114688\n"
"ds_read_b64_tr_b16 a[2:3], v236 offset:2048\n"
"v_mfma_f32_16x16x32_bf16 v[212:215], a[144:147], v[40:43], v[212:215]\n"
"buffer_load_dwordx4 v232, s[52:55], s76 offen lds\n"
"v_mul_f32 v208, v240, v208\n"
"v_mfma_f32_16x16x32_bf16 v[212:215], a[148:151], v[44:47], v[212:215]\n"
"ds_read_b64_tr_b16 a[4:5], v236 offset:4096\n"
"ds_read_b64_tr_b16 a[6:7], v236 offset:6144\n"
"v_mfma_f32_16x16x32_bf16 v[212:215], a[160:163], v[48:51], v[212:215]\n"
"ds_read_b64_tr_b16 a[16:17], v236 offset:8192\n"
"v_mul_f32 v209, v240, v209\n"
"v_mfma_f32_16x16x32_bf16 v[212:215], a[164:167], v[52:55], v[212:215]\n"
"ds_read_b64_tr_b16 a[18:19], v236 offset:10240\n"
"v_mul_f32 v210, v240, v210\n"
"v_mfma_f32_16x16x32_bf16 v[212:215], a[176:179], v[56:59], v[212:215]\n"
"s_add_u32 m0, s95, 118784\n"
"ds_read_b64_tr_b16 a[20:21], v236 offset:12288\n"
"v_mul_f32 v211, v240, v211\n"
"v_mfma_f32_16x16x32_bf16 v[212:215], a[180:183], v[60:63], v[212:215]\n"
"s_waitcnt lgkmcnt(8)\n"
"buffer_load_dwordx4 v232, s[52:55], s77 offen lds\n"
"v_mfma_f32_16x16x32_bf16 v[216:219], a[192:195], v[0:3], 0\n"
"ds_read_b64_tr_b16 a[22:23], v236 offset:14336\n"
"ds_read_b64_tr_b16 a[32:33], v236 offset:16384\n"
"v_mfma_f32_16x16x32_bf16 v[216:219], a[196:199], v[4:7], v[216:219]\n"
"ds_read_b64_tr_b16 a[34:35], v236 offset:18432\n"
"v_mfma_f32_16x16x32_bf16 v[216:219], a[208:211], v[8:11], v[216:219]\n"
"ds_read_b64_tr_b16 a[36:37], v236 offset:20480\n"
"v_mul_f32 v212, v241, v212\n"
"v_mfma_f32_16x16x32_bf16 v[216:219], a[212:215], v[12:15], v[216:219]\n"
"s_add_u32 m0, s95, 122880\n"
"ds_read_b64_tr_b16 a[38:39], v236 offset:22528\n"
"v_mul_f32 v213, v241, v213\n"
"v_mfma_f32_16x16x32_bf16 v[216:219], a[224:227], v[16:19], v[216:219]\n"
"buffer_load_dwordx4 v232, s[52:55], s78 offen lds\n"
"v_mul_f32 v214, v241, v214\n"
"v_mfma_f32_16x16x32_bf16 v[216:219], a[228:231], v[20:23], v[216:219]\n"
"ds_read_b64_tr_b16 a[48:49], v236 offset:24576\n"
"ds_read_b64_tr_b16 a[50:51], v236 offset:26624\n"
"v_mfma_f32_16x16x32_bf16 v[216:219], a[240:243], v[24:27], v[216:219]\n"
"s_waitcnt lgkmcnt(8)\n"
"ds_read_b64_tr_b16 a[52:53], v236 offset:28672\n"
"v_mul_f32 v215, v241, v215\n"
"v_mfma_f32_16x16x32_bf16 v[216:219], a[244:247], v[28:31], v[216:219]\n"
"ds_read_b64_tr_b16 a[54:55], v236 offset:30720\n"
"v_mfma_f32_16x16x32_bf16 v[220:223], a[192:195], v[32:35], 0\n"
"s_add_u32 m0, s95, 126976\n"
"ds_read_b64_tr_b16 a[64:65], v237 offset:0\n"
"v_mfma_f32_16x16x32_bf16 v[220:223], a[196:199], v[36:39], v[220:223]\n"
"buffer_load_dwordx4 v232, s[52:55], s79 offen lds\n"
"v_mfma_f32_16x16x32_bf16 v[220:223], a[208:211], v[40:43], v[220:223]\n"
"ds_read_b64_tr_b16 a[66:67], v237 offset:2048\n"
"ds_read_b64_tr_b16 a[68:69], v237 offset:4096\n"
"v_mfma_f32_16x16x32_bf16 v[220:223], a[212:215], v[44:47], v[220:223]\n"
"ds_read_b64_tr_b16 a[70:71], v237 offset:6144\n"
"v_mul_f32 v216, v240, v216\n"
"v_mfma_f32_16x16x32_bf16 v[220:223], a[224:227], v[48:51], v[220:223]\n"
"ds_read_b64_tr_b16 a[80:81], v237 offset:8192\n"
"v_mul_f32 v217, v240, v217\n"
"v_mfma_f32_16x16x32_bf16 v[220:223], a[228:231], v[52:55], v[220:223]\n"
"s_cmp_ge_u32 s84, 768\n"
"ds_read_b64_tr_b16 a[82:83], v237 offset:10240\n"
"v_mul_f32 v218, v240, v218\n"
"v_mfma_f32_16x16x32_bf16 v[220:223], a[240:243], v[56:59], v[220:223]\n"
"s_cselect_b32 s86, -896, 0\n"
"ds_read_b64_tr_b16 a[84:85], v237 offset:12288\n"
"v_mul_f32 v219, v240, v219\n"
"v_mfma_f32_16x16x32_bf16 v[220:223], a[244:247], v[60:63], v[220:223]\n"
"s_add_u32 s86, s86, 128\n"
"ds_read_b64_tr_b16 a[86:87], v237 offset:14336\n"
"v_mfma_f32_16x16x32_bf16 v[192:195], a[0:3], v[64:67], v[192:195]\n"
"s_add_u32 s84, s84, s86\n"
"ds_read_b64_tr_b16 a[96:97], v237 offset:16384\n"
"v_add_u32 v232, s86, v232\n"
"v_mfma_f32_16x16x32_bf16 v[192:195], a[4:7], v[68:71], v[192:195]\n"
"s_add_u32 m0, s95, 65536\n"
"ds_read_b64_tr_b16 a[98:99], v237 offset:18432\n"
"v_mfma_f32_16x16x32_bf16 v[192:195], a[16:19], v[72:75], v[192:195]\n"
"s_waitcnt lgkmcnt(9)\n"
"buffer_load_dwordx4 v232, s[52:55], s64 offen lds\n"
"v_mul_f32 v220, v241, v220\n"
"v_mfma_f32_16x16x32_bf16 v[192:195], a[20:23], v[76:79], v[192:195]\n"
"ds_read_b64_tr_b16 a[100:101], v237 offset:20480\n"
"ds_read_b64_tr_b16 a[102:103], v237 offset:22528\n"
"v_mfma_f32_16x16x32_bf16 v[192:195], a[32:35], v[80:83], v[192:195]\n"
"ds_read_b64_tr_b16 a[112:113], v237 offset:24576\n"
"v_mul_f32 v221, v241, v221\n"
"v_mfma_f32_16x16x32_bf16 v[192:195], a[36:39], v[84:87], v[192:195]\n"
"ds_read_b64_tr_b16 a[114:115], v237 offset:26624\n"
"v_mul_f32 v222, v241, v222\n"
"v_mfma_f32_16x16x32_bf16 v[192:195], a[48:51], v[88:91], v[192:195]\n"
"s_add_u32 m0, s95, 69632\n"
"ds_read_b64_tr_b16 a[116:117], v237 offset:28672\n"
"v_mul_f32 v223, v241, v223\n"
"v_mfma_f32_16x16x32_bf16 v[192:195], a[52:55], v[92:95], v[192:195]\n"
"s_waitcnt vmcnt(9)\n"
"buffer_load_dwordx4 v232, s[52:55], s65 offen lds\n"
"buffer_store_dwordx2 v[244:245], v234, s[56:59], 0 offen offset:128 sc1\n"
"v_mfma_f32_16x16x32_bf16 v[196:199], a[0:3], v[96:99], v[196:199]\n"
"ds_read_b64_tr_b16 a[118:119], v237 offset:30720\n"
"ds_read_b64_tr_b16 a[128:129], v238 offset:0\n"
"v_mfma_f32_16x16x32_bf16 v[196:199], a[4:7], v[100:103], v[196:199]\n"
"ds_read_b64_tr_b16 a[130:131], v238 offset:2048\n"
"v_mfma_f32_16x16x32_bf16 v[196:199], a[16:19], v[104:107], v[196:199]\n"
"ds_read_b64_tr_b16 a[132:133], v238 offset:4096\n"
"v_cvt_pk_bf16_f32 v192, v192, v193\n"
"v_mfma_f32_16x16x32_bf16 v[196:199], a[20:23], v[108:111], v[196:199]\n"
"s_add_u32 m0, s95, 73728\n"
"ds_read_b64_tr_b16 a[134:135], v238 offset:6144\n"
"v_cvt_pk_bf16_f32 v193, v194, v195\n"
"v_mfma_f32_16x16x32_bf16 v[196:199], a[32:35], v[112:115], v[196:199]\n"
"buffer_load_dwordx4 v232, s[52:55], s66 offen lds\n"
"v_mfma_f32_16x16x32_bf16 v[196:199], a[36:39], v[116:119], v[196:199]\n"
"buffer_store_dwordx2 v[192:193], v233, s[56:59], s85 offen offset:0 sc1\n"
"ds_read_b64_tr_b16 a[144:145], v238 offset:8192\n"
"ds_read_b64_tr_b16 a[146:147], v238 offset:10240\n"
"v_mfma_f32_16x16x32_bf16 v[196:199], a[48:51], v[120:123], v[196:199]\n"
"ds_read_b64_tr_b16 a[148:149], v238 offset:12288\n"
"v_mfma_f32_16x16x32_bf16 v[196:199], a[52:55], v[124:127], v[196:199]\n"
"s_waitcnt lgkmcnt(8)\n"
"ds_read_b64_tr_b16 a[150:151], v238 offset:14336\n"
"v_mfma_f32_16x16x32_bf16 v[200:203], a[64:67], v[64:67], v[200:203]\n"
"s_add_u32 m0, s95, 77824\n"
"ds_read_b64_tr_b16 a[160:161], v238 offset:16384\n"
"v_mfma_f32_16x16x32_bf16 v[200:203], a[68:71], v[68:71], v[200:203]\n"
"buffer_load_dwordx4 v232, s[52:55], s67 offen lds\n"
"v_mfma_f32_16x16x32_bf16 v[200:203], a[80:83], v[72:75], v[200:203]\n"
"ds_read_b64_tr_b16 a[162:163], v238 offset:18432\n"
"ds_read_b64_tr_b16 a[164:165], v238 offset:20480\n"
"v_mfma_f32_16x16x32_bf16 v[200:203], a[84:87], v[76:79], v[200:203]\n"
"ds_read_b64_tr_b16 a[166:167], v238 offset:22528\n"
"v_cvt_pk_bf16_f32 v196, v196, v197\n"
"v_mfma_f32_16x16x32_bf16 v[200:203], a[96:99], v[80:83], v[200:203]\n"
"ds_read_b64_tr_b16 a[176:177], v238 offset:24576\n"
"v_cvt_pk_bf16_f32 v197, v198, v199\n"
"v_mfma_f32_16x16x32_bf16 v[200:203], a[100:103], v[84:87], v[200:203]\n"
"s_add_u32 m0, s95, 81920\n"
"buffer_store_dwordx2 v[196:197], v233, s[56:59], s85 offen offset:512 sc1\n"
"ds_read_b64_tr_b16 a[178:179], v238 offset:26624\n"
"v_mfma_f32_16x16x32_bf16 v[200:203], a[112:115], v[88:91], v[200:203]\n"
"s_waitcnt lgkmcnt(8)\n"
"buffer_load_dwordx4 v232, s[52:55], s68 offen lds\n"
"v_mfma_f32_16x16x32_bf16 v[200:203], a[116:119], v[92:95], v[200:203]\n"
"ds_read_b64_tr_b16 a[180:181], v238 offset:28672\n"
"ds_read_b64_tr_b16 a[182:183], v238 offset:30720\n"
"v_mfma_f32_16x16x32_bf16 v[204:207], a[64:67], v[96:99], v[204:207]\n"
"ds_read_b64_tr_b16 a[192:193], v239 offset:0\n"
"v_mfma_f32_16x16x32_bf16 v[204:207], a[68:71], v[100:103], v[204:207]\n"
"ds_read_b64_tr_b16 a[194:195], v239 offset:2048\n"
"v_mfma_f32_16x16x32_bf16 v[204:207], a[80:83], v[104:107], v[204:207]\n"
"s_add_u32 m0, s95, 86016\n"
"ds_read_b64_tr_b16 a[196:197], v239 offset:4096\n"
"v_cvt_pk_bf16_f32 v200, v200, v201\n"
"v_mfma_f32_16x16x32_bf16 v[204:207], a[84:87], v[108:111], v[204:207]\n"
"buffer_load_dwordx4 v232, s[52:55], s69 offen lds\n"
"v_cvt_pk_bf16_f32 v201, v202, v203\n"
"v_mfma_f32_16x16x32_bf16 v[204:207], a[96:99], v[112:115], v[204:207]\n"
"ds_read_b64_tr_b16 a[198:199], v239 offset:6144\n"
"ds_read_b64_tr_b16 a[208:209], v239 offset:8192\n"
"v_mfma_f32_16x16x32_bf16 v[204:207], a[100:103], v[116:119], v[204:207]\n"
"buffer_store_dwordx2 v[200:201], v233, s[56:59], s85 offen offset:1024 sc1\n"
"ds_read_b64_tr_b16 a[210:211], v239 offset:10240\n"
"v_mfma_f32_16x16x32_bf16 v[204:207], a[112:115], v[120:123], v[204:207]\n"
"ds_read_b64_tr_b16 a[212:213], v239 offset:12288\n"
"v_mfma_f32_16x16x32_bf16 v[204:207], a[116:119], v[124:127], v[204:207]\n"
"s_add_u32 m0, s95, 90112\n"
"ds_read_b64_tr_b16 a[214:215], v239 offset:14336\n"
"v_mfma_f32_16x16x32_bf16 v[208:211], a[128:131], v[64:67], v[208:211]\n"
"buffer_load_dwordx4 v232, s[52:55], s70 offen lds\n"
"v_mfma_f32_16x16x32_bf16 v[208:211], a[132:135], v[68:71], v[208:211]\n"
"ds_read_b64_tr_b16 a[224:225], v239 offset:16384\n"
"ds_read_b64_tr_b16 a[226:227], v239 offset:18432\n"
"v_mfma_f32_16x16x32_bf16 v[208:211], a[144:147], v[72:75], v[208:211]\n"
"s_waitcnt lgkmcnt(8)\n"
"ds_read_b64_tr_b16 a[228:229], v239 offset:20480\n"
"v_cvt_pk_bf16_f32 v204, v204, v205\n"
"v_mfma_f32_16x16x32_bf16 v[208:211], a[148:151], v[76:79], v[208:211]\n"
"ds_read_b64_tr_b16 a[230:231], v239 offset:22528\n"
"v_cvt_pk_bf16_f32 v205, v206, v207\n"
"v_mfma_f32_16x16x32_bf16 v[208:211], a[160:163], v[80:83], v[208:211]\n"
"s_add_u32 m0, s95, 94208\n"
"ds_read_b64_tr_b16 a[240:241], v239 offset:24576\n"
"v_mfma_f32_16x16x32_bf16 v[208:211], a[164:167], v[84:87], v[208:211]\n"
"buffer_load_dwordx4 v232, s[52:55], s71 offen lds\n"
"v_mfma_f32_16x16x32_bf16 v[208:211], a[176:179], v[88:91], v[208:211]\n"
"s_waitcnt vmcnt(26)\n"
"buffer_store_dwordx2 v[204:205], v233, s[56:59], s85 offen offset:1536 sc1\n"
"ds_read_b64_tr_b16 a[242:243], v239 offset:26624\n"
"ds_read_b64_tr_b16 a[244:245], v239 offset:28672\n"
"v_mfma_f32_16x16x32_bf16 v[208:211], a[180:183], v[92:95], v[208:211]\n"
"s_barrier\n"
"ds_read_b64_tr_b16 a[246:247], v239 offset:30720\n"
"v_mfma_f32_16x16x32_bf16 v[212:215], a[128:131], v[96:99], v[212:215]\n"
"ds_read_b64_tr_b16 a[0:1], v236 offset:32768\n"
"v_mfma_f32_16x16x32_bf16 v[212:215], a[132:135], v[100:103], v[212:215]\n"
"ds_read_b64_tr_b16 a[2:3], v236 offset:34816\n"
"v_mfma_f32_16x16x32_bf16 v[212:215], a[144:147], v[104:107], v[212:215]\n"
"ds_read_b64_tr_b16 a[4:5], v236 offset:36864\n"
"v_cvt_pk_bf16_f32 v208, v208, v209\n"
"v_mfma_f32_16x16x32_bf16 v[212:215], a[148:151], v[108:111], v[212:215]\n"
"ds_read_b64_tr_b16 a[6:7], v236 offset:38912\n"
"v_cvt_pk_bf16_f32 v209, v210, v211\n"
"v_mfma_f32_16x16x32_bf16 v[212:215], a[160:163], v[112:115], v[212:215]\n"
"ds_read_b64_tr_b16 a[16:17], v236 offset:40960\n"
"v_mfma_f32_16x16x32_bf16 v[212:215], a[164:167], v[116:119], v[212:215]\n"
"ds_read_b64_tr_b16 a[18:19], v236 offset:43008\n"
"v_mfma_f32_16x16x32_bf16 v[212:215], a[176:179], v[120:123], v[212:215]\n"
"buffer_store_dwordx2 v[208:209], v233, s[56:59], s85 offen offset:2048 sc1\n"
"ds_read_b64_tr_b16 a[20:21], v236 offset:45056\n"
"v_mfma_f32_16x16x32_bf16 v[212:215], a[180:183], v[124:127], v[212:215]\n"
"ds_read_b64_tr_b16 a[22:23], v236 offset:47104\n"
"v_mfma_f32_16x16x32_bf16 v[216:219], a[192:195], v[64:67], v[216:219]\n"
"s_waitcnt lgkmcnt(8)\n"
"ds_read_b64_tr_b16 a[32:33], v236 offset:49152\n"
"v_mfma_f32_16x16x32_bf16 v[216:219], a[196:199], v[68:71], v[216:219]\n"
"ds_read_b64_tr_b16 a[34:35], v236 offset:51200\n"
"v_mfma_f32_16x16x32_bf16 v[216:219], a[208:211], v[72:75], v[216:219]\n"
"ds_read_b64_tr_b16 a[36:37], v236 offset:53248\n"
"v_cvt_pk_bf16_f32 v212, v212, v213\n"
"v_mfma_f32_16x16x32_bf16 v[216:219], a[212:215], v[76:79], v[216:219]\n"
"ds_read_b64_tr_b16 a[38:39], v236 offset:55296\n"
"v_cvt_pk_bf16_f32 v213, v214, v215\n"
"v_mfma_f32_16x16x32_bf16 v[216:219], a[224:227], v[80:83], v[216:219]\n"
"ds_read_b64_tr_b16 a[48:49], v236 offset:57344\n"
"v_mfma_f32_16x16x32_bf16 v[216:219], a[228:231], v[84:87], v[216:219]\n"
"ds_read_b64_tr_b16 a[50:51], v236 offset:59392\n"
"v_mfma_f32_16x16x32_bf16 v[216:219], a[240:243], v[88:91], v[216:219]\n"
"buffer_store_dwordx2 v[212:213], v233, s[56:59], s85 offen offset:2560 sc1\n"
"ds_read_b64_tr_b16 a[52:53], v236 offset:61440\n"
"v_mfma_f32_16x16x32_bf16 v[216:219], a[244:247], v[92:95], v[216:219]\n"
"ds_read_b64_tr_b16 a[54:55], v236 offset:63488\n"
"v_mfma_f32_16x16x32_bf16 v[220:223], a[192:195], v[96:99], v[220:223]\n"
"ds_read_b64_tr_b16 a[64:65], v237 offset:32768\n"
"v_add_u32 v236, 65536, v236\n"
"v_mfma_f32_16x16x32_bf16 v[220:223], a[196:199], v[100:103], v[220:223]\n"
"ds_read_b64_tr_b16 a[66:67], v237 offset:34816\n"
"v_mfma_f32_16x16x32_bf16 v[220:223], a[208:211], v[104:107], v[220:223]\n"
"ds_read_b64_tr_b16 a[68:69], v237 offset:36864\n"
"v_cvt_pk_bf16_f32 v216, v216, v217\n"
"v_mfma_f32_16x16x32_bf16 v[220:223], a[212:215], v[108:111], v[220:223]\n"
"ds_read_b64_tr_b16 a[70:71], v237 offset:38912\n"
"v_cvt_pk_bf16_f32 v217, v218, v219\n"
"v_mfma_f32_16x16x32_bf16 v[220:223], a[224:227], v[112:115], v[220:223]\n"
"ds_read_b64_tr_b16 a[80:81], v237 offset:40960\n"
"v_mfma_f32_16x16x32_bf16 v[220:223], a[228:231], v[116:119], v[220:223]\n"
"ds_read_b64_tr_b16 a[82:83], v237 offset:43008\n"
"v_mfma_f32_16x16x32_bf16 v[220:223], a[240:243], v[120:123], v[220:223]\n"
"buffer_store_dwordx2 v[216:217], v233, s[56:59], s85 offen offset:3072 sc1\n"
"ds_read_b64_tr_b16 a[84:85], v237 offset:45056\n"
"v_mfma_f32_16x16x32_bf16 v[220:223], a[244:247], v[124:127], v[220:223]\n"
"s_waitcnt lgkmcnt(8)\n"
"ds_read_b64_tr_b16 a[86:87], v237 offset:47104\n"
"v_mfma_f32_16x16x32_bf16 v[128:131], a[0:3], v[0:3], 0\n"
"ds_read_b64_tr_b16 a[96:97], v237 offset:49152\n"
"v_mfma_f32_16x16x32_bf16 v[128:131], a[4:7], v[4:7], v[128:131]\n"
"ds_read_b64_tr_b16 a[98:99], v237 offset:51200\n"
"v_mfma_f32_16x16x32_bf16 v[128:131], a[16:19], v[8:11], v[128:131]\n"
"ds_read_b64_tr_b16 a[100:101], v237 offset:53248\n"
"v_cvt_pk_bf16_f32 v220, v220, v221\n"
"v_mfma_f32_16x16x32_bf16 v[128:131], a[20:23], v[12:15], v[128:131]\n"
"ds_read_b64_tr_b16 a[102:103], v237 offset:55296\n"
"v_cvt_pk_bf16_f32 v221, v222, v223\n"
"v_mfma_f32_16x16x32_bf16 v[128:131], a[32:35], v[16:19], v[128:131]\n"
"ds_read_b64_tr_b16 a[112:113], v237 offset:57344\n"
"v_mfma_f32_16x16x32_bf16 v[128:131], a[36:39], v[20:23], v[128:131]\n"
"ds_read_b64_tr_b16 a[114:115], v237 offset:59392\n"
"v_mfma_f32_16x16x32_bf16 v[128:131], a[48:51], v[24:27], v[128:131]\n"
"s_waitcnt lgkmcnt(8)\n"
"buffer_store_dwordx2 v[220:221], v233, s[56:59], s85 offen offset:3584 sc1\n"
"ds_read_b64_tr_b16 a[116:117], v237 offset:61440\n"
"v_mfma_f32_16x16x32_bf16 v[128:131], a[52:55], v[28:31], v[128:131]\n"
"s_add_u32 s85, s85, 131072\n"
"ds_read_b64_tr_b16 a[118:119], v237 offset:63488\n"
"v_mfma_f32_16x16x32_bf16 v[132:135], a[0:3], v[32:35], 0\n"
"s_cmp_eq_u32 s85, 917504\n"
"ds_read_b64_tr_b16 a[128:129], v238 offset:32768\n"
"v_add_u32 v237, 65536, v237\n"
"v_mfma_f32_16x16x32_bf16 v[132:135], a[4:7], v[36:39], v[132:135]\n"
"s_cselect_b32 s85, 0, s85\n"
"ds_read_b64_tr_b16 a[130:131], v238 offset:34816\n"
"v_mfma_f32_16x16x32_bf16 v[132:135], a[16:19], v[40:43], v[132:135]\n"
"ds_read_b64_tr_b16 a[132:133], v238 offset:36864\n"
"v_mul_f32 v128, v240, v128\n"
"v_mfma_f32_16x16x32_bf16 v[132:135], a[20:23], v[44:47], v[132:135]\n"
"ds_read_b64_tr_b16 a[134:135], v238 offset:38912\n"
"v_mul_f32 v129, v240, v129\n"
"v_mfma_f32_16x16x32_bf16 v[132:135], a[32:35], v[48:51], v[132:135]\n"
"ds_read_b64_tr_b16 a[144:145], v238 offset:40960\n"
"v_mul_f32 v130, v240, v130\n"
"v_mfma_f32_16x16x32_bf16 v[132:135], a[36:39], v[52:55], v[132:135]\n"
"ds_read_b64_tr_b16 a[146:147], v238 offset:43008\n"
"v_mul_f32 v131, v240, v131\n"
"v_mfma_f32_16x16x32_bf16 v[132:135], a[48:51], v[56:59], v[132:135]\n"
"ds_read_b64_tr_b16 a[148:149], v238 offset:45056\n"
"v_mfma_f32_16x16x32_bf16 v[132:135], a[52:55], v[60:63], v[132:135]\n"
"ds_read_b64_tr_b16 a[150:151], v238 offset:47104\n"
"v_mfma_f32_16x16x32_bf16 v[136:139], a[64:67], v[0:3], 0\n"
"ds_read_b64_tr_b16 a[160:161], v238 offset:49152\n"
"v_mfma_f32_16x16x32_bf16 v[136:139], a[68:71], v[4:7], v[136:139]\n"
"ds_read_b64_tr_b16 a[162:163], v238 offset:51200\n"
"v_mfma_f32_16x16x32_bf16 v[136:139], a[80:83], v[8:11], v[136:139]\n"
"s_waitcnt lgkmcnt(8)\n"
"ds_read_b64_tr_b16 a[164:165], v238 offset:53248\n"
"v_mul_f32 v132, v241, v132\n"
"v_mfma_f32_16x16x32_bf16 v[136:139], a[84:87], v[12:15], v[136:139]\n"
"ds_read_b64_tr_b16 a[166:167], v238 offset:55296\n"
"v_mul_f32 v133, v241, v133\n"
"v_mfma_f32_16x16x32_bf16 v[136:139], a[96:99], v[16:19], v[136:139]\n"
"ds_read_b64_tr_b16 a[176:177], v238 offset:57344\n"
"v_mul_f32 v134, v241, v134\n"
"v_mfma_f32_16x16x32_bf16 v[136:139], a[100:103], v[20:23], v[136:139]\n"
"s_add_u32 m0, s95, 131072\n"
"ds_read_b64_tr_b16 a[178:179], v238 offset:59392\n"
"v_mul_f32 v135, v241, v135\n"
"v_mfma_f32_16x16x32_bf16 v[136:139], a[112:115], v[24:27], v[136:139]\n"
"buffer_load_dwordx4 v232, s[52:55], s72 offen lds\n"
"v_mfma_f32_16x16x32_bf16 v[136:139], a[116:119], v[28:31], v[136:139]\n"
"ds_read_b64_tr_b16 a[180:181], v238 offset:61440\n"
"ds_read_b64_tr_b16 a[182:183], v238 offset:63488\n"
"v_mfma_f32_16x16x32_bf16 v[140:143], a[64:67], v[32:35], 0\n"
"ds_read_b64_tr_b16 a[192:193], v239 offset:32768\n"
"v_add_u32 v238, 65536, v238\n"
"v_mfma_f32_16x16x32_bf16 v[140:143], a[68:71], v[36:39], v[140:143]\n"
"ds_read_b64_tr_b16 a[194:195], v239 offset:34816\n"
"v_mfma_f32_16x16x32_bf16 v[140:143], a[80:83], v[40:43], v[140:143]\n"
"s_add_u32 m0, s95, 135168\n"
"ds_read_b64_tr_b16 a[196:197], v239 offset:36864\n"
"v_mul_f32 v136, v240, v136\n"
"v_mfma_f32_16x16x32_bf16 v[140:143], a[84:87], v[44:47], v[140:143]\n"
"buffer_load_dwordx4 v232, s[52:55], s73 offen lds\n"
"v_mul_f32 v137, v240, v137\n"
"v_mfma_f32_16x16x32_bf16 v[140:143], a[96:99], v[48:51], v[140:143]\n"
"ds_read_b64_tr_b16 a[198:199], v239 offset:38912\n"
"ds_read_b64_tr_b16 a[208:209], v239 offset:40960\n"
"v_mfma_f32_16x16x32_bf16 v[140:143], a[100:103], v[52:55], v[140:143]\n"
"ds_read_b64_tr_b16 a[210:211], v239 offset:43008\n"
"v_mul_f32 v138, v240, v138\n"
"v_mfma_f32_16x16x32_bf16 v[140:143], a[112:115], v[56:59], v[140:143]\n"
"ds_read_b64_tr_b16 a[212:213], v239 offset:45056\n"
"v_mul_f32 v139, v240, v139\n"
"v_mfma_f32_16x16x32_bf16 v[140:143], a[116:119], v[60:63], v[140:143]\n"
"s_add_u32 m0, s95, 139264\n"
"ds_read_b64_tr_b16 a[214:215], v239 offset:47104\n"
"v_mfma_f32_16x16x32_bf16 v[144:147], a[128:131], v[0:3], 0\n"
"s_waitcnt lgkmcnt(8)\n"
"buffer_load_dwordx4 v232, s[52:55], s74 offen lds\n"
"v_mfma_f32_16x16x32_bf16 v[144:147], a[132:135], v[4:7], v[144:147]\n"
"ds_read_b64_tr_b16 a[224:225], v239 offset:49152\n"
"ds_read_b64_tr_b16 a[226:227], v239 offset:51200\n"
"v_mfma_f32_16x16x32_bf16 v[144:147], a[144:147], v[8:11], v[144:147]\n"
"ds_read_b64_tr_b16 a[228:229], v239 offset:53248\n"
"v_mul_f32 v140, v241, v140\n"
"v_mfma_f32_16x16x32_bf16 v[144:147], a[148:151], v[12:15], v[144:147]\n"
"ds_read_b64_tr_b16 a[230:231], v239 offset:55296\n"
"v_mul_f32 v141, v241, v141\n"
"v_mfma_f32_16x16x32_bf16 v[144:147], a[160:163], v[16:19], v[144:147]\n"
"s_add_u32 m0, s95, 143360\n"
"ds_read_b64_tr_b16 a[240:241], v239 offset:57344\n"
"v_mul_f32 v142, v241, v142\n"
"v_mfma_f32_16x16x32_bf16 v[144:147], a[164:167], v[20:23], v[144:147]\n"
"buffer_load_dwordx4 v232, s[52:55], s75 offen lds\n"
"v_mul_f32 v143, v241, v143\n"
"v_mfma_f32_16x16x32_bf16 v[144:147], a[176:179], v[24:27], v[144:147]\n"
"s_waitcnt vmcnt(21)\n"
"ds_read_b64_tr_b16 a[242:243], v239 offset:59392\n"
"ds_read_b64_tr_b16 a[244:245], v239 offset:61440\n"
"v_mfma_f32_16x16x32_bf16 v[144:147], a[180:183], v[28:31], v[144:147]\n"
"s_barrier\n"
"ds_read_b64_tr_b16 a[246:247], v239 offset:63488\n"
"v_mfma_f32_16x16x32_bf16 v[148:151], a[128:131], v[32:35], 0\n"
"ds_read_b64_tr_b16 a[0:1], v236 offset:32768\n"
"v_add_u32 v239, 65536, v239\n"
"v_mfma_f32_16x16x32_bf16 v[148:151], a[132:135], v[36:39], v[148:151]\n"
"s_add_u32 m0, s95, 147456\n"
"ds_read_b64_tr_b16 a[2:3], v236 offset:34816\n"
"v_mfma_f32_16x16x32_bf16 v[148:151], a[144:147], v[40:43], v[148:151]\n"
"buffer_load_dwordx4 v232, s[52:55], s76 offen lds\n"
"v_mul_f32 v144, v240, v144\n"
"v_mfma_f32_16x16x32_bf16 v[148:151], a[148:151], v[44:47], v[148:151]\n"
"ds_read_b64_tr_b16 a[4:5], v236 offset:36864\n"
"ds_read_b64_tr_b16 a[6:7], v236 offset:38912\n"
"v_mfma_f32_16x16x32_bf16 v[148:151], a[160:163], v[48:51], v[148:151]\n"
"ds_read_b64_tr_b16 a[16:17], v236 offset:40960\n"
"v_mul_f32 v145, v240, v145\n"
"v_mfma_f32_16x16x32_bf16 v[148:151], a[164:167], v[52:55], v[148:151]\n"
"ds_read_b64_tr_b16 a[18:19], v236 offset:43008\n"
"v_mul_f32 v146, v240, v146\n"
"v_mfma_f32_16x16x32_bf16 v[148:151], a[176:179], v[56:59], v[148:151]\n"
"s_add_u32 m0, s95, 151552\n"
"ds_read_b64_tr_b16 a[20:21], v236 offset:45056\n"
"v_mul_f32 v147, v240, v147\n"
"v_mfma_f32_16x16x32_bf16 v[148:151], a[180:183], v[60:63], v[148:151]\n"
"s_waitcnt lgkmcnt(8)\n"
"buffer_load_dwordx4 v232, s[52:55], s77 offen lds\n"
"v_mfma_f32_16x16x32_bf16 v[152:155], a[192:195], v[0:3], 0\n"
"ds_read_b64_tr_b16 a[22:23], v236 offset:47104\n"
"ds_read_b64_tr_b16 a[32:33], v236 offset:49152\n"
"v_mfma_f32_16x16x32_bf16 v[152:155], a[196:199], v[4:7], v[152:155]\n"
"ds_read_b64_tr_b16 a[34:35], v236 offset:51200\n"
"v_mfma_f32_16x16x32_bf16 v[152:155], a[208:211], v[8:11], v[152:155]\n"
"ds_read_b64_tr_b16 a[36:37], v236 offset:53248\n"
"v_mul_f32 v148, v241, v148\n"
"v_mfma_f32_16x16x32_bf16 v[152:155], a[212:215], v[12:15], v[152:155]\n"
"s_add_u32 m0, s95, 155648\n"
"ds_read_b64_tr_b16 a[38:39], v236 offset:55296\n"
"v_mul_f32 v149, v241, v149\n"
"v_mfma_f32_16x16x32_bf16 v[152:155], a[224:227], v[16:19], v[152:155]\n"
"buffer_load_dwordx4 v232, s[52:55], s78 offen lds\n"
"v_mul_f32 v150, v241, v150\n"
"v_mfma_f32_16x16x32_bf16 v[152:155], a[228:231], v[20:23], v[152:155]\n"
"ds_read_b64_tr_b16 a[48:49], v236 offset:57344\n"
"ds_read_b64_tr_b16 a[50:51], v236 offset:59392\n"
"v_mfma_f32_16x16x32_bf16 v[152:155], a[240:243], v[24:27], v[152:155]\n"
"s_waitcnt lgkmcnt(8)\n"
"ds_read_b64_tr_b16 a[52:53], v236 offset:61440\n"
"v_mul_f32 v151, v241, v151\n"
"v_mfma_f32_16x16x32_bf16 v[152:155], a[244:247], v[28:31], v[152:155]\n"
"ds_read_b64_tr_b16 a[54:55], v236 offset:63488\n"
"v_mfma_f32_16x16x32_bf16 v[156:159], a[192:195], v[32:35], 0\n"
"s_add_u32 m0, s95, 159744\n"
"ds_read_b64_tr_b16 a[64:65], v237 offset:32768\n"
"v_mfma_f32_16x16x32_bf16 v[156:159], a[196:199], v[36:39], v[156:159]\n"
"buffer_load_dwordx4 v232, s[52:55], s79 offen lds\n"
"v_mfma_f32_16x16x32_bf16 v[156:159], a[208:211], v[40:43], v[156:159]\n"
"ds_read_b64_tr_b16 a[66:67], v237 offset:34816\n"
"ds_read_b64_tr_b16 a[68:69], v237 offset:36864\n"
"v_mfma_f32_16x16x32_bf16 v[156:159], a[212:215], v[44:47], v[156:159]\n"
"ds_read_b64_tr_b16 a[70:71], v237 offset:38912\n"
"v_mul_f32 v152, v240, v152\n"
"v_mfma_f32_16x16x32_bf16 v[156:159], a[224:227], v[48:51], v[156:159]\n"
"ds_read_b64_tr_b16 a[80:81], v237 offset:40960\n"
"v_mul_f32 v153, v240, v153\n"
"v_mfma_f32_16x16x32_bf16 v[156:159], a[228:231], v[52:55], v[156:159]\n"
"s_cmp_ge_u32 s84, 768\n"
"ds_read_b64_tr_b16 a[82:83], v237 offset:43008\n"
"v_mul_f32 v154, v240, v154\n"
"v_mfma_f32_16x16x32_bf16 v[156:159], a[240:243], v[56:59], v[156:159]\n"
"s_cselect_b32 s86, -896, 0\n"
"ds_read_b64_tr_b16 a[84:85], v237 offset:45056\n"
"v_mul_f32 v155, v240, v155\n"
"v_mfma_f32_16x16x32_bf16 v[156:159], a[244:247], v[60:63], v[156:159]\n"
"s_add_u32 s86, s86, 128\n"
"ds_read_b64_tr_b16 a[86:87], v237 offset:47104\n"
"v_mfma_f32_16x16x32_bf16 v[128:131], a[0:3], v[64:67], v[128:131]\n"
"s_add_u32 s84, s84, s86\n"
"ds_read_b64_tr_b16 a[96:97], v237 offset:49152\n"
"v_add_u32 v232, s86, v232\n"
"v_mfma_f32_16x16x32_bf16 v[128:131], a[4:7], v[68:71], v[128:131]\n"
"s_add_u32 m0, s95, 0\n"
"ds_read_b64_tr_b16 a[98:99], v237 offset:51200\n"
"v_mfma_f32_16x16x32_bf16 v[128:131], a[16:19], v[72:75], v[128:131]\n"
"s_waitcnt lgkmcnt(9)\n"
"buffer_load_dwordx4 v232, s[52:55], s64 offen lds\n"
"v_mul_f32 v156, v241, v156\n"
"v_mfma_f32_16x16x32_bf16 v[128:131], a[20:23], v[76:79], v[128:131]\n"
"ds_read_b64_tr_b16 a[100:101], v237 offset:53248\n"
"ds_read_b64_tr_b16 a[102:103], v237 offset:55296\n"
"v_mfma_f32_16x16x32_bf16 v[128:131], a[32:35], v[80:83], v[128:131]\n"
"ds_read_b64_tr_b16 a[112:113], v237 offset:57344\n"
"v_mul_f32 v157, v241, v157\n"
"v_mfma_f32_16x16x32_bf16 v[128:131], a[36:39], v[84:87], v[128:131]\n"
"ds_read_b64_tr_b16 a[114:115], v237 offset:59392\n"
"v_mul_f32 v158, v241, v158\n"
"v_mfma_f32_16x16x32_bf16 v[128:131], a[48:51], v[88:91], v[128:131]\n"
"s_add_u32 m0, s95, 4096\n"
"ds_read_b64_tr_b16 a[116:117], v237 offset:61440\n"
"v_mul_f32 v159, v241, v159\n"
"v_mfma_f32_16x16x32_bf16 v[128:131], a[52:55], v[92:95], v[128:131]\n"
"s_waitcnt vmcnt(9)\n"
"buffer_load_dwordx4 v232, s[52:55], s65 offen lds\n"
"buffer_store_dwordx2 v[244:245], v234, s[56:59], 0 offen offset:256 sc1\n"
"v_mfma_f32_16x16x32_bf16 v[132:135], a[0:3], v[96:99], v[132:135]\n"
"ds_read_b64_tr_b16 a[118:119], v237 offset:63488\n"
"ds_read_b64_tr_b16 a[128:129], v238 offset:32768\n"
"v_mfma_f32_16x16x32_bf16 v[132:135], a[4:7], v[100:103], v[132:135]\n"
"ds_read_b64_tr_b16 a[130:131], v238 offset:34816\n"
"v_mfma_f32_16x16x32_bf16 v[132:135], a[16:19], v[104:107], v[132:135]\n"
"ds_read_b64_tr_b16 a[132:133], v238 offset:36864\n"
"v_cvt_pk_bf16_f32 v128, v128, v129\n"
"v_mfma_f32_16x16x32_bf16 v[132:135], a[20:23], v[108:111], v[132:135]\n"
"s_add_u32 m0, s95, 8192\n"
"ds_read_b64_tr_b16 a[134:135], v238 offset:38912\n"
"v_cvt_pk_bf16_f32 v129, v130, v131\n"
"v_mfma_f32_16x16x32_bf16 v[132:135], a[32:35], v[112:115], v[132:135]\n"
"buffer_load_dwordx4 v232, s[52:55], s66 offen lds\n"
"v_mfma_f32_16x16x32_bf16 v[132:135], a[36:39], v[116:119], v[132:135]\n"
"buffer_store_dwordx2 v[128:129], v233, s[56:59], s85 offen offset:0 sc1\n"
"ds_read_b64_tr_b16 a[144:145], v238 offset:40960\n"
"ds_read_b64_tr_b16 a[146:147], v238 offset:43008\n"
"v_mfma_f32_16x16x32_bf16 v[132:135], a[48:51], v[120:123], v[132:135]\n"
"ds_read_b64_tr_b16 a[148:149], v238 offset:45056\n"
"v_mfma_f32_16x16x32_bf16 v[132:135], a[52:55], v[124:127], v[132:135]\n"
"s_waitcnt lgkmcnt(8)\n"
"ds_read_b64_tr_b16 a[150:151], v238 offset:47104\n"
"v_mfma_f32_16x16x32_bf16 v[136:139], a[64:67], v[64:67], v[136:139]\n"
"s_add_u32 m0, s95, 12288\n"
"ds_read_b64_tr_b16 a[160:161], v238 offset:49152\n"
"v_mfma_f32_16x16x32_bf16 v[136:139], a[68:71], v[68:71], v[136:139]\n"
"buffer_load_dwordx4 v232, s[52:55], s67 offen lds\n"
"v_mfma_f32_16x16x32_bf16 v[136:139], a[80:83], v[72:75], v[136:139]\n"
"ds_read_b64_tr_b16 a[162:163], v238 offset:51200\n"
"ds_read_b64_tr_b16 a[164:165], v238 offset:53248\n"
"v_mfma_f32_16x16x32_bf16 v[136:139], a[84:87], v[76:79], v[136:139]\n"
"ds_read_b64_tr_b16 a[166:167], v238 offset:55296\n"
"v_cvt_pk_bf16_f32 v132, v132, v133\n"
"v_mfma_f32_16x16x32_bf16 v[136:139], a[96:99], v[80:83], v[136:139]\n"
"ds_read_b64_tr_b16 a[176:177], v238 offset:57344\n"
"v_cvt_pk_bf16_f32 v133, v134, v135\n"
"v_mfma_f32_16x16x32_bf16 v[136:139], a[100:103], v[84:87], v[136:139]\n"
"s_add_u32 m0, s95, 16384\n"
"buffer_store_dwordx2 v[132:133], v233, s[56:59], s85 offen offset:512 sc1\n"
"ds_read_b64_tr_b16 a[178:179], v238 offset:59392\n"
"v_mfma_f32_16x16x32_bf16 v[136:139], a[112:115], v[88:91], v[136:139]\n"
"s_waitcnt lgkmcnt(8)\n"
"buffer_load_dwordx4 v232, s[52:55], s68 offen lds\n"
"v_mfma_f32_16x16x32_bf16 v[136:139], a[116:119], v[92:95], v[136:139]\n"
"ds_read_b64_tr_b16 a[180:181], v238 offset:61440\n"
"ds_read_b64_tr_b16 a[182:183], v238 offset:63488\n"
"v_mfma_f32_16x16x32_bf16 v[140:143], a[64:67], v[96:99], v[140:143]\n"
"ds_read_b64_tr_b16 a[192:193], v239 offset:32768\n"
"v_mfma_f32_16x16x32_bf16 v[140:143], a[68:71], v[100:103], v[140:143]\n"
"ds_read_b64_tr_b16 a[194:195], v239 offset:34816\n"
"v_mfma_f32_16x16x32_bf16 v[140:143], a[80:83], v[104:107], v[140:143]\n"
"s_add_u32 m0, s95, 20480\n"
"ds_read_b64_tr_b16 a[196:197], v239 offset:36864\n"
"v_cvt_pk_bf16_f32 v136, v136, v137\n"
"v_mfma_f32_16x16x32_bf16 v[140:143], a[84:87], v[108:111], v[140:143]\n"
"buffer_load_dwordx4 v232, s[52:55], s69 offen lds\n"
"v_cvt_pk_bf16_f32 v137, v138, v139\n"
"v_mfma_f32_16x16x32_bf16 v[140:143], a[96:99], v[112:115], v[140:143]\n"
"ds_read_b64_tr_b16 a[198:199], v239 offset:38912\n"
"ds_read_b64_tr_b16 a[208:209], v239 offset:40960\n"
"v_mfma_f32_16x16x32_bf16 v[140:143], a[100:103], v[116:119], v[140:143]\n"
"buffer_store_dwordx2 v[136:137], v233, s[56:59], s85 offen offset:1024 sc1\n"
"ds_read_b64_tr_b16 a[210:211], v239 offset:43008\n"
"v_mfma_f32_16x16x32_bf16 v[140:143], a[112:115], v[120:123], v[140:143]\n"
"ds_read_b64_tr_b16 a[212:213], v239 offset:45056\n"
"v_mfma_f32_16x16x32_bf16 v[140:143], a[116:119], v[124:127], v[140:143]\n"
"s_add_u32 m0, s95, 24576\n"
"ds_read_b64_tr_b16 a[214:215], v239 offset:47104\n"
"v_mfma_f32_16x16x32_bf16 v[144:147], a[128:131], v[64:67], v[144:147]\n"
"buffer_load_dwordx4 v232, s[52:55], s70 offen lds\n"
"v_mfma_f32_16x16x32_bf16 v[144:147], a[132:135], v[68:71], v[144:147]\n"
"ds_read_b64_tr_b16 a[224:225], v239 offset:49152\n"
"ds_read_b64_tr_b16 a[226:227], v239 offset:51200\n"
"v_mfma_f32_16x16x32_bf16 v[144:147], a[144:147], v[72:75], v[144:147]\n"
"s_waitcnt lgkmcnt(8)\n"
"ds_read_b64_tr_b16 a[228:229], v239 offset:53248\n"
"v_cvt_pk_bf16_f32 v140, v140, v141\n"
"v_mfma_f32_16x16x32_bf16 v[144:147], a[148:151], v[76:79], v[144:147]\n"
"ds_read_b64_tr_b16 a[230:231], v239 offset:55296\n"
"v_cvt_pk_bf16_f32 v141, v142, v143\n"
"v_mfma_f32_16x16x32_bf16 v[144:147], a[160:163], v[80:83], v[144:147]\n"
"s_add_u32 m0, s95, 28672\n"
"ds_read_b64_tr_b16 a[240:241], v239 offset:57344\n"
"v_mfma_f32_16x16x32_bf16 v[144:147], a[164:167], v[84:87], v[144:147]\n"
"buffer_load_dwordx4 v232, s[52:55], s71 offen lds\n"
"v_mfma_f32_16x16x32_bf16 v[144:147], a[176:179], v[88:91], v[144:147]\n"
"s_waitcnt vmcnt(25)\n"
"buffer_store_dwordx2 v[140:141], v233, s[56:59], s85 offen offset:1536 sc1\n"
"ds_read_b64_tr_b16 a[242:243], v239 offset:59392\n"
"ds_read_b64_tr_b16 a[244:245], v239 offset:61440\n"
"v_mfma_f32_16x16x32_bf16 v[144:147], a[180:183], v[92:95], v[144:147]\n"
"s_barrier\n"
"ds_read_b64_tr_b16 a[246:247], v239 offset:63488\n"
"v_mfma_f32_16x16x32_bf16 v[148:151], a[128:131], v[96:99], v[148:151]\n"
"ds_read_b64_tr_b16 a[0:1], v236 offset:0\n"
"v_mfma_f32_16x16x32_bf16 v[148:151], a[132:135], v[100:103], v[148:151]\n"
"ds_read_b64_tr_b16 a[2:3], v236 offset:2048\n"
"v_mfma_f32_16x16x32_bf16 v[148:151], a[144:147], v[104:107], v[148:151]\n"
"ds_read_b64_tr_b16 a[4:5], v236 offset:4096\n"
"v_cvt_pk_bf16_f32 v144, v144, v145\n"
"v_mfma_f32_16x16x32_bf16 v[148:151], a[148:151], v[108:111], v[148:151]\n"
"ds_read_b64_tr_b16 a[6:7], v236 offset:6144\n"
"v_cvt_pk_bf16_f32 v145, v146, v147\n"
"v_mfma_f32_16x16x32_bf16 v[148:151], a[160:163], v[112:115], v[148:151]\n"
"ds_read_b64_tr_b16 a[16:17], v236 offset:8192\n"
"v_mfma_f32_16x16x32_bf16 v[148:151], a[164:167], v[116:119], v[148:151]\n"
"ds_read_b64_tr_b16 a[18:19], v236 offset:10240\n"
"v_mfma_f32_16x16x32_bf16 v[148:151], a[176:179], v[120:123], v[148:151]\n"
"buffer_store_dwordx2 v[144:145], v233, s[56:59], s85 offen offset:2048 sc1\n"
"ds_read_b64_tr_b16 a[20:21], v236 offset:12288\n"
"v_mfma_f32_16x16x32_bf16 v[148:151], a[180:183], v[124:127], v[148:151]\n"
"ds_read_b64_tr_b16 a[22:23], v236 offset:14336\n"
"v_mfma_f32_16x16x32_bf16 v[152:155], a[192:195], v[64:67], v[152:155]\n"
"s_waitcnt lgkmcnt(8)\n"
"ds_read_b64_tr_b16 a[32:33], v236 offset:16384\n"
"v_mfma_f32_16x16x32_bf16 v[152:155], a[196:199], v[68:71], v[152:155]\n"
"ds_read_b64_tr_b16 a[34:35], v236 offset:18432\n"
"v_mfma_f32_16x16x32_bf16 v[152:155], a[208:211], v[72:75], v[152:155]\n"
"ds_read_b64_tr_b16 a[36:37], v236 offset:20480\n"
"v_cvt_pk_bf16_f32 v148, v148, v149\n"
"v_mfma_f32_16x16x32_bf16 v[152:155], a[212:215], v[76:79], v[152:155]\n"
"ds_read_b64_tr_b16 a[38:39], v236 offset:22528\n"
"v_cvt_pk_bf16_f32 v149, v150, v151\n"
"v_mfma_f32_16x16x32_bf16 v[152:155], a[224:227], v[80:83], v[152:155]\n"
"ds_read_b64_tr_b16 a[48:49], v236 offset:24576\n"
"v_mfma_f32_16x16x32_bf16 v[152:155], a[228:231], v[84:87], v[152:155]\n"
"ds_read_b64_tr_b16 a[50:51], v236 offset:26624\n"
"v_mfma_f32_16x16x32_bf16 v[152:155], a[240:243], v[88:91], v[152:155]\n"
"buffer_store_dwordx2 v[148:149], v233, s[56:59], s85 offen offset:2560 sc1\n"
"ds_read_b64_tr_b16 a[52:53], v236 offset:28672\n"
"v_mfma_f32_16x16x32_bf16 v[152:155], a[244:247], v[92:95], v[152:155]\n"
"ds_read_b64_tr_b16 a[54:55], v236 offset:30720\n"
"v_mfma_f32_16x16x32_bf16 v[156:159], a[192:195], v[96:99], v[156:159]\n"
"ds_read_b64_tr_b16 a[64:65], v237 offset:0\n"
"v_add_u32 v236, 65536, v236\n"
"v_mfma_f32_16x16x32_bf16 v[156:159], a[196:199], v[100:103], v[156:159]\n"
"ds_read_b64_tr_b16 a[66:67], v237 offset:2048\n"
"v_mfma_f32_16x16x32_bf16 v[156:159], a[208:211], v[104:107], v[156:159]\n"
"ds_read_b64_tr_b16 a[68:69], v237 offset:4096\n"
"v_cvt_pk_bf16_f32 v152, v152, v153\n"
"v_mfma_f32_16x16x32_bf16 v[156:159], a[212:215], v[108:111], v[156:159]\n"
"ds_read_b64_tr_b16 a[70:71], v237 offset:6144\n"
"v_cvt_pk_bf16_f32 v153, v154, v155\n"
"v_mfma_f32_16x16x32_bf16 v[156:159], a[224:227], v[112:115], v[156:159]\n"
"ds_read_b64_tr_b16 a[80:81], v237 offset:8192\n"
"v_mfma_f32_16x16x32_bf16 v[156:159], a[228:231], v[116:119], v[156:159]\n"
"ds_read_b64_tr_b16 a[82:83], v237 offset:10240\n"
"v_mfma_f32_16x16x32_bf16 v[156:159], a[240:243], v[120:123], v[156:159]\n"
"buffer_store_dwordx2 v[152:153], v233, s[56:59], s85 offen offset:3072 sc1\n"
"ds_read_b64_tr_b16 a[84:85], v237 offset:12288\n"
"v_mfma_f32_16x16x32_bf16 v[156:159], a[244:247], v[124:127], v[156:159]\n"
"s_waitcnt lgkmcnt(8)\n"
"ds_read_b64_tr_b16 a[86:87], v237 offset:14336\n"
"v_mfma_f32_16x16x32_bf16 v[160:163], a[0:3], v[0:3], 0\n"
"ds_read_b64_tr_b16 a[96:97], v237 offset:16384\n"
"v_mfma_f32_16x16x32_bf16 v[160:163], a[4:7], v[4:7], v[160:163]\n"
"ds_read_b64_tr_b16 a[98:99], v237 offset:18432\n"
"v_mfma_f32_16x16x32_bf16 v[160:163], a[16:19], v[8:11], v[160:163]\n"
"ds_read_b64_tr_b16 a[100:101], v237 offset:20480\n"
"v_cvt_pk_bf16_f32 v156, v156, v157\n"
"v_mfma_f32_16x16x32_bf16 v[160:163], a[20:23], v[12:15], v[160:163]\n"
"ds_read_b64_tr_b16 a[102:103], v237 offset:22528\n"
"v_cvt_pk_bf16_f32 v157, v158, v159\n"
"v_mfma_f32_16x16x32_bf16 v[160:163], a[32:35], v[16:19], v[160:163]\n"
"ds_read_b64_tr_b16 a[112:113], v237 offset:24576\n"
"v_mfma_f32_16x16x32_bf16 v[160:163], a[36:39], v[20:23], v[160:163]\n"
"ds_read_b64_tr_b16 a[114:115], v237 offset:26624\n"
"v_mfma_f32_16x16x32_bf16 v[160:163], a[48:51], v[24:27], v[160:163]\n"
"s_waitcnt lgkmcnt(8)\n"
"buffer_store_dwordx2 v[156:157], v233, s[56:59], s85 offen offset:3584 sc1\n"
"ds_read_b64_tr_b16 a[116:117], v237 offset:28672\n"
"v_mfma_f32_16x16x32_bf16 v[160:163], a[52:55], v[28:31], v[160:163]\n"
"s_add_u32 s85, s85, 131072\n"
"ds_read_b64_tr_b16 a[118:119], v237 offset:30720\n"
"v_mfma_f32_16x16x32_bf16 v[164:167], a[0:3], v[32:35], 0\n"
"s_cmp_eq_u32 s85, 917504\n"
"ds_read_b64_tr_b16 a[128:129], v238 offset:0\n"
"v_add_u32 v237, 65536, v237\n"
"v_mfma_f32_16x16x32_bf16 v[164:167], a[4:7], v[36:39], v[164:167]\n"
"s_cselect_b32 s85, 0, s85\n"
"ds_read_b64_tr_b16 a[130:131], v238 offset:2048\n"
"v_mfma_f32_16x16x32_bf16 v[164:167], a[16:19], v[40:43], v[164:167]\n"
"ds_read_b64_tr_b16 a[132:133], v238 offset:4096\n"
"v_mul_f32 v160, v240, v160\n"
"v_mfma_f32_16x16x32_bf16 v[164:167], a[20:23], v[44:47], v[164:167]\n"
"ds_read_b64_tr_b16 a[134:135], v238 offset:6144\n"
"v_mul_f32 v161, v240, v161\n"
"v_mfma_f32_16x16x32_bf16 v[164:167], a[32:35], v[48:51], v[164:167]\n"
"ds_read_b64_tr_b16 a[144:145], v238 offset:8192\n"
"v_mul_f32 v162, v240, v162\n"
"v_mfma_f32_16x16x32_bf16 v[164:167], a[36:39], v[52:55], v[164:167]\n"
"ds_read_b64_tr_b16 a[146:147], v238 offset:10240\n"
"v_mul_f32 v163, v240, v163\n"
"v_mfma_f32_16x16x32_bf16 v[164:167], a[48:51], v[56:59], v[164:167]\n"
"ds_read_b64_tr_b16 a[148:149], v238 offset:12288\n"
"v_mfma_f32_16x16x32_bf16 v[164:167], a[52:55], v[60:63], v[164:167]\n"
"ds_read_b64_tr_b16 a[150:151], v238 offset:14336\n"
"v_mfma_f32_16x16x32_bf16 v[168:171], a[64:67], v[0:3], 0\n"
"ds_read_b64_tr_b16 a[160:161], v238 offset:16384\n"
"v_mfma_f32_16x16x32_bf16 v[168:171], a[68:71], v[4:7], v[168:171]\n"
"ds_read_b64_tr_b16 a[162:163], v238 offset:18432\n"
"v_mfma_f32_16x16x32_bf16 v[168:171], a[80:83], v[8:11], v[168:171]\n"
"s_waitcnt lgkmcnt(8)\n"
"ds_read_b64_tr_b16 a[164:165], v238 offset:20480\n"
"v_mul_f32 v164, v241, v164\n"
"v_mfma_f32_16x16x32_bf16 v[168:171], a[84:87], v[12:15], v[168:171]\n"
"ds_read_b64_tr_b16 a[166:167], v238 offset:22528\n"
"v_mul_f32 v165, v241, v165\n"
"v_mfma_f32_16x16x32_bf16 v[168:171], a[96:99], v[16:19], v[168:171]\n"
"ds_read_b64_tr_b16 a[176:177], v238 offset:24576\n"
"v_mul_f32 v166, v241, v166\n"
"v_mfma_f32_16x16x32_bf16 v[168:171], a[100:103], v[20:23], v[168:171]\n"
"s_add_u32 m0, s95, 32768\n"
"ds_read_b64_tr_b16 a[178:179], v238 offset:26624\n"
"v_mul_f32 v167, v241, v167\n"
"v_mfma_f32_16x16x32_bf16 v[168:171], a[112:115], v[24:27], v[168:171]\n"
"buffer_load_dwordx4 v232, s[52:55], s72 offen lds\n"
"v_mfma_f32_16x16x32_bf16 v[168:171], a[116:119], v[28:31], v[168:171]\n"
"ds_read_b64_tr_b16 a[180:181], v238 offset:28672\n"
"ds_read_b64_tr_b16 a[182:183], v238 offset:30720\n"
"v_mfma_f32_16x16x32_bf16 v[172:175], a[64:67], v[32:35], 0\n"
"ds_read_b64_tr_b16 a[192:193], v239 offset:0\n"
"v_add_u32 v238, 65536, v238\n"
"v_mfma_f32_16x16x32_bf16 v[172:175], a[68:71], v[36:39], v[172:175]\n"
"ds_read_b64_tr_b16 a[194:195], v239 offset:2048\n"
"v_mfma_f32_16x16x32_bf16 v[172:175], a[80:83], v[40:43], v[172:175]\n"
"s_add_u32 m0, s95, 36864\n"
"ds_read_b64_tr_b16 a[196:197], v239 offset:4096\n"
"v_mul_f32 v168, v240, v168\n"
"v_mfma_f32_16x16x32_bf16 v[172:175], a[84:87], v[44:47], v[172:175]\n"
"buffer_load_dwordx4 v232, s[52:55], s73 offen lds\n"
"v_mul_f32 v169, v240, v169\n"
"v_mfma_f32_16x16x32_bf16 v[172:175], a[96:99], v[48:51], v[172:175]\n"
"ds_read_b64_tr_b16 a[198:199], v239 offset:6144\n"
"ds_read_b64_tr_b16 a[208:209], v239 offset:8192\n"
"v_mfma_f32_16x16x32_bf16 v[172:175], a[100:103], v[52:55], v[172:175]\n"
"ds_read_b64_tr_b16 a[210:211], v239 offset:10240\n"
"v_mul_f32 v170, v240, v170\n"
"v_mfma_f32_16x16x32_bf16 v[172:175], a[112:115], v[56:59], v[172:175]\n"
"ds_read_b64_tr_b16 a[212:213], v239 offset:12288\n"
"v_mul_f32 v171, v240, v171\n"
"v_mfma_f32_16x16x32_bf16 v[172:175], a[116:119], v[60:63], v[172:175]\n"
"s_add_u32 m0, s95, 40960\n"
"ds_read_b64_tr_b16 a[214:215], v239 offset:14336\n"
"v_mfma_f32_16x16x32_bf16 v[176:179], a[128:131], v[0:3], 0\n"
"s_waitcnt lgkmcnt(8)\n"
"buffer_load_dwordx4 v232, s[52:55], s74 offen lds\n"
"v_mfma_f32_16x16x32_bf16 v[176:179], a[132:135], v[4:7], v[176:179]\n"
"ds_read_b64_tr_b16 a[224:225], v239 offset:16384\n"
"ds_read_b64_tr_b16 a[226:227], v239 offset:18432\n"
"v_mfma_f32_16x16x32_bf16 v[176:179], a[144:147], v[8:11], v[176:179]\n"
"ds_read_b64_tr_b16 a[228:229], v239 offset:20480\n"
"v_mul_f32 v172, v241, v172\n"
"v_mfma_f32_16x16x32_bf16 v[176:179], a[148:151], v[12:15], v[176:179]\n"
"ds_read_b64_tr_b16 a[230:231], v239 offset:22528\n"
"v_mul_f32 v173, v241, v173\n"
"v_mfma_f32_16x16x32_bf16 v[176:179], a[160:163], v[16:19], v[176:179]\n"
"s_add_u32 m0, s95, 45056\n"
"ds_read_b64_tr_b16 a[240:241], v239 offset:24576\n"
"v_mul_f32 v174, v241, v174\n"
"v_mfma_f32_16x16x32_bf16 v[176:179], a[164:167], v[20:23], v[176:179]\n"
"buffer_load_dwordx4 v232, s[52:55], s75 offen lds\n"
"v_mul_f32 v175, v241, v175\n"
"v_mfma_f32_16x16x32_bf16 v[176:179], a[176:179], v[24:27], v[176:179]\n"
"s_waitcnt vmcnt(21)\n"
"ds_read_b64_tr_b16 a[242:243], v239 offset:26624\n"
"ds_read_b64_tr_b16 a[244:245], v239 offset:28672\n"
"v_mfma_f32_16x16x32_bf16 v[176:179], a[180:183], v[28:31], v[176:179]\n"
"s_barrier\n"
"ds_read_b64_tr_b16 a[246:247], v239 offset:30720\n"
"v_mfma_f32_16x16x32_bf16 v[180:183], a[128:131], v[32:35], 0\n"
"ds_read_b64_tr_b16 a[0:1], v236 offset:0\n"
"v_add_u32 v239, 65536, v239\n"
"v_mfma_f32_16x16x32_bf16 v[180:183], a[132:135], v[36:39], v[180:183]\n"
"s_add_u32 m0, s95, 49152\n"
"ds_read_b64_tr_b16 a[2:3], v236 offset:2048\n"
"v_mfma_f32_16x16x32_bf16 v[180:183], a[144:147], v[40:43], v[180:183]\n"
"buffer_load_dwordx4 v232, s[52:55], s76 offen lds\n"
"v_mul_f32 v176, v240, v176\n"
"v_mfma_f32_16x16x32_bf16 v[180:183], a[148:151], v[44:47], v[180:183]\n"
"ds_read_b64_tr_b16 a[4:5], v236 offset:4096\n"
"ds_read_b64_tr_b16 a[6:7], v236 offset:6144\n"
"v_mfma_f32_16x16x32_bf16 v[180:183], a[160:163], v[48:51], v[180:183]\n"
"ds_read_b64_tr_b16 a[16:17], v236 offset:8192\n"
"v_mul_f32 v177, v240, v177\n"
"v_mfma_f32_16x16x32_bf16 v[180:183], a[164:167], v[52:55], v[180:183]\n"
"ds_read_b64_tr_b16 a[18:19], v236 offset:10240\n"
"v_mul_f32 v178, v240, v178\n"
"v_mfma_f32_16x16x32_bf16 v[180:183], a[176:179], v[56:59], v[180:183]\n"
"s_add_u32 m0, s95, 53248\n"
"ds_read_b64_tr_b16 a[20:21], v236 offset:12288\n"
"v_mul_f32 v179, v240, v179\n"
"v_mfma_f32_16x16x32_bf16 v[180:183], a[180:183], v[60:63], v[180:183]\n"
"s_waitcnt lgkmcnt(8)\n"
"buffer_load_dwordx4 v232, s[52:55], s77 offen lds\n"
"v_mfma_f32_16x16x32_bf16 v[184:187], a[192:195], v[0:3], 0\n"
"ds_read_b64_tr_b16 a[22:23], v236 offset:14336\n"
"ds_read_b64_tr_b16 a[32:33], v236 offset:16384\n"
"buffer_load_dwordx2 v[246:247], v235, s[56:59], 0 offen sc1\n"
"v_mfma_f32_16x16x32_bf16 v[184:187], a[196:199], v[4:7], v[184:187]\n"
"ds_read_b64_tr_b16 a[34:35], v236 offset:18432\n"
"v_mfma_f32_16x16x32_bf16 v[184:187], a[208:211], v[8:11], v[184:187]\n"
"ds_read_b64_tr_b16 a[36:37], v236 offset:20480\n"
"v_mul_f32 v180, v241, v180\n"
"v_mfma_f32_16x16x32_bf16 v[184:187], a[212:215], v[12:15], v[184:187]\n"
"s_add_u32 m0, s95, 57344\n"
"ds_read_b64_tr_b16 a[38:39], v236 offset:22528\n"
"v_mul_f32 v181, v241, v181\n"
"v_mfma_f32_16x16x32_bf16 v[184:187], a[224:227], v[16:19], v[184:187]\n"
"buffer_load_dwordx4 v232, s[52:55], s78 offen lds\n"
"v_mul_f32 v182, v241, v182\n"
"v_mfma_f32_16x16x32_bf16 v[184:187], a[228:231], v[20:23], v[184:187]\n"
"ds_read_b64_tr_b16 a[48:49], v236 offset:24576\n"
"ds_read_b64_tr_b16 a[50:51], v236 offset:26624\n"
"v_mfma_f32_16x16x32_bf16 v[184:187], a[240:243], v[24:27], v[184:187]\n"
"s_waitcnt lgkmcnt(8)\n"
"ds_read_b64_tr_b16 a[52:53], v236 offset:28672\n"
"v_mul_f32 v183, v241, v183\n"
"v_mfma_f32_16x16x32_bf16 v[184:187], a[244:247], v[28:31], v[184:187]\n"
"ds_read_b64_tr_b16 a[54:55], v236 offset:30720\n"
"v_mfma_f32_16x16x32_bf16 v[188:191], a[192:195], v[32:35], 0\n"
"s_add_u32 m0, s95, 61440\n"
"ds_read_b64_tr_b16 a[64:65], v237 offset:0\n"
"v_add_u32 v236, -131072, v236\n"
"v_mfma_f32_16x16x32_bf16 v[188:191], a[196:199], v[36:39], v[188:191]\n"
"buffer_load_dwordx4 v232, s[52:55], s79 offen lds\n"
"v_mfma_f32_16x16x32_bf16 v[188:191], a[208:211], v[40:43], v[188:191]\n"
"ds_read_b64_tr_b16 a[66:67], v237 offset:2048\n"
"ds_read_b64_tr_b16 a[68:69], v237 offset:4096\n"
"v_mfma_f32_16x16x32_bf16 v[188:191], a[212:215], v[44:47], v[188:191]\n"
"ds_read_b64_tr_b16 a[70:71], v237 offset:6144\n"
"v_mul_f32 v184, v240, v184\n"
"v_mfma_f32_16x16x32_bf16 v[188:191], a[224:227], v[48:51], v[188:191]\n"
"ds_read_b64_tr_b16 a[80:81], v237 offset:8192\n"
"v_mul_f32 v185, v240, v185\n"
"v_mfma_f32_16x16x32_bf16 v[188:191], a[228:231], v[52:55], v[188:191]\n"
"s_cmp_ge_u32 s84, 768\n"
"ds_read_b64_tr_b16 a[82:83], v237 offset:10240\n"
"v_mul_f32 v186, v240, v186\n"
"v_mfma_f32_16x16x32_bf16 v[188:191], a[240:243], v[56:59], v[188:191]\n"
"s_cselect_b32 s86, -896, 0\n"
"ds_read_b64_tr_b16 a[84:85], v237 offset:12288\n"
"v_mul_f32 v187, v240, v187\n"
"v_mfma_f32_16x16x32_bf16 v[188:191], a[244:247], v[60:63], v[188:191]\n"
"s_add_u32 s86, s86, 128\n"
"ds_read_b64_tr_b16 a[86:87], v237 offset:14336\n"
"v_mfma_f32_16x16x32_bf16 v[160:163], a[0:3], v[64:67], v[160:163]\n"
"s_add_u32 s84, s84, s86\n"
"ds_read_b64_tr_b16 a[96:97], v237 offset:16384\n"
"v_add_u32 v232, s86, v232\n"
"v_mfma_f32_16x16x32_bf16 v[160:163], a[4:7], v[68:71], v[160:163]\n"
"s_add_u32 m0, s95, 98304\n"
"ds_read_b64_tr_b16 a[98:99], v237 offset:18432\n"
"v_mfma_f32_16x16x32_bf16 v[160:163], a[16:19], v[72:75], v[160:163]\n"
"s_waitcnt lgkmcnt(9)\n"
"buffer_load_dwordx4 v232, s[52:55], s64 offen lds\n"
"v_mul_f32 v188, v241, v188\n"
"v_mfma_f32_16x16x32_bf16 v[160:163], a[20:23], v[76:79], v[160:163]\n"
"ds_read_b64_tr_b16 a[100:101], v237 offset:20480\n"
"ds_read_b64_tr_b16 a[102:103], v237 offset:22528\n"
"v_mfma_f32_16x16x32_bf16 v[160:163], a[32:35], v[80:83], v[160:163]\n"
"ds_read_b64_tr_b16 a[112:113], v237 offset:24576\n"
"v_mul_f32 v189, v241, v189\n"
"v_mfma_f32_16x16x32_bf16 v[160:163], a[36:39], v[84:87], v[160:163]\n"
"ds_read_b64_tr_b16 a[114:115], v237 offset:26624\n"
"v_mul_f32 v190, v241, v190\n"
"v_mfma_f32_16x16x32_bf16 v[160:163], a[48:51], v[88:91], v[160:163]\n"
"s_add_u32 m0, s95, 102400\n"
"ds_read_b64_tr_b16 a[116:117], v237 offset:28672\n"
"v_mul_f32 v191, v241, v191\n"
"v_mfma_f32_16x16x32_bf16 v[160:163], a[52:55], v[92:95], v[160:163]\n"
"s_waitcnt vmcnt(10)\n"
"ds_read_b64_tr_b16 a[118:119], v237 offset:30720\n"
"buffer_load_dwordx4 v232, s[52:55], s65 offen lds\n"
"buffer_store_dwordx2 v[244:245], v234, s[56:59], 0 offen offset:384 sc1\n"
"v_mfma_f32_16x16x32_bf16 v[164:167], a[0:3], v[96:99], v[164:167]\n"
"ds_read_b64_tr_b16 a[128:129], v238 offset:0\n"
"v_add_u32 v237, -131072, v237\n"
"v_mfma_f32_16x16x32_bf16 v[164:167], a[4:7], v[100:103], v[164:167]\n"
"ds_read_b64_tr_b16 a[130:131], v238 offset:2048\n"
"v_mfma_f32_16x16x32_bf16 v[164:167], a[16:19], v[104:107], v[164:167]\n"
"ds_read_b64_tr_b16 a[132:133], v238 offset:4096\n"
"v_cvt_pk_bf16_f32 v160, v160, v161\n"
"v_mfma_f32_16x16x32_bf16 v[164:167], a[20:23], v[108:111], v[164:167]\n"
"s_add_u32 m0, s95, 106496\n"
"ds_read_b64_tr_b16 a[134:135], v238 offset:6144\n"
"v_cvt_pk_bf16_f32 v161, v162, v163\n"
"v_mfma_f32_16x16x32_bf16 v[164:167], a[32:35], v[112:115], v[164:167]\n"
"buffer_load_dwordx4 v232, s[52:55], s66 offen lds\n"
"v_mfma_f32_16x16x32_bf16 v[164:167], a[36:39], v[116:119], v[164:167]\n"
"buffer_store_dwordx2 v[160:161], v233, s[56:59], s85 offen offset:0 sc1\n"
"ds_read_b64_tr_b16 a[144:145], v238 offset:8192\n"
"ds_read_b64_tr_b16 a[146:147], v238 offset:10240\n"
"v_mfma_f32_16x16x32_bf16 v[164:167], a[48:51], v[120:123], v[164:167]\n"
"ds_read_b64_tr_b16 a[148:149], v238 offset:12288\n"
"v_mfma_f32_16x16x32_bf16 v[164:167], a[52:55], v[124:127], v[164:167]\n"
"s_waitcnt lgkmcnt(8)\n"
"ds_read_b64_tr_b16 a[150:151], v238 offset:14336\n"
"v_mfma_f32_16x16x32_bf16 v[168:171], a[64:67], v[64:67], v[168:171]\n"
"s_add_u32 m0, s95, 110592\n"
"ds_read_b64_tr_b16 a[160:161], v238 offset:16384\n"
"v_mfma_f32_16x16x32_bf16 v[168:171], a[68:71], v[68:71], v[168:171]\n"
"buffer_load_dwordx4 v232, s[52:55], s67 offen lds\n"
"v_mfma_f32_16x16x32_bf16 v[168:171], a[80:83], v[72:75], v[168:171]\n"
"ds_read_b64_tr_b16 a[162:163], v238 offset:18432\n"
"ds_read_b64_tr_b16 a[164:165], v238 offset:20480\n"
"v_mfma_f32_16x16x32_bf16 v[168:171], a[84:87], v[76:79], v[168:171]\n"
"ds_read_b64_tr_b16 a[166:167], v238 offset:22528\n"
"v_cvt_pk_bf16_f32 v164, v164, v165\n"
"v_mfma_f32_16x16x32_bf16 v[168:171], a[96:99], v[80:83], v[168:171]\n"
"ds_read_b64_tr_b16 a[176:177], v238 offset:24576\n"
"v_cvt_pk_bf16_f32 v165, v166, v167\n"
"v_mfma_f32_16x16x32_bf16 v[168:171], a[100:103], v[84:87], v[168:171]\n"
"s_add_u32 m0, s95, 114688\n"
"buffer_store_dwordx2 v[164:165], v233, s[56:59], s85 offen offset:512 sc1\n"
"ds_read_b64_tr_b16 a[178:179], v238 offset:26624\n"
"v_mfma_f32_16x16x32_bf16 v[168:171], a[112:115], v[88:91], v[168:171]\n"
"s_waitcnt lgkmcnt(8)\n"
"buffer_load_dwordx4 v232, s[52:55], s68 offen lds\n"
"v_mfma_f32_16x16x32_bf16 v[168:171], a[116:119], v[92:95], v[168:171]\n"
"ds_read_b64_tr_b16 a[180:181], v238 offset:28672\n"
"ds_read_b64_tr_b16 a[182:183], v238 offset:30720\n"
"v_mfma_f32_16x16x32_bf16 v[172:175], a[64:67], v[96:99], v[172:175]\n"
"ds_read_b64_tr_b16 a[192:193], v239 offset:0\n"
"v_add_u32 v238, -131072, v238\n"
"v_mfma_f32_16x16x32_bf16 v[172:175], a[68:71], v[100:103], v[172:175]\n"
"ds_read_b64_tr_b16 a[194:195], v239 offset:2048\n"
"v_mfma_f32_16x16x32_bf16 v[172:175], a[80:83], v[104:107], v[172:175]\n"
"s_add_u32 m0, s95, 118784\n"
"ds_read_b64_tr_b16 a[196:197], v239 offset:4096\n"
"v_cvt_pk_bf16_f32 v168, v168, v169\n"
"v_mfma_f32_16x16x32_bf16 v[172:175], a[84:87], v[108:111], v[172:175]\n"
"buffer_load_dwordx4 v232, s[52:55], s69 offen lds\n"
"v_cvt_pk_bf16_f32 v169, v170, v171\n"
"v_mfma_f32_16x16x32_bf16 v[172:175], a[96:99], v[112:115], v[172:175]\n"
"ds_read_b64_tr_b16 a[198:199], v239 offset:6144\n"
"ds_read_b64_tr_b16 a[208:209], v239 offset:8192\n"
"v_mfma_f32_16x16x32_bf16 v[172:175], a[100:103], v[116:119], v[172:175]\n"
"s_waitcnt vmcnt(11)\n"
"buffer_store_dwordx2 v[168:169], v233, s[56:59], s85 offen offset:1024 sc1\n"
"ds_read_b64_tr_b16 a[210:211], v239 offset:10240\n"
"v_mfma_f32_16x16x32_bf16 v[172:175], a[112:115], v[120:123], v[172:175]\n"
"ds_read_b64_tr_b16 a[212:213], v239 offset:12288\n"
"v_cmp_eq_u64 vcc, v[246:247], v[244:245]\n"
"v_mfma_f32_16x16x32_bf16 v[172:175], a[116:119], v[124:127], v[172:175]\n"
"s_add_u32 m0, s95, 122880\n"
"ds_read_b64_tr_b16 a[214:215], v239 offset:14336\n"
"v_mfma_f32_16x16x32_bf16 v[176:179], a[128:131], v[64:67], v[176:179]\n"
"s_and_b32 s87, vcc_lo, 3\n"
"buffer_load_dwordx4 v232, s[52:55], s70 offen lds\n"
"v_mfma_f32_16x16x32_bf16 v[176:179], a[132:135], v[68:71], v[176:179]\n"
"s_or_b32 s90, s90, s87\n"
"ds_read_b64_tr_b16 a[224:225], v239 offset:16384\n"
"ds_read_b64_tr_b16 a[226:227], v239 offset:18432\n"
"v_mfma_f32_16x16x32_bf16 v[176:179], a[144:147], v[72:75], v[176:179]\n"
"s_waitcnt lgkmcnt(8)\n"
"s_bitcmp1_b32 s90, 0\n"
"ds_read_b64_tr_b16 a[228:229], v239 offset:20480\n"
"v_cvt_pk_bf16_f32 v172, v172, v173\n"
"v_mfma_f32_16x16x32_bf16 v[176:179], a[148:151], v[76:79], v[176:179]\n"
"s_cselect_b32 s46, s43, 0\n"
"ds_read_b64_tr_b16 a[230:231], v239 offset:22528\n"
"v_cvt_pk_bf16_f32 v173, v174, v175\n"
"v_mfma_f32_16x16x32_bf16 v[176:179], a[160:163], v[80:83], v[176:179]\n"
"s_add_u32 m0, s95, 126976\n"
"ds_read_b64_tr_b16 a[240:241], v239 offset:24576\n"
"buffer_load_dwordx2 a[8:9], v233, s[44:47], s36 offen offset:0 sc1\n"
"v_mfma_f32_16x16x32_bf16 v[176:179], a[164:167], v[84:87], v[176:179]\n"
"buffer_load_dwordx4 v232, s[52:55], s71 offen lds\n"
"v_mfma_f32_16x16x32_bf16 v[176:179], a[176:179], v[88:91], v[176:179]\n"
"s_waitcnt vmcnt(27)\n"
"buffer_store_dwordx2 v[172:173], v233, s[56:59], s85 offen offset:1536 sc1\n"
"ds_read_b64_tr_b16 a[242:243], v239 offset:26624\n"
"ds_read_b64_tr_b16 a[244:245], v239 offset:28672\n"
"v_mfma_f32_16x16x32_bf16 v[176:179], a[180:183], v[92:95], v[176:179]\n"
"s_barrier\n"
"ds_read_b64_tr_b16 a[246:247], v239 offset:30720\n"
"v_mfma_f32_16x16x32_bf16 v[180:183], a[128:131], v[96:99], v[180:183]\n"
"ds_read_b64_tr_b16 a[0:1], v236 offset:0\n"
"v_add_u32 v239, -131072, v239\n"
"v_mfma_f32_16x16x32_bf16 v[180:183], a[132:135], v[100:103], v[180:183]\n"
"ds_read_b64_tr_b16 a[2:3], v236 offset:2048\n"
"buffer_load_dwordx2 a[40:41], v233, s[44:47], s36 offen offset:512 sc1\n"
"v_mfma_f32_16x16x32_bf16 v[180:183], a[144:147], v[104:107], v[180:183]\n"
"ds_read_b64_tr_b16 a[4:5], v236 offset:4096\n"
"v_cvt_pk_bf16_f32 v176, v176, v177\n"
"v_mfma_f32_16x16x32_bf16 v[180:183], a[148:151], v[108:111], v[180:183]\n"
"ds_read_b64_tr_b16 a[6:7], v236 offset:6144\n"
"v_cvt_pk_bf16_f32 v177, v178, v179\n"
"v_mfma_f32_16x16x32_bf16 v[180:183], a[160:163], v[112:115], v[180:183]\n"
"ds_read_b64_tr_b16 a[16:17], v236 offset:8192\n"
"v_mfma_f32_16x16x32_bf16 v[180:183], a[164:167], v[116:119], v[180:183]\n"
"ds_read_b64_tr_b16 a[18:19], v236 offset:10240\n"
"v_mfma_f32_16x16x32_bf16 v[180:183], a[176:179], v[120:123], v[180:183]\n"
"buffer_store_dwordx2 v[176:177], v233, s[56:59], s85 offen offset:2048 sc1\n"
"ds_read_b64_tr_b16 a[20:21], v236 offset:12288\n"
"v_mfma_f32_16x16x32_bf16 v[180:183], a[180:183], v[124:127], v[180:183]\n"
"ds_read_b64_tr_b16 a[22:23], v236 offset:14336\n"
"buffer_load_dwordx2 a[72:73], v233, s[44:47], s36 offen offset:1024 sc1\n"
"v_mfma_f32_16x16x32_bf16 v[184:187], a[192:195], v[64:67], v[184:187]\n"
"s_waitcnt lgkmcnt(8)\n"
"ds_read_b64_tr_b16 a[32:33], v236 offset:16384\n"
"v_mfma_f32_16x16x32_bf16 v[184:187], a[196:199], v[68:71], v[184:187]\n"
"ds_read_b64_tr_b16 a[34:35], v236 offset:18432\n"
"v_mfma_f32_16x16x32_bf16 v[184:187], a[208:211], v[72:75], v[184:187]\n"
"ds_read_b64_tr_b16 a[36:37], v236 offset:20480\n"
"v_cvt_pk_bf16_f32 v180, v180, v181\n"
"v_mfma_f32_16x16x32_bf16 v[184:187], a[212:215], v[76:79], v[184:187]\n"
"ds_read_b64_tr_b16 a[38:39], v236 offset:22528\n"
"v_cvt_pk_bf16_f32 v181, v182, v183\n"
"v_mfma_f32_16x16x32_bf16 v[184:187], a[224:227], v[80:83], v[184:187]\n"
"ds_read_b64_tr_b16 a[48:49], v236 offset:24576\n"
"buffer_load_dwordx2 a[104:105], v233, s[44:47], s36 offen offset:1536 sc1\n"
"v_mfma_f32_16x16x32_bf16 v[184:187], a[228:231], v[84:87], v[184:187]\n"
"ds_read_b64_tr_b16 a[50:51], v236 offset:26624\n"
"v_mfma_f32_16x16x32_bf16 v[184:187], a[240:243], v[88:91], v[184:187]\n"
"buffer_store_dwordx2 v[180:181], v233, s[56:59], s85 offen offset:2560 sc1\n"
"ds_read_b64_tr_b16 a[52:53], v236 offset:28672\n"
"v_mfma_f32_16x16x32_bf16 v[184:187], a[244:247], v[92:95], v[184:187]\n"
"ds_read_b64_tr_b16 a[54:55], v236 offset:30720\n"
"v_mfma_f32_16x16x32_bf16 v[188:191], a[192:195], v[96:99], v[188:191]\n"
"ds_read_b64_tr_b16 a[64:65], v237 offset:0\n"
"v_mfma_f32_16x16x32_bf16 v[188:191], a[196:199], v[100:103], v[188:191]\n"
"ds_read_b64_tr_b16 a[66:67], v237 offset:2048\n"
"buffer_load_dwordx2 a[136:137], v233, s[44:47], s36 offen offset:2048 sc1\n"
"v_mfma_f32_16x16x32_bf16 v[188:191], a[208:211], v[104:107], v[188:191]\n"
"ds_read_b64_tr_b16 a[68:69], v237 offset:4096\n"
"v_cvt_pk_bf16_f32 v184, v184, v185\n"
"v_mfma_f32_16x16x32_bf16 v[188:191], a[212:215], v[108:111], v[188:191]\n"
"ds_read_b64_tr_b16 a[70:71], v237 offset:6144\n"
"v_cvt_pk_bf16_f32 v185, v186, v187\n"
"v_mfma_f32_16x16x32_bf16 v[188:191], a[224:227], v[112:115], v[188:191]\n"
"ds_read_b64_tr_b16 a[80:81], v237 offset:8192\n"
"v_mfma_f32_16x16x32_bf16 v[188:191], a[228:231], v[116:119], v[188:191]\n"
"ds_read_b64_tr_b16 a[82:83], v237 offset:10240\n"
"v_mfma_f32_16x16x32_bf16 v[188:191], a[240:243], v[120:123], v[188:191]\n"
"buffer_store_dwordx2 v[184:185], v233, s[56:59], s85 offen offset:3072 sc1\n"
"ds_read_b64_tr_b16 a[84:85], v237 offset:12288\n"
"v_mfma_f32_16x16x32_bf16 v[188:191], a[244:247], v[124:127], v[188:191]\n"
"s_waitcnt lgkmcnt(8)\n"
"ds_read_b64_tr_b16 a[86:87], v237 offset:14336\n"
"buffer_load_dwordx2 a[168:169], v233, s[44:47], s36 offen offset:2560 sc1\n"
"v_mfma_f32_16x16x32_bf16 v[192:195], a[0:3], v[0:3], 0\n"
"ds_read_b64_tr_b16 a[96:97], v237 offset:16384\n"
"v_mfma_f32_16x16x32_bf16 v[192:195], a[4:7], v[4:7], v[192:195]\n"
"ds_read_b64_tr_b16 a[98:99], v237 offset:18432\n"
"v_mfma_f32_16x16x32_bf16 v[192:195], a[16:19], v[8:11], v[192:195]\n"
"ds_read_b64_tr_b16 a[100:101], v237 offset:20480\n"
"v_cvt_pk_bf16_f32 v188, v188, v189\n"
"v_mfma_f32_16x16x32_bf16 v[192:195], a[20:23], v[12:15], v[192:195]\n"
"ds_read_b64_tr_b16 a[102:103], v237 offset:22528\n"
"v_cvt_pk_bf16_f32 v189, v190, v191\n"
"v_mfma_f32_16x16x32_bf16 v[192:195], a[32:35], v[16:19], v[192:195]\n"
"ds_read_b64_tr_b16 a[112:113], v237 offset:24576\n"
"buffer_load_dwordx2 a[200:201], v233, s[44:47], s36 offen offset:3072 sc1\n"
"v_mfma_f32_16x16x32_bf16 v[192:195], a[36:39], v[20:23], v[192:195]\n"
"ds_read_b64_tr_b16 a[114:115], v237 offset:26624\n"
"v_mfma_f32_16x16x32_bf16 v[192:195], a[48:51], v[24:27], v[192:195]\n"
"s_waitcnt lgkmcnt(8)\n"
"buffer_store_dwordx2 v[188:189], v233, s[56:59], s85 offen offset:3584 sc1\n"
"ds_read_b64_tr_b16 a[116:117], v237 offset:28672\n"
"v_mfma_f32_16x16x32_bf16 v[192:195], a[52:55], v[28:31], v[192:195]\n"
"s_add_u32 s85, s85, 131072\n"
"ds_read_b64_tr_b16 a[118:119], v237 offset:30720\n"
"v_mfma_f32_16x16x32_bf16 v[196:199], a[0:3], v[32:35], 0\n"
"s_cmp_eq_u32 s85, 917504\n"
"ds_read_b64_tr_b16 a[128:129], v238 offset:0\n"
"v_mfma_f32_16x16x32_bf16 v[196:199], a[4:7], v[36:39], v[196:199]\n"
"s_cselect_b32 s85, 0, s85\n"
"ds_read_b64_tr_b16 a[130:131], v238 offset:2048\n"
"buffer_load_dwordx2 a[232:233], v233, s[44:47], s36 offen offset:3584 sc1\n"
"v_mfma_f32_16x16x32_bf16 v[196:199], a[16:19], v[40:43], v[196:199]\n"
"s_bitcmp1_b32 s90, 1\n"
"ds_read_b64_tr_b16 a[132:133], v238 offset:4096\n"
"v_mul_f32 v192, v240, v192\n"
"v_mfma_f32_16x16x32_bf16 v[196:199], a[20:23], v[44:47], v[196:199]\n"
"s_cselect_b32 s46, s43, 0\n"
"ds_read_b64_tr_b16 a[134:135], v238 offset:6144\n"
"v_mul_f32 v193, v240, v193\n"
"v_mfma_f32_16x16x32_bf16 v[196:199], a[32:35], v[48:51], v[196:199]\n"
"ds_read_b64_tr_b16 a[144:145], v238 offset:8192\n"
"buffer_load_dwordx2 a[10:11], v233, s[44:47], s37 offen offset:0 sc1\n"
"v_mul_f32 v194, v240, v194\n"
"v_mfma_f32_16x16x32_bf16 v[196:199], a[36:39], v[52:55], v[196:199]\n"
"ds_read_b64_tr_b16 a[146:147], v238 offset:10240\n"
"v_mul_f32 v195, v240, v195\n"
"v_mfma_f32_16x16x32_bf16 v[196:199], a[48:51], v[56:59], v[196:199]\n"
"ds_read_b64_tr_b16 a[148:149], v238 offset:12288\n"
"v_mfma_f32_16x16x32_bf16 v[196:199], a[52:55], v[60:63], v[196:199]\n"
"ds_read_b64_tr_b16 a[150:151], v238 offset:14336\n"
"v_mfma_f32_16x16x32_bf16 v[200:203], a[64:67], v[0:3], 0\n"
"ds_read_b64_tr_b16 a[160:161], v238 offset:16384\n"
"v_mfma_f32_16x16x32_bf16 v[200:203], a[68:71], v[4:7], v[200:203]\n"
"ds_read_b64_tr_b16 a[162:163], v238 offset:18432\n"
"buffer_load_dwordx2 a[42:43], v233, s[44:47], s37 offen offset:512 sc1\n"
"v_mfma_f32_16x16x32_bf16 v[200:203], a[80:83], v[8:11], v[200:203]\n"
"s_waitcnt lgkmcnt(8)\n"
"ds_read_b64_tr_b16 a[164:165], v238 offset:20480\n"
"v_mul_f32 v196, v241, v196\n"
"v_mfma_f32_16x16x32_bf16 v[200:203], a[84:87], v[12:15], v[200:203]\n"
"ds_read_b64_tr_b16 a[166:167], v238 offset:22528\n"
"v_mul_f32 v197, v241, v197\n"
"v_mfma_f32_16x16x32_bf16 v[200:203], a[96:99], v[16:19], v[200:203]\n"
"ds_read_b64_tr_b16 a[176:177], v238 offset:24576\n"
"v_mul_f32 v198, v241, v198\n"
"v_mfma_f32_16x16x32_bf16 v[200:203], a[100:103], v[20:23], v[200:203]\n"
"s_add_u32 m0, s95, 65536\n"
"ds_read_b64_tr_b16 a[178:179], v238 offset:26624\n"
"v_mul_f32 v199, v241, v199\n"
"v_mfma_f32_16x16x32_bf16 v[200:203], a[112:115], v[24:27], v[200:203]\n"
"buffer_load_dwordx4 v232, s[52:55], s72 offen lds\n"
"v_mfma_f32_16x16x32_bf16 v[200:203], a[116:119], v[28:31], v[200:203]\n"
"ds_read_b64_tr_b16 a[180:181], v238 offset:28672\n"
"ds_read_b64_tr_b16 a[182:183], v238 offset:30720\n"
"buffer_load_dwordx2 a[74:75], v233, s[44:47], s37 offen offset:1024 sc1\n"
"v_mfma_f32_16x16x32_bf16 v[204:207], a[64:67], v[32:35], 0\n"
"ds_read_b64_tr_b16 a[192:193], v239 offset:0\n"
"v_mfma_f32_16x16x32_bf16 v[204:207], a[68:71], v[36:39], v[204:207]\n"
"ds_read_b64_tr_b16 a[194:195], v239 offset:2048\n"
"v_mfma_f32_16x16x32_bf16 v[204:207], a[80:83], v[40:43], v[204:207]\n"
"s_add_u32 m0, s95, 69632\n"
"ds_read_b64_tr_b16 a[196:197], v239 offset:4096\n"
"v_mul_f32 v200, v240, v200\n"
"v_mfma_f32_16x16x32_bf16 v[204:207], a[84:87], v[44:47], v[204:207]\n"
"buffer_load_dwordx4 v232, s[52:55], s73 offen lds\n"
"v_mul_f32 v201, v240, v201\n"
"v_mfma_f32_16x16x32_bf16 v[204:207], a[96:99], v[48:51], v[204:207]\n"
"ds_read_b64_tr_b16 a[198:199], v239 offset:6144\n"
"ds_read_b64_tr_b16 a[208:209], v239 offset:8192\n"
"buffer_load_dwordx2 a[106:107], v233, s[44:47], s37 offen offset:1536 sc1\n"
"v_mfma_f32_16x16x32_bf16 v[204:207], a[100:103], v[52:55], v[204:207]\n"
"ds_read_b64_tr_b16 a[210:211], v239 offset:10240\n"
"v_mul_f32 v202, v240, v202\n"
"v_mfma_f32_16x16x32_bf16 v[204:207], a[112:115], v[56:59], v[204:207]\n"
"ds_read_b64_tr_b16 a[212:213], v239 offset:12288\n"
"v_mul_f32 v203, v240, v203\n"
"v_mfma_f32_16x16x32_bf16 v[204:207], a[116:119], v[60:63], v[204:207]\n"
"s_add_u32 m0, s95, 73728\n"
"ds_read_b64_tr_b16 a[214:215], v239 offset:14336\n"
"v_mfma_f32_16x16x32_bf16 v[208:211], a[128:131], v[0:3], 0\n"
"s_waitcnt lgkmcnt(8)\n"
"buffer_load_dwordx4 v232, s[52:55], s74 offen lds\n"
"v_mfma_f32_16x16x32_bf16 v[208:211], a[132:135], v[4:7], v[208:211]\n"
"ds_read_b64_tr_b16 a[224:225], v239 offset:16384\n"
"ds_read_b64_tr_b16 a[226:227], v239 offset:18432\n"
"buffer_load_dwordx2 a[138:139], v233, s[44:47], s37 offen offset:2048 sc1\n"
"v_mfma_f32_16x16x32_bf16 v[208:211], a[144:147], v[8:11], v[208:211]\n"
"ds_read_b64_tr_b16 a[228:229], v239 offset:20480\n"
"v_mul_f32 v204, v241, v204\n"
"v_mfma_f32_16x16x32_bf16 v[208:211], a[148:151], v[12:15], v[208:211]\n"
"ds_read_b64_tr_b16 a[230:231], v239 offset:22528\n"
"v_mul_f32 v205, v241, v205\n"
"v_mfma_f32_16x16x32_bf16 v[208:211], a[160:163], v[16:19], v[208:211]\n"
"s_add_u32 m0, s95, 77824\n"
"ds_read_b64_tr_b16 a[240:241], v239 offset:24576\n"
"v_mul_f32 v206, v241, v206\n"
"v_mfma_f32_16x16x32_bf16 v[208:211], a[164:167], v[20:23], v[208:211]\n"
"buffer_load_dwordx4 v232, s[52:55], s75 offen lds\n"
"v_mul_f32 v207, v241, v207\n"
"v_mfma_f32_16x16x32_bf16 v[208:211], a[176:179], v[24:27], v[208:211]\n"
"s_waitcnt vmcnt(34)\n"
"ds_read_b64_tr_b16 a[242:243], v239 offset:26624\n"
"ds_read_b64_tr_b16 a[244:245], v239 offset:28672\n"
"buffer_load_dwordx2 a[170:171], v233, s[44:47], s37 offen offset:2560 sc1\n"
"v_mfma_f32_16x16x32_bf16 v[208:211], a[180:183], v[28:31], v[208:211]\n"
"s_barrier\n"
"ds_read_b64_tr_b16 a[246:247], v239 offset:30720\n"
"v_mfma_f32_16x16x32_bf16 v[212:215], a[128:131], v[32:35], 0\n"
"ds_read_b64_tr_b16 a[0:1], v236 offset:32768\n"
"buffer_load_dwordx2 v[246:247], v235, s[56:59], 0 offen sc1\n"
"v_mfma_f32_16x16x32_bf16 v[212:215], a[132:135], v[36:39], v[212:215]\n"
"s_add_u32 m0, s95, 81920\n"
"ds_read_b64_tr_b16 a[2:3], v236 offset:34816\n"
"v_mfma_f32_16x16x32_bf16 v[212:215], a[144:147], v[40:43], v[212:215]\n"
"buffer_load_dwordx4 v232, s[52:55], s76 offen lds\n"
"v_mul_f32 v208, v240, v208\n"
"v_mfma_f32_16x16x32_bf16 v[212:215], a[148:151], v[44:47], v[212:215]\n"
"ds_read_b64_tr_b16 a[4:5], v236 offset:36864\n"
"ds_read_b64_tr_b16 a[6:7], v236 offset:38912\n"
"buffer_load_dwordx2 a[202:203], v233, s[44:47], s37 offen offset:3072 sc1\n"
"v_mfma_f32_16x16x32_bf16 v[212:215], a[160:163], v[48:51], v[212:215]\n"
"ds_read_b64_tr_b16 a[16:17], v236 offset:40960\n"
"v_mul_f32 v209, v240, v209\n"
"v_mfma_f32_16x16x32_bf16 v[212:215], a[164:167], v[52:55], v[212:215]\n"
"ds_read_b64_tr_b16 a[18:19], v236 offset:43008\n"
"v_mul_f32 v210, v240, v210\n"
"v_mfma_f32_16x16x32_bf16 v[212:215], a[176:179], v[56:59], v[212:215]\n"
"s_add_u32 m0, s95, 86016\n"
"ds_read_b64_tr_b16 a[20:21], v236 offset:45056\n"
"v_mul_f32 v211, v240, v211\n"
"v_mfma_f32_16x16x32_bf16 v[212:215], a[180:183], v[60:63], v[212:215]\n"
"s_waitcnt lgkmcnt(8)\n"
"buffer_load_dwordx4 v232, s[52:55], s77 offen lds\n"
"v_mfma_f32_16x16x32_bf16 v[216:219], a[192:195], v[0:3], 0\n"
"ds_read_b64_tr_b16 a[22:23], v236 offset:47104\n"
"ds_read_b64_tr_b16 a[32:33], v236 offset:49152\n"
"buffer_load_dwordx2 a[234:235], v233, s[44:47], s37 offen offset:3584 sc1\n"
"v_mfma_f32_16x16x32_bf16 v[216:219], a[196:199], v[4:7], v[216:219]\n"
"ds_read_b64_tr_b16 a[34:35], v236 offset:51200\n"
"v_mfma_f32_16x16x32_bf16 v[216:219], a[208:211], v[8:11], v[216:219]\n"
"ds_read_b64_tr_b16 a[36:37], v236 offset:53248\n"
"v_mul_f32 v212, v241, v212\n"
"v_mfma_f32_16x16x32_bf16 v[216:219], a[212:215], v[12:15], v[216:219]\n"
"s_add_u32 m0, s95, 90112\n"
"ds_read_b64_tr_b16 a[38:39], v236 offset:55296\n"
"v_mul_f32 v213, v241, v213\n"
"v_mfma_f32_16x16x32_bf16 v[216:219], a[224:227], v[16:19], v[216:219]\n"
"buffer_load_dwordx4 v232, s[52:55], s78 offen lds\n"
"v_mul_f32 v214, v241, v214\n"
"v_mfma_f32_16x16x32_bf16 v[216:219], a[228:231], v[20:23], v[216:219]\n"
"ds_read_b64_tr_b16 a[48:49], v236 offset:57344\n"
"ds_read_b64_tr_b16 a[50:51], v236 offset:59392\n"
"v_mfma_f32_16x16x32_bf16 v[216:219], a[240:243], v[24:27], v[216:219]\n"
"s_waitcnt lgkmcnt(8)\n"
"ds_read_b64_tr_b16 a[52:53], v236 offset:61440\n"
"v_mul_f32 v215, v241, v215\n"
"v_mfma_f32_16x16x32_bf16 v[216:219], a[244:247], v[28:31], v[216:219]\n"
"ds_read_b64_tr_b16 a[54:55], v236 offset:63488\n"
"v_mfma_f32_16x16x32_bf16 v[220:223], a[192:195], v[32:35], 0\n"
"s_add_u32 m0, s95, 94208\n"
"ds_read_b64_tr_b16 a[64:65], v237 offset:32768\n"
"v_add_u32 v236, 65536, v236\n"
"v_mfma_f32_16x16x32_bf16 v[220:223], a[196:199], v[36:39], v[220:223]\n"
"buffer_load_dwordx4 v232, s[52:55], s79 offen lds\n"
"v_mfma_f32_16x16x32_bf16 v[220:223], a[208:211], v[40:43], v[220:223]\n"
"ds_read_b64_tr_b16 a[66:67], v237 offset:34816\n"
"ds_read_b64_tr_b16 a[68:69], v237 offset:36864\n"
"v_mfma_f32_16x16x32_bf16 v[220:223], a[212:215], v[44:47], v[220:223]\n"
"ds_read_b64_tr_b16 a[70:71], v237 offset:38912\n"
"v_mul_f32 v216, v240, v216\n"
"v_mfma_f32_16x16x32_bf16 v[220:223], a[224:227], v[48:51], v[220:223]\n"
"ds_read_b64_tr_b16 a[80:81], v237 offset:40960\n"
"v_mul_f32 v217, v240, v217\n"
"v_mfma_f32_16x16x32_bf16 v[220:223], a[228:231], v[52:55], v[220:223]\n"
"ds_read_b64_tr_b16 a[82:83], v237 offset:43008\n"
"v_mul_f32 v218, v240, v218\n"
"v_mfma_f32_16x16x32_bf16 v[220:223], a[240:243], v[56:59], v[220:223]\n"
"ds_read_b64_tr_b16 a[84:85], v237 offset:45056\n"
"v_mul_f32 v219, v240, v219\n"
"v_mfma_f32_16x16x32_bf16 v[220:223], a[244:247], v[60:63], v[220:223]\n"
"ds_read_b64_tr_b16 a[86:87], v237 offset:47104\n"
"v_mfma_f32_16x16x32_bf16 v[192:195], a[0:3], v[64:67], v[192:195]\n"
"ds_read_b64_tr_b16 a[96:97], v237 offset:49152\n"
"v_mfma_f32_16x16x32_bf16 v[192:195], a[4:7], v[68:71], v[192:195]\n"
"ds_read_b64_tr_b16 a[98:99], v237 offset:51200\n"
"v_mfma_f32_16x16x32_bf16 v[192:195], a[16:19], v[72:75], v[192:195]\n"
"s_waitcnt lgkmcnt(9)\n"
"ds_read_b64_tr_b16 a[100:101], v237 offset:53248\n"
"v_mul_f32 v220, v241, v220\n"
"v_mfma_f32_16x16x32_bf16 v[192:195], a[20:23], v[76:79], v[192:195]\n"
"ds_read_b64_tr_b16 a[102:103], v237 offset:55296\n"
"v_mul_f32 v221, v241, v221\n"
"v_mfma_f32_16x16x32_bf16 v[192:195], a[32:35], v[80:83], v[192:195]\n"
"ds_read_b64_tr_b16 a[112:113], v237 offset:57344\n"
"v_mul_f32 v222, v241, v222\n"
"v_mfma_f32_16x16x32_bf16 v[192:195], a[36:39], v[84:87], v[192:195]\n"
"ds_read_b64_tr_b16 a[114:115], v237 offset:59392\n"
"v_mul_f32 v223, v241, v223\n"
"v_mfma_f32_16x16x32_bf16 v[192:195], a[48:51], v[88:91], v[192:195]\n"
"s_waitcnt vmcnt(18)\n"
"ds_read_b64_tr_b16 a[116:117], v237 offset:61440\n"
"buffer_store_dwordx2 v[244:245], v234, s[56:59], 0 offen offset:512 sc1\n"
"v_mfma_f32_16x16x32_bf16 v[192:195], a[52:55], v[92:95], v[192:195]\n"
"ds_read_b64_tr_b16 a[118:119], v237 offset:63488\n"
"v_mfma_f32_16x16x32_bf16 v[196:199], a[0:3], v[96:99], v[196:199]\n"
"ds_read_b64_tr_b16 a[128:129], v238 offset:32768\n"
"v_add_u32 v237, 65536, v237\n"
"v_mfma_f32_16x16x32_bf16 v[196:199], a[4:7], v[100:103], v[196:199]\n"
"ds_read_b64_tr_b16 a[130:131], v238 offset:34816\n"
"v_mfma_f32_16x16x32_bf16 v[196:199], a[16:19], v[104:107], v[196:199]\n"
"ds_read_b64_tr_b16 a[132:133], v238 offset:36864\n"
"v_cvt_pk_bf16_f32 v192, v192, v193\n"
"v_mfma_f32_16x16x32_bf16 v[196:199], a[20:23], v[108:111], v[196:199]\n"
"ds_read_b64_tr_b16 a[134:135], v238 offset:38912\n"
"v_cvt_pk_bf16_f32 v193, v194, v195\n"
"v_mfma_f32_16x16x32_bf16 v[196:199], a[32:35], v[112:115], v[196:199]\n"
"buffer_store_dwordx2 v[192:193], v233, s[56:59], s85 offen offset:0 sc1\n"
"ds_read_b64_tr_b16 a[144:145], v238 offset:40960\n"
"v_mfma_f32_16x16x32_bf16 v[196:199], a[36:39], v[116:119], v[196:199]\n"
"ds_read_b64_tr_b16 a[146:147], v238 offset:43008\n"
"v_mfma_f32_16x16x32_bf16 v[196:199], a[48:51], v[120:123], v[196:199]\n"
"ds_read_b64_tr_b16 a[148:149], v238 offset:45056\n"
"v_mfma_f32_16x16x32_bf16 v[196:199], a[52:55], v[124:127], v[196:199]\n"
"s_waitcnt lgkmcnt(8)\n"
"ds_read_b64_tr_b16 a[150:151], v238 offset:47104\n"
"v_mfma_f32_16x16x32_bf16 v[200:203], a[64:67], v[64:67], v[200:203]\n"
"ds_read_b64_tr_b16 a[160:161], v238 offset:49152\n"
"v_mfma_f32_16x16x32_bf16 v[200:203], a[68:71], v[68:71], v[200:203]\n"
"ds_read_b64_tr_b16 a[162:163], v238 offset:51200\n"
"v_mfma_f32_16x16x32_bf16 v[200:203], a[80:83], v[72:75], v[200:203]\n"
"ds_read_b64_tr_b16 a[164:165], v238 offset:53248\n"
"v_cvt_pk_bf16_f32 v196, v196, v197\n"
"v_mfma_f32_16x16x32_bf16 v[200:203], a[84:87], v[76:79], v[200:203]\n"
"ds_read_b64_tr_b16 a[166:167], v238 offset:55296\n"
"v_cvt_pk_bf16_f32 v197, v198, v199\n"
"v_mfma_f32_16x16x32_bf16 v[200:203], a[96:99], v[80:83], v[200:203]\n"
"buffer_store_dwordx2 v[196:197], v233, s[56:59], s85 offen offset:512 sc1\n"
"ds_read_b64_tr_b16 a[176:177], v238 offset:57344\n"
"v_mfma_f32_16x16x32_bf16 v[200:203], a[100:103], v[84:87], v[200:203]\n"
"s_waitcnt vmcnt(9)\n"
"ds_read_b64_tr_b16 a[178:179], v238 offset:59392\n"
"v_cmp_eq_u64 vcc, v[246:247], v[244:245]\n"
"v_mfma_f32_16x16x32_bf16 v[200:203], a[112:115], v[88:91], v[200:203]\n"
"s_waitcnt lgkmcnt(8)\n"
"s_and_b32 s87, vcc_lo, 76\n"
"ds_read_b64_tr_b16 a[180:181], v238 offset:61440\n"
"v_mfma_f32_16x16x32_bf16 v[200:203], a[116:119], v[92:95], v[200:203]\n"
"s_or_b32 s90, s90, s87\n"
"ds_read_b64_tr_b16 a[182:183], v238 offset:63488\n"
"v_mfma_f32_16x16x32_bf16 v[204:207], a[64:67], v[96:99], v[204:207]\n"
"s_bitcmp1_b32 s90, 2\n"
"ds_read_b64_tr_b16 a[192:193], v239 offset:32768\n"
"v_add_u32 v238, 65536, v238\n"
"v_mfma_f32_16x16x32_bf16 v[204:207], a[68:71], v[100:103], v[204:207]\n"
"s_cselect_b32 s46, s43, 0\n"
"ds_read_b64_tr_b16 a[194:195], v239 offset:34816\n"
"v_mfma_f32_16x16x32_bf16 v[204:207], a[80:83], v[104:107], v[204:207]\n"
"ds_read_b64_tr_b16 a[196:197], v239 offset:36864\n"
"buffer_load_dwordx2 a[12:13], v233, s[44:47], s38 offen offset:0 sc1\n"
"v_cvt_pk_bf16_f32 v200, v200, v201\n"
"v_mfma_f32_16x16x32_bf16 v[204:207], a[84:87], v[108:111], v[204:207]\n"
"ds_read_b64_tr_b16 a[198:199], v239 offset:38912\n"
"v_cvt_pk_bf16_f32 v201, v202, v203\n"
"v_mfma_f32_16x16x32_bf16 v[204:207], a[96:99], v[112:115], v[204:207]\n"
"buffer_store_dwordx2 v[200:201], v233, s[56:59], s85 offen offset:1024 sc1\n"
"ds_read_b64_tr_b16 a[208:209], v239 offset:40960\n"
"v_mfma_f32_16x16x32_bf16 v[204:207], a[100:103], v[116:119], v[204:207]\n"
"ds_read_b64_tr_b16 a[210:211], v239 offset:43008\n"
"v_mfma_f32_16x16x32_bf16 v[204:207], a[112:115], v[120:123], v[204:207]\n"
"ds_read_b64_tr_b16 a[212:213], v239 offset:45056\n"
"v_mfma_f32_16x16x32_bf16 v[204:207], a[116:119], v[124:127], v[204:207]\n"
"ds_read_b64_tr_b16 a[214:215], v239 offset:47104\n"
"buffer_load_dwordx2 a[44:45], v233, s[44:47], s38 offen offset:512 sc1\n"
"v_mfma_f32_16x16x32_bf16 v[208:211], a[128:131], v[64:67], v[208:211]\n"
"ds_read_b64_tr_b16 a[224:225], v239 offset:49152\n"
"v_mfma_f32_16x16x32_bf16 v[208:211], a[132:135], v[68:71], v[208:211]\n"
"ds_read_b64_tr_b16 a[226:227], v239 offset:51200\n"
"v_mfma_f32_16x16x32_bf16 v[208:211], a[144:147], v[72:75], v[208:211]\n"
"s_waitcnt lgkmcnt(8)\n"
"ds_read_b64_tr_b16 a[228:229], v239 offset:53248\n"
"v_cvt_pk_bf16_f32 v204, v204, v205\n"
"v_mfma_f32_16x16x32_bf16 v[208:211], a[148:151], v[76:79], v[208:211]\n"
"ds_read_b64_tr_b16 a[230:231], v239 offset:55296\n"
"v_cvt_pk_bf16_f32 v205, v206, v207\n"
"v_mfma_f32_16x16x32_bf16 v[208:211], a[160:163], v[80:83], v[208:211]\n"
"buffer_store_dwordx2 v[204:205], v233, s[56:59], s85 offen offset:1536 sc1\n"
"ds_read_b64_tr_b16 a[240:241], v239 offset:57344\n"
"v_mfma_f32_16x16x32_bf16 v[208:211], a[164:167], v[84:87], v[208:211]\n"
"ds_read_b64_tr_b16 a[242:243], v239 offset:59392\n"
"buffer_load_dwordx2 a[76:77], v233, s[44:47], s38 offen offset:1024 sc1\n"
"v_mfma_f32_16x16x32_bf16 v[208:211], a[176:179], v[88:91], v[208:211]\n"
"s_waitcnt vmcnt(37)\n"
"ds_read_b64_tr_b16 a[244:245], v239 offset:61440\n"
"v_mfma_f32_16x16x32_bf16 v[208:211], a[180:183], v[92:95], v[208:211]\n"
"s_barrier\n"
"ds_read_b64_tr_b16 a[246:247], v239 offset:63488\n"
"v_mfma_f32_16x16x32_bf16 v[212:215], a[128:131], v[96:99], v[212:215]\n"
"ds_read_b64_tr_b16 a[0:1], v236 offset:32768\n"
"v_add_u32 v239, 65536, v239\n"
"v_mfma_f32_16x16x32_bf16 v[212:215], a[132:135], v[100:103], v[212:215]\n"
"ds_read_b64_tr_b16 a[2:3], v236 offset:34816\n"
"v_mfma_f32_16x16x32_bf16 v[212:215], a[144:147], v[104:107], v[212:215]\n"
"ds_read_b64_tr_b16 a[4:5], v236 offset:36864\n"
"buffer_load_dwordx2 a[108:109], v233, s[44:47], s38 offen offset:1536 sc1\n"
"v_cvt_pk_bf16_f32 v208, v208, v209\n"
"v_mfma_f32_16x16x32_bf16 v[212:215], a[148:151], v[108:111], v[212:215]\n"
"ds_read_b64_tr_b16 a[6:7], v236 offset:38912\n"
"v_cvt_pk_bf16_f32 v209, v210, v211\n"
"v_mfma_f32_16x16x32_bf16 v[212:215], a[160:163], v[112:115], v[212:215]\n"
"buffer_store_dwordx2 v[208:209], v233, s[56:59], s85 offen offset:2048 sc1\n"
"ds_read_b64_tr_b16 a[16:17], v236 offset:40960\n"
"v_mfma_f32_16x16x32_bf16 v[212:215], a[164:167], v[116:119], v[212:215]\n"
"ds_read_b64_tr_b16 a[18:19], v236 offset:43008\n"
"v_mfma_f32_16x16x32_bf16 v[212:215], a[176:179], v[120:123], v[212:215]\n"
"ds_read_b64_tr_b16 a[20:21], v236 offset:45056\n"
"v_mfma_f32_16x16x32_bf16 v[212:215], a[180:183], v[124:127], v[212:215]\n"
"ds_read_b64_tr_b16 a[22:23], v236 offset:47104\n"
"buffer_load_dwordx2 a[140:141], v233, s[44:47], s38 offen offset:2048 sc1\n"
"v_mfma_f32_16x16x32_bf16 v[216:219], a[192:195], v[64:67], v[216:219]\n"
"s_waitcnt lgkmcnt(8)\n"
"ds_read_b64_tr_b16 a[32:33], v236 offset:49152\n"
"v_mfma_f32_16x16x32_bf16 v[216:219], a[196:199], v[68:71], v[216:219]\n"
"ds_read_b64_tr_b16 a[34:35], v236 offset:51200\n"
"v_mfma_f32_16x16x32_bf16 v[216:219], a[208:211], v[72:75], v[216:219]\n"
"ds_read_b64_tr_b16 a[36:37], v236 offset:53248\n"
"v_cvt_pk_bf16_f32 v212, v212, v213\n"
"v_mfma_f32_16x16x32_bf16 v[216:219], a[212:215], v[76:79], v[216:219]\n"
"ds_read_b64_tr_b16 a[38:39], v236 offset:55296\n"
"v_cvt_pk_bf16_f32 v213, v214, v215\n"
"v_mfma_f32_16x16x32_bf16 v[216:219], a[224:227], v[80:83], v[216:219]\n"
"buffer_store_dwordx2 v[212:213], v233, s[56:59], s85 offen offset:2560 sc1\n"
"ds_read_b64_tr_b16 a[48:49], v236 offset:57344\n"
"v_mfma_f32_16x16x32_bf16 v[216:219], a[228:231], v[84:87], v[216:219]\n"
"ds_read_b64_tr_b16 a[50:51], v236 offset:59392\n"
"buffer_load_dwordx2 a[172:173], v233, s[44:47], s38 offen offset:2560 sc1\n"
"v_mfma_f32_16x16x32_bf16 v[216:219], a[240:243], v[88:91], v[216:219]\n"
"ds_read_b64_tr_b16 a[52:53], v236 offset:61440\n"
"v_mfma_f32_16x16x32_bf16 v[216:219], a[244:247], v[92:95], v[216:219]\n"
"ds_read_b64_tr_b16 a[54:55], v236 offset:63488\n"
"v_mfma_f32_16x16x32_bf16 v[220:223], a[192:195], v[96:99], v[220:223]\n"
"ds_read_b64_tr_b16 a[64:65], v237 offset:32768\n"
"v_mfma_f32_16x16x32_bf16 v[220:223], a[196:199], v[100:103], v[220:223]\n"
"ds_read_b64_tr_b16 a[66:67], v237 offset:34816\n"
"v_mfma_f32_16x16x32_bf16 v[220:223], a[208:211], v[104:107], v[220:223]\n"
"ds_read_b64_tr_b16 a[68:69], v237 offset:36864\n"
"buffer_load_dwordx2 a[204:205], v233, s[44:47], s38 offen offset:3072 sc1\n"
"v_cvt_pk_bf16_f32 v216, v216, v217\n"
"v_mfma_f32_16x16x32_bf16 v[220:223], a[212:215], v[108:111], v[220:223]\n"
"ds_read_b64_tr_b16 a[70:71], v237 offset:38912\n"
"v_cvt_pk_bf16_f32 v217, v218, v219\n"
"v_mfma_f32_16x16x32_bf16 v[220:223], a[224:227], v[112:115], v[220:223]\n"
"buffer_store_dwordx2 v[216:217], v233, s[56:59], s85 offen offset:3072 sc1\n"
"ds_read_b64_tr_b16 a[80:81], v237 offset:40960\n"
"v_mfma_f32_16x16x32_bf16 v[220:223], a[228:231], v[116:119], v[220:223]\n"
"ds_read_b64_tr_b16 a[82:83], v237 offset:43008\n"
"v_mfma_f32_16x16x32_bf16 v[220:223], a[240:243], v[120:123], v[220:223]\n"
"ds_read_b64_tr_b16 a[84:85], v237 offset:45056\n"
"v_mfma_f32_16x16x32_bf16 v[220:223], a[244:247], v[124:127], v[220:223]\n"
"s_waitcnt lgkmcnt(8)\n"
"ds_read_b64_tr_b16 a[86:87], v237 offset:47104\n"
"buffer_load_dwordx2 a[236:237], v233, s[44:47], s38 offen offset:3584 sc1\n"
"v_mfma_f32_16x16x32_bf16 v[128:131], a[0:3], v[0:3], 0\n"
"s_bitcmp1_b32 s90, 3\n"
"ds_read_b64_tr_b16 a[96:97], v237 offset:49152\n"
"v_mfma_f32_16x16x32_bf16 v[128:131], a[4:7], v[4:7], v[128:131]\n"
"s_cselect_b32 s46, s43, 0\n"
"ds_read_b64_tr_b16 a[98:99], v237 offset:51200\n"
"v_mfma_f32_16x16x32_bf16 v[128:131], a[16:19], v[8:11], v[128:131]\n"
"ds_read_b64_tr_b16 a[100:101], v237 offset:53248\n"
"buffer_load_dwordx2 a[14:15], v233, s[44:47], s39 offen offset:0 sc1\n"
"v_cvt_pk_bf16_f32 v220, v220, v221\n"
"v_mfma_f32_16x16x32_bf16 v[128:131], a[20:23], v[12:15], v[128:131]\n"
"ds_read_b64_tr_b16 a[102:103], v237 offset:55296\n"
"v_cvt_pk_bf16_f32 v221, v222, v223\n"
"v_mfma_f32_16x16x32_bf16 v[128:131], a[32:35], v[16:19], v[128:131]\n"
"buffer_store_dwordx2 v[220:221], v233, s[56:59], s85 offen offset:3584 sc1\n"
"ds_read_b64_tr_b16 a[112:113], v237 offset:57344\n"
"v_mfma_f32_16x16x32_bf16 v[128:131], a[36:39], v[20:23], v[128:131]\n"
"s_add_u32 s85, s85, 131072\n"
"ds_read_b64_tr_b16 a[114:115], v237 offset:59392\n"
"v_mfma_f32_16x16x32_bf16 v[128:131], a[48:51], v[24:27], v[128:131]\n"
"s_waitcnt lgkmcnt(8)\n"
"s_cmp_eq_u32 s85, 917504\n"
"ds_read_b64_tr_b16 a[116:117], v237 offset:61440\n"
"v_mfma_f32_16x16x32_bf16 v[128:131], a[52:55], v[28:31], v[128:131]\n"
"s_cselect_b32 s85, 0, s85\n"
"ds_read_b64_tr_b16 a[118:119], v237 offset:63488\n"
"buffer_load_dwordx2 a[46:47], v233, s[44:47], s39 offen offset:512 sc1\n"
"v_mfma_f32_16x16x32_bf16 v[132:135], a[0:3], v[32:35], 0\n"
"ds_read_b64_tr_b16 a[128:129], v238 offset:32768\n"
"v_mfma_f32_16x16x32_bf16 v[132:135], a[4:7], v[36:39], v[132:135]\n"
"ds_read_b64_tr_b16 a[130:131], v238 offset:34816\n"
"v_mfma_f32_16x16x32_bf16 v[132:135], a[16:19], v[40:43], v[132:135]\n"
"ds_read_b64_tr_b16 a[132:133], v238 offset:36864\n"
"v_mul_f32 v128, v240, v128\n"
"v_mfma_f32_16x16x32_bf16 v[132:135], a[20:23], v[44:47], v[132:135]\n"
"ds_read_b64_tr_b16 a[134:135], v238 offset:38912\n"
"v_mul_f32 v129, v240, v129\n"
"v_mfma_f32_16x16x32_bf16 v[132:135], a[32:35], v[48:51], v[132:135]\n"
"ds_read_b64_tr_b16 a[144:145], v238 offset:40960\n"
"buffer_load_dwordx2 a[78:79], v233, s[44:47], s39 offen offset:1024 sc1\n"
"v_mul_f32 v130, v240, v130\n"
"v_mfma_f32_16x16x32_bf16 v[132:135], a[36:39], v[52:55], v[132:135]\n"
"ds_read_b64_tr_b16 a[146:147], v238 offset:43008\n"
"v_mul_f32 v131, v240, v131\n"
"v_mfma_f32_16x16x32_bf16 v[132:135], a[48:51], v[56:59], v[132:135]\n"
"ds_read_b64_tr_b16 a[148:149], v238 offset:45056\n"
"v_mfma_f32_16x16x32_bf16 v[132:135], a[52:55], v[60:63], v[132:135]\n"
"ds_read_b64_tr_b16 a[150:151], v238 offset:47104\n"
"v_mfma_f32_16x16x32_bf16 v[136:139], a[64:67], v[0:3], 0\n"
"ds_read_b64_tr_b16 a[160:161], v238 offset:49152\n"
"v_mfma_f32_16x16x32_bf16 v[136:139], a[68:71], v[4:7], v[136:139]\n"
"ds_read_b64_tr_b16 a[162:163], v238 offset:51200\n"
"buffer_load_dwordx2 a[110:111], v233, s[44:47], s39 offen offset:1536 sc1\n"
"v_mfma_f32_16x16x32_bf16 v[136:139], a[80:83], v[8:11], v[136:139]\n"
"s_waitcnt lgkmcnt(8)\n"
"ds_read_b64_tr_b16 a[164:165], v238 offset:53248\n"
"v_mul_f32 v132, v241, v132\n"
"v_mfma_f32_16x16x32_bf16 v[136:139], a[84:87], v[12:15], v[136:139]\n"
"ds_read_b64_tr_b16 a[166:167], v238 offset:55296\n"
"v_mul_f32 v133, v241, v133\n"
"v_mfma_f32_16x16x32_bf16 v[136:139], a[96:99], v[16:19], v[136:139]\n"
"ds_read_b64_tr_b16 a[176:177], v238 offset:57344\n"
"v_mul_f32 v134, v241, v134\n"
"v_mfma_f32_16x16x32_bf16 v[136:139], a[100:103], v[20:23], v[136:139]\n"
"ds_read_b64_tr_b16 a[178:179], v238 offset:59392\n"
"v_mul_f32 v135, v241, v135\n"
"v_mfma_f32_16x16x32_bf16 v[136:139], a[112:115], v[24:27], v[136:139]\n"
"ds_read_b64_tr_b16 a[180:181], v238 offset:61440\n"
"buffer_load_dwordx2 a[142:143], v233, s[44:47], s39 offen offset:2048 sc1\n"
"v_mfma_f32_16x16x32_bf16 v[136:139], a[116:119], v[28:31], v[136:139]\n"
"ds_read_b64_tr_b16 a[182:183], v238 offset:63488\n"
"v_mfma_f32_16x16x32_bf16 v[140:143], a[64:67], v[32:35], 0\n"
"ds_read_b64_tr_b16 a[192:193], v239 offset:32768\n"
"v_mfma_f32_16x16x32_bf16 v[140:143], a[68:71], v[36:39], v[140:143]\n"
"ds_read_b64_tr_b16 a[194:195], v239 offset:34816\n"
"v_mfma_f32_16x16x32_bf16 v[140:143], a[80:83], v[40:43], v[140:143]\n"
"ds_read_b64_tr_b16 a[196:197], v239 offset:36864\n"
"v_mul_f32 v136, v240, v136\n"
"v_mfma_f32_16x16x32_bf16 v[140:143], a[84:87], v[44:47], v[140:143]\n"
"ds_read_b64_tr_b16 a[198:199], v239 offset:38912\n"
"buffer_load_dwordx2 a[174:175], v233, s[44:47], s39 offen offset:2560 sc1\n"
"v_mul_f32 v137, v240, v137\n"
"v_mfma_f32_16x16x32_bf16 v[140:143], a[96:99], v[48:51], v[140:143]\n"
"ds_read_b64_tr_b16 a[208:209], v239 offset:40960\n"
"buffer_load_dwordx2 v[246:247], v235, s[56:59], 0 offen sc1\n"
"v_mul_f32 v138, v240, v138\n"
"v_mfma_f32_16x16x32_bf16 v[140:143], a[100:103], v[52:55], v[140:143]\n"
"ds_read_b64_tr_b16 a[210:211], v239 offset:43008\n"
"v_mul_f32 v139, v240, v139\n"
"v_mfma_f32_16x16x32_bf16 v[140:143], a[112:115], v[56:59], v[140:143]\n"
"ds_read_b64_tr_b16 a[212:213], v239 offset:45056\n"
"v_mfma_f32_16x16x32_bf16 v[140:143], a[116:119], v[60:63], v[140:143]\n"
"ds_read_b64_tr_b16 a[214:215], v239 offset:47104\n"
"v_mfma_f32_16x16x32_bf16 v[144:147], a[128:131], v[0:3], 0\n"
"s_waitcnt lgkmcnt(8)\n"
"ds_read_b64_tr_b16 a[224:225], v239 offset:49152\n"
"buffer_load_dwordx2 a[206:207], v233, s[44:47], s39 offen offset:3072 sc1\n"
"v_mfma_f32_16x16x32_bf16 v[144:147], a[132:135], v[4:7], v[144:147]\n"
"ds_read_b64_tr_b16 a[226:227], v239 offset:51200\n"
"v_mfma_f32_16x16x32_bf16 v[144:147], a[144:147], v[8:11], v[144:147]\n"
"ds_read_b64_tr_b16 a[228:229], v239 offset:53248\n"
"v_mul_f32 v140, v241, v140\n"
"v_mfma_f32_16x16x32_bf16 v[144:147], a[148:151], v[12:15], v[144:147]\n"
"ds_read_b64_tr_b16 a[230:231], v239 offset:55296\n"
"v_mul_f32 v141, v241, v141\n"
"v_mfma_f32_16x16x32_bf16 v[144:147], a[160:163], v[16:19], v[144:147]\n"
"ds_read_b64_tr_b16 a[240:241], v239 offset:57344\n"
"v_mul_f32 v142, v241, v142\n"
"v_mfma_f32_16x16x32_bf16 v[144:147], a[164:167], v[20:23], v[144:147]\n"
"ds_read_b64_tr_b16 a[242:243], v239 offset:59392\n"
"buffer_load_dwordx2 a[238:239], v233, s[44:47], s39 offen offset:3584 sc1\n"
"v_mul_f32 v143, v241, v143\n"
"v_mfma_f32_16x16x32_bf16 v[144:147], a[176:179], v[24:27], v[144:147]\n"
"s_waitcnt vmcnt(26)\n"
"ds_read_b64_tr_b16 a[244:245], v239 offset:61440\n"
"v_mfma_f32_16x16x32_bf16 v[144:147], a[180:183], v[28:31], v[144:147]\n"
"s_barrier\n"
"ds_read_b64_tr_b16 a[246:247], v239 offset:63488\n"
"v_mfma_f32_16x16x32_bf16 v[148:151], a[128:131], v[32:35], 0\n"
"s_bitcmp1_b32 s90, 6\n"
"ds_read_b64_tr_b16 a[0:1], v236 offset:0\n"
"v_mfma_f32_16x16x32_bf16 v[148:151], a[132:135], v[36:39], v[148:151]\n"
"s_cselect_b32 s46, s43, 0\n"
"ds_read_b64_tr_b16 a[2:3], v236 offset:2048\n"
"v_mfma_f32_16x16x32_bf16 v[148:151], a[144:147], v[40:43], v[148:151]\n"
"ds_read_b64_tr_b16 a[4:5], v236 offset:4096\n"
"buffer_load_dwordx2 a[26:27], v233, s[44:47], s42 offen offset:0 sc1\n"
"v_mul_f32 v144, v240, v144\n"
"v_mfma_f32_16x16x32_bf16 v[148:151], a[148:151], v[44:47], v[148:151]\n"
"ds_read_b64_tr_b16 a[6:7], v236 offset:6144\n"
"v_mul_f32 v145, v240, v145\n"
"v_mfma_f32_16x16x32_bf16 v[148:151], a[160:163], v[48:51], v[148:151]\n"
"ds_read_b64_tr_b16 a[16:17], v236 offset:8192\n"
"v_mul_f32 v146, v240, v146\n"
"v_mfma_f32_16x16x32_bf16 v[148:151], a[164:167], v[52:55], v[148:151]\n"
"ds_read_b64_tr_b16 a[18:19], v236 offset:10240\n"
"v_mul_f32 v147, v240, v147\n"
"v_mfma_f32_16x16x32_bf16 v[148:151], a[176:179], v[56:59], v[148:151]\n"
"ds_read_b64_tr_b16 a[20:21], v236 offset:12288\n"
"v_mfma_f32_16x16x32_bf16 v[148:151], a[180:183], v[60:63], v[148:151]\n"
"s_waitcnt lgkmcnt(8)\n"
"ds_read_b64_tr_b16 a[22:23], v236 offset:14336\n"
"buffer_load_dwordx2 a[58:59], v233, s[44:47], s42 offen offset:512 sc1\n"
"v_mfma_f32_16x16x32_bf16 v[152:155], a[192:195], v[0:3], 0\n"
"ds_read_b64_tr_b16 a[32:33], v236 offset:16384\n"
"v_mfma_f32_16x16x32_bf16 v[152:155], a[196:199], v[4:7], v[152:155]\n"
"ds_read_b64_tr_b16 a[34:35], v236 offset:18432\n"
"v_mfma_f32_16x16x32_bf16 v[152:155], a[208:211], v[8:11], v[152:155]\n"
"ds_read_b64_tr_b16 a[36:37], v236 offset:20480\n"
"v_mul_f32 v148, v241, v148\n"
"v_mfma_f32_16x16x32_bf16 v[152:155], a[212:215], v[12:15], v[152:155]\n"
"ds_read_b64_tr_b16 a[38:39], v236 offset:22528\n"
"v_mul_f32 v149, v241, v149\n"
"v_mfma_f32_16x16x32_bf16 v[152:155], a[224:227], v[16:19], v[152:155]\n"
"ds_read_b64_tr_b16 a[48:49], v236 offset:24576\n"
"buffer_load_dwordx2 a[90:91], v233, s[44:47], s42 offen offset:1024 sc1\n"
"v_mul_f32 v150, v241, v150\n"
"v_mfma_f32_16x16x32_bf16 v[152:155], a[228:231], v[20:23], v[152:155]\n"
"ds_read_b64_tr_b16 a[50:51], v236 offset:26624\n"
"v_mul_f32 v151, v241, v151\n"
"v_mfma_f32_16x16x32_bf16 v[152:155], a[240:243], v[24:27], v[152:155]\n"
"s_waitcnt lgkmcnt(8)\n"
"ds_read_b64_tr_b16 a[52:53], v236 offset:28672\n"
"v_mfma_f32_16x16x32_bf16 v[152:155], a[244:247], v[28:31], v[152:155]\n"
"ds_read_b64_tr_b16 a[54:55], v236 offset:30720\n"
"v_mfma_f32_16x16x32_bf16 v[156:159], a[192:195], v[32:35], 0\n"
"ds_read_b64_tr_b16 a[64:65], v237 offset:0\n"
"v_mfma_f32_16x16x32_bf16 v[156:159], a[196:199], v[36:39], v[156:159]\n"
"ds_read_b64_tr_b16 a[66:67], v237 offset:2048\n"
"buffer_load_dwordx2 a[122:123], v233, s[44:47], s42 offen offset:1536 sc1\n"
"v_mfma_f32_16x16x32_bf16 v[156:159], a[208:211], v[40:43], v[156:159]\n"
"ds_read_b64_tr_b16 a[68:69], v237 offset:4096\n"
"v_mul_f32 v152, v240, v152\n"
"v_mfma_f32_16x16x32_bf16 v[156:159], a[212:215], v[44:47], v[156:159]\n"
"ds_read_b64_tr_b16 a[70:71], v237 offset:6144\n"
"v_mul_f32 v153, v240, v153\n"
"v_mfma_f32_16x16x32_bf16 v[156:159], a[224:227], v[48:51], v[156:159]\n"
"ds_read_b64_tr_b16 a[80:81], v237 offset:8192\n"
"v_mul_f32 v154, v240, v154\n"
"v_mfma_f32_16x16x32_bf16 v[156:159], a[228:231], v[52:55], v[156:159]\n"
"ds_read_b64_tr_b16 a[82:83], v237 offset:10240\n"
"v_mul_f32 v155, v240, v155\n"
"v_mfma_f32_16x16x32_bf16 v[156:159], a[240:243], v[56:59], v[156:159]\n"
"ds_read_b64_tr_b16 a[84:85], v237 offset:12288\n"
"buffer_load_dwordx2 a[154:155], v233, s[44:47], s42 offen offset:2048 sc1\n"
"v_mfma_f32_16x16x32_bf16 v[156:159], a[244:247], v[60:63], v[156:159]\n"
"ds_read_b64_tr_b16 a[86:87], v237 offset:14336\n"
"v_mfma_f32_16x16x32_bf16 v[128:131], a[0:3], v[64:67], v[128:131]\n"
"ds_read_b64_tr_b16 a[96:97], v237 offset:16384\n"
"v_mfma_f32_16x16x32_bf16 v[128:131], a[4:7], v[68:71], v[128:131]\n"
"ds_read_b64_tr_b16 a[98:99], v237 offset:18432\n"
"v_mfma_f32_16x16x32_bf16 v[128:131], a[16:19], v[72:75], v[128:131]\n"
"s_waitcnt lgkmcnt(8)\n"
"ds_read_b64_tr_b16 a[100:101], v237 offset:20480\n"
"v_mul_f32 v156, v241, v156\n"
"v_mfma_f32_16x16x32_bf16 v[128:131], a[20:23], v[76:79], v[128:131]\n"
"ds_read_b64_tr_b16 a[102:103], v237 offset:22528\n"
"buffer_load_dwordx2 a[186:187], v233, s[44:47], s42 offen offset:2560 sc1\n"
"v_mul_f32 v157, v241, v157\n"
"v_mfma_f32_16x16x32_bf16 v[128:131], a[32:35], v[80:83], v[128:131]\n"
"s_waitcnt vmcnt(14)\n"
"ds_read_b64_tr_b16 a[112:113], v237 offset:24576\n"
"buffer_store_dwordx2 v[244:245], v234, s[56:59], 0 offen offset:640 sc1\n"
"v_mul_f32 v158, v241, v158\n"
"v_mfma_f32_16x16x32_bf16 v[128:131], a[36:39], v[84:87], v[128:131]\n"
"ds_read_b64_tr_b16 a[114:115], v237 offset:26624\n"
"v_mul_f32 v159, v241, v159\n"
"v_mfma_f32_16x16x32_bf16 v[128:131], a[48:51], v[88:91], v[128:131]\n"
"ds_read_b64_tr_b16 a[116:117], v237 offset:28672\n"
"v_mfma_f32_16x16x32_bf16 v[128:131], a[52:55], v[92:95], v[128:131]\n"
"ds_read_b64_tr_b16 a[118:119], v237 offset:30720\n"
"v_mfma_f32_16x16x32_bf16 v[132:135], a[0:3], v[96:99], v[132:135]\n"
"ds_read_b64_tr_b16 a[128:129], v238 offset:0\n"
"buffer_load_dwordx2 a[218:219], v233, s[44:47], s42 offen offset:3072 sc1\n"
"v_mfma_f32_16x16x32_bf16 v[132:135], a[4:7], v[100:103], v[132:135]\n"
"ds_read_b64_tr_b16 a[130:131], v238 offset:2048\n"
"v_mfma_f32_16x16x32_bf16 v[132:135], a[16:19], v[104:107], v[132:135]\n"
"ds_read_b64_tr_b16 a[132:133], v238 offset:4096\n"
"v_cvt_pk_bf16_f32 v160, v128, v129\n"
"v_mfma_f32_16x16x32_bf16 v[132:135], a[20:23], v[108:111], v[132:135]\n"
"ds_read_b64_tr_b16 a[134:135], v238 offset:6144\n"
"v_cvt_pk_bf16_f32 v161, v130, v131\n"
"v_mfma_f32_16x16x32_bf16 v[132:135], a[32:35], v[112:115], v[132:135]\n"
"buffer_store_dwordx2 v[160:161], v233, s[48:51], s85 offen offset:0 sc1\n"
"ds_read_b64_tr_b16 a[144:145], v238 offset:8192\n"
"v_mfma_f32_16x16x32_bf16 v[132:135], a[36:39], v[116:119], v[132:135]\n"
"ds_read_b64_tr_b16 a[146:147], v238 offset:10240\n"
"buffer_load_dwordx2 a[250:251], v233, s[44:47], s42 offen offset:3584 sc1\n"
"v_mfma_f32_16x16x32_bf16 v[132:135], a[48:51], v[120:123], v[132:135]\n"
"ds_read_b64_tr_b16 a[148:149], v238 offset:12288\n"
"v_mfma_f32_16x16x32_bf16 v[132:135], a[52:55], v[124:127], v[132:135]\n"
"ds_read_b64_tr_b16 a[150:151], v238 offset:14336\n"
"v_mfma_f32_16x16x32_bf16 v[136:139], a[64:67], v[64:67], v[136:139]\n"
"s_waitcnt lgkmcnt(8)\n"
"ds_read_b64_tr_b16 a[160:161], v238 offset:16384\n"
"v_mfma_f32_16x16x32_bf16 v[136:139], a[68:71], v[68:71], v[136:139]\n"
"ds_read_b64_tr_b16 a[162:163], v238 offset:18432\n"
"v_mfma_f32_16x16x32_bf16 v[136:139], a[80:83], v[72:75], v[136:139]\n"
"s_waitcnt vmcnt(12)\n"
"ds_read_b64_tr_b16 a[164:165], v238 offset:20480\n"
"v_cvt_pk_bf16_f32 v164, v132, v133\n"
"v_mfma_f32_16x16x32_bf16 v[136:139], a[84:87], v[76:79], v[136:139]\n"
"ds_read_b64_tr_b16 a[166:167], v238 offset:22528\n"
"v_cvt_pk_bf16_f32 v165, v134, v135\n"
"v_mfma_f32_16x16x32_bf16 v[136:139], a[96:99], v[80:83], v[136:139]\n"
"buffer_store_dwordx2 v[164:165], v233, s[48:51], s85 offen offset:512 sc1\n"
"ds_read_b64_tr_b16 a[176:177], v238 offset:24576\n"
"v_mfma_f32_16x16x32_bf16 v[136:139], a[100:103], v[84:87], v[136:139]\n"
"ds_read_b64_tr_b16 a[178:179], v238 offset:26624\n"
"v_cmp_eq_u64 vcc, v[246:247], v[244:245]\n"
"v_mfma_f32_16x16x32_bf16 v[136:139], a[112:115], v[88:91], v[136:139]\n"
"s_and_b32 s87, vcc_lo, 16\n"
"ds_read_b64_tr_b16 a[180:181], v238 offset:28672\n"
"v_mfma_f32_16x16x32_bf16 v[136:139], a[116:119], v[92:95], v[136:139]\n"
"s_or_b32 s90, s90, s87\n"
"ds_read_b64_tr_b16 a[182:183], v238 offset:30720\n"
"v_mfma_f32_16x16x32_bf16 v[140:143], a[64:67], v[96:99], v[140:143]\n"
"s_bitcmp1_b32 s90, 4\n"
"ds_read_b64_tr_b16 a[192:193], v239 offset:0\n"
"v_mfma_f32_16x16x32_bf16 v[140:143], a[68:71], v[100:103], v[140:143]\n"
"s_cselect_b32 s46, s43, 0\n"
"ds_read_b64_tr_b16 a[194:195], v239 offset:2048\n"
"v_mfma_f32_16x16x32_bf16 v[140:143], a[80:83], v[104:107], v[140:143]\n"
"ds_read_b64_tr_b16 a[196:197], v239 offset:4096\n"
"buffer_load_dwordx2 a[24:25], v233, s[44:47], s40 offen offset:0 sc1\n"
"v_cvt_pk_bf16_f32 v168, v136, v137\n"
"v_mfma_f32_16x16x32_bf16 v[140:143], a[84:87], v[108:111], v[140:143]\n"
"ds_read_b64_tr_b16 a[198:199], v239 offset:6144\n"
"v_cvt_pk_bf16_f32 v169, v138, v139\n"
"v_mfma_f32_16x16x32_bf16 v[140:143], a[96:99], v[112:115], v[140:143]\n"
"buffer_store_dwordx2 v[168:169], v233, s[48:51], s85 offen offset:1024 sc1\n"
"ds_read_b64_tr_b16 a[208:209], v239 offset:8192\n"
"v_mfma_f32_16x16x32_bf16 v[140:143], a[100:103], v[116:119], v[140:143]\n"
"ds_read_b64_tr_b16 a[210:211], v239 offset:10240\n"
"v_mfma_f32_16x16x32_bf16 v[140:143], a[112:115], v[120:123], v[140:143]\n"
"ds_read_b64_tr_b16 a[212:213], v239 offset:12288\n"
"v_mfma_f32_16x16x32_bf16 v[140:143], a[116:119], v[124:127], v[140:143]\n"
"s_waitcnt lgkmcnt(8)\n"
"ds_read_b64_tr_b16 a[214:215], v239 offset:14336\n"
"buffer_load_dwordx2 a[56:57], v233, s[44:47], s40 offen offset:512 sc1\n"
"v_mfma_f32_16x16x32_bf16 v[144:147], a[128:131], v[64:67], v[144:147]\n"
"ds_read_b64_tr_b16 a[224:225], v239 offset:16384\n"
"v_mfma_f32_16x16x32_bf16 v[144:147], a[132:135], v[68:71], v[144:147]\n"
"ds_read_b64_tr_b16 a[226:227], v239 offset:18432\n"
"v_mfma_f32_16x16x32_bf16 v[144:147], a[144:147], v[72:75], v[144:147]\n"
"ds_read_b64_tr_b16 a[228:229], v239 offset:20480\n"
"v_cvt_pk_bf16_f32 v172, v140, v141\n"
"v_mfma_f32_16x16x32_bf16 v[144:147], a[148:151], v[76:79], v[144:147]\n"
"ds_read_b64_tr_b16 a[230:231], v239 offset:22528\n"
"v_cvt_pk_bf16_f32 v173, v142, v143\n"
"v_mfma_f32_16x16x32_bf16 v[144:147], a[160:163], v[80:83], v[144:147]\n"
"buffer_store_dwordx2 v[172:173], v233, s[48:51], s85 offen offset:1536 sc1\n"
"ds_read_b64_tr_b16 a[240:241], v239 offset:24576\n"
"v_mfma_f32_16x16x32_bf16 v[144:147], a[164:167], v[84:87], v[144:147]\n"
"ds_read_b64_tr_b16 a[242:243], v239 offset:26624\n"
"buffer_load_dwordx2 a[88:89], v233, s[44:47], s40 offen offset:1024 sc1\n"
"v_mfma_f32_16x16x32_bf16 v[144:147], a[176:179], v[88:91], v[144:147]\n"
"s_waitcnt lgkmcnt(8)\n"
"ds_read_b64_tr_b16 a[244:245], v239 offset:28672\n"
"v_mfma_f32_16x16x32_bf16 v[144:147], a[180:183], v[92:95], v[144:147]\n"
"ds_read_b64_tr_b16 a[246:247], v239 offset:30720\n"
"v_mfma_f32_16x16x32_bf16 v[148:151], a[128:131], v[96:99], v[148:151]\n"
"v_mfma_f32_16x16x32_bf16 v[148:151], a[132:135], v[100:103], v[148:151]\n"
"v_mfma_f32_16x16x32_bf16 v[148:151], a[144:147], v[104:107], v[148:151]\n"
"buffer_load_dwordx2 a[120:121], v233, s[44:47], s40 offen offset:1536 sc1\n"
"v_cvt_pk_bf16_f32 v176, v144, v145\n"
"v_cvt_pk_bf16_f32 v177, v146, v147\n"
"v_mfma_f32_16x16x32_bf16 v[148:151], a[148:151], v[108:111], v[148:151]\n"
"buffer_store_dwordx2 v[176:177], v233, s[48:51], s85 offen offset:2048 sc1\n"
"v_mfma_f32_16x16x32_bf16 v[148:151], a[160:163], v[112:115], v[148:151]\n"
"v_mfma_f32_16x16x32_bf16 v[148:151], a[164:167], v[116:119], v[148:151]\n"
"v_mfma_f32_16x16x32_bf16 v[148:151], a[176:179], v[120:123], v[148:151]\n"
"v_mfma_f32_16x16x32_bf16 v[148:151], a[180:183], v[124:127], v[148:151]\n"
"buffer_load_dwordx2 a[152:153], v233, s[44:47], s40 offen offset:2048 sc1\n"
"v_mfma_f32_16x16x32_bf16 v[152:155], a[192:195], v[64:67], v[152:155]\n"
"v_mfma_f32_16x16x32_bf16 v[152:155], a[196:199], v[68:71], v[152:155]\n"
"v_mfma_f32_16x16x32_bf16 v[152:155], a[208:211], v[72:75], v[152:155]\n"
"s_waitcnt lgkmcnt(0)\n"
"v_cvt_pk_bf16_f32 v180, v148, v149\n"
"v_cvt_pk_bf16_f32 v181, v150, v151\n"
"v_mfma_f32_16x16x32_bf16 v[152:155], a[212:215], v[76:79], v[152:155]\n"
"buffer_store_dwordx2 v[180:181], v233, s[48:51], s85 offen offset:2560 sc1\n"
"v_mfma_f32_16x16x32_bf16 v[152:155], a[224:227], v[80:83], v[152:155]\n"
"buffer_load_dwordx2 a[184:185], v233, s[44:47], s40 offen offset:2560 sc1\n"
"v_mfma_f32_16x16x32_bf16 v[152:155], a[228:231], v[84:87], v[152:155]\n"
"v_mfma_f32_16x16x32_bf16 v[152:155], a[240:243], v[88:91], v[152:155]\n"
"v_mfma_f32_16x16x32_bf16 v[152:155], a[244:247], v[92:95], v[152:155]\n"
"v_mfma_f32_16x16x32_bf16 v[156:159], a[192:195], v[96:99], v[156:159]\n"
"v_mfma_f32_16x16x32_bf16 v[156:159], a[196:199], v[100:103], v[156:159]\n"
"buffer_load_dwordx2 a[216:217], v233, s[44:47], s40 offen offset:3072 sc1\n"
"v_mfma_f32_16x16x32_bf16 v[156:159], a[208:211], v[104:107], v[156:159]\n"
"v_cvt_pk_bf16_f32 v184, v152, v153\n"
"v_cvt_pk_bf16_f32 v185, v154, v155\n"
"v_mfma_f32_16x16x32_bf16 v[156:159], a[212:215], v[108:111], v[156:159]\n"
"buffer_store_dwordx2 v[184:185], v233, s[48:51], s85 offen offset:3072 sc1\n"
"v_mfma_f32_16x16x32_bf16 v[156:159], a[224:227], v[112:115], v[156:159]\n"
"v_mfma_f32_16x16x32_bf16 v[156:159], a[228:231], v[116:119], v[156:159]\n"
"v_mfma_f32_16x16x32_bf16 v[156:159], a[240:243], v[120:123], v[156:159]\n"
"buffer_load_dwordx2 a[248:249], v233, s[44:47], s40 offen offset:3584 sc1\n"
"v_mfma_f32_16x16x32_bf16 v[156:159], a[244:247], v[124:127], v[156:159]\n"
"buffer_load_dwordx2 v[246:247], v235, s[56:59], 0 offen sc1\n"
"s_cmp_eq_u32 s34, 7\n"
"s_cbranch_scc0 Lowner%=\n"
"s_nop 7\n"
"v_cvt_pk_bf16_f32 v188, v156, v157\n"
"v_cvt_pk_bf16_f32 v189, v158, v159\n"
"buffer_store_dwordx2 v[188:189], v233, s[48:51], s85 offen offset:3584 sc1\n"
"s_waitcnt vmcnt(0)\n"
"buffer_store_dwordx2 v[244:245], v234, s[56:59], 0 offen offset:768 sc1\n"
"s_branch Ldone%=\n"
"Lowner%=:\n"
"s_mov_b32 s46, s43\n"
"s_mov_b32 s81, 0x40000\n"
"s_mov_b32 s94, 0\n"
"Lpoll%=:\n"
"s_mov_b32 s92, s90\n"
"s_waitcnt vmcnt(0)\n"
"v_cmp_eq_u64 vcc, v[246:247], v[244:245]\n"
"s_sub_u32 s81, s81, 1\n"
"s_cmp_eq_u32 s81, 0\n"
"s_cselect_b32 s93, -1, vcc_lo\n"
"s_andn2_b32 s93, s93, s90\n"
"s_and_b32 s93, s93, 0x7f\n"
"s_cmp_eq_u32 s93, 0\n"
"s_cbranch_scc1 Lskb0%=\n"
"s_bitcmp1_b32 s93, 5\n"
"s_cbranch_scc0 Lskb5%=\n"
"buffer_load_dwordx2 a[28:29], v233, s[44:47], s41 offen offset:0 sc1\n"
"buffer_load_dwordx2 a[60:61], v233, s[44:47], s41 offen offset:512 sc1\n"
"buffer_load_dwordx2 a[92:93], v233, s[44:47], s41 offen offset:1024 sc1\n"
"buffer_load_dwordx2 a[124:125], v233, s[44:47], s41 offen offset:1536 sc1\n"
"buffer_load_dwordx2 a[156:157], v233, s[44:47], s41 offen offset:2048 sc1\n"
"buffer_load_dwordx2 a[188:189], v233, s[44:47], s41 offen offset:2560 sc1\n"
"buffer_load_dwordx2 a[220:221], v233, s[44:47], s41 offen offset:3072 sc1\n"
"buffer_load_dwordx2 a[252:253], v233, s[44:47], s41 offen offset:3584 sc1\n"
"s_cmp_eq_u32 s93, 32\n"
"s_cbranch_scc1 Lskb0%=\n"
"Lskb5%=:\n"
"s_bitcmp1_b32 s93, 6\n"
"s_cbranch_scc0 Lskb6%=\n"
"buffer_load_dwordx2 a[26:27], v233, s[44:47], s42 offen offset:0 sc1\n"
"buffer_load_dwordx2 a[58:59], v233, s[44:47], s42 offen offset:512 sc1\n"
"buffer_load_dwordx2 a[90:91], v233, s[44:47], s42 offen offset:1024 sc1\n"
"buffer_load_dwordx2 a[122:123], v233, s[44:47], s42 offen offset:1536 sc1\n"
"buffer_load_dwordx2 a[154:155], v233, s[44:47], s42 offen offset:2048 sc1\n"
"buffer_load_dwordx2 a[186:187], v233, s[44:47], s42 offen offset:2560 sc1\n"
"buffer_load_dwordx2 a[218:219], v233, s[44:47], s42 offen offset:3072 sc1\n"
"buffer_load_dwordx2 a[250:251], v233, s[44:47], s42 offen offset:3584 sc1\n"
"Lskb6%=:\n"
"s_bitcmp1_b32 s93, 4\n"
"s_cbranch_scc0 Lskb4%=\n"
"buffer_load_dwordx2 a[24:25], v233, s[44:47], s40 offen offset:0 sc1\n"
"buffer_load_dwordx2 a[56:57], v233, s[44:47], s40 offen offset:512 sc1\n"
"buffer_load_dwordx2 a[88:89], v233, s[44:47], s40 offen offset:1024 sc1\n"
"buffer_load_dwordx2 a[120:121], v233, s[44:47], s40 offen offset:1536 sc1\n"
"buffer_load_dwordx2 a[152:153], v233, s[44:47], s40 offen offset:2048 sc1\n"
"buffer_load_dwordx2 a[184:185], v233, s[44:47], s40 offen offset:2560 sc1\n"
"buffer_load_dwordx2 a[216:217], v233, s[44:47], s40 offen offset:3072 sc1\n"
"buffer_load_dwordx2 a[248:249], v233, s[44:47], s40 offen offset:3584 sc1\n"
"Lskb4%=:\n"
"s_bitcmp1_b32 s93, 3\n"
"s_cbranch_scc0 Lskb3%=\n"
"buffer_load_dwordx2 a[14:15], v233, s[44:47], s39 offen offset:0 sc1\n"
"buffer_load_dwordx2 a[46:47], v233, s[44:47], s39 offen offset:512 sc1\n"
"buffer_load_dwordx2 a[78:79], v233, s[44:47], s39 offen offset:1024 sc1\n"
"buffer_load_dwordx2 a[110:111], v233, s[44:47], s39 offen offset:1536 sc1\n"
"buffer_load_dwordx2 a[142:143], v233, s[44:47], s39 offen offset:2048 sc1\n"
"buffer_load_dwordx2 a[174:175], v233, s[44:47], s39 offen offset:2560 sc1\n"
"buffer_load_dwordx2 a[206:207], v233, s[44:47], s39 offen offset:3072 sc1\n"
"buffer_load_dwordx2 a[238:239], v233, s[44:47], s39 offen offset:3584 sc1\n"
"Lskb3%=:\n"
"s_bitcmp1_b32 s93, 2\n"
"s_cbranch_scc0 Lskb2%=\n"
"buffer_load_dwordx2 a[12:13], v233, s[44:47], s38 offen offset:0 sc1\n"
"buffer_load_dwordx2 a[44:45], v233, s[44:47], s38 offen offset:512 sc1\n"
"buffer_load_dwordx2 a[76:77], v233, s[44:47], s38 offen offset:1024 sc1\n"
"buffer_load_dwordx2 a[108:109], v233, s[44:47], s38 offen offset:1536 sc1\n"
"buffer_load_dwordx2 a[140:141], v233, s[44:47], s38 offen offset:2048 sc1\n"
"buffer_load_dwordx2 a[172:173], v233, s[44:47], s38 offen offset:2560 sc1\n"
"buffer_load_dwordx2 a[204:205], v233, s[44:47], s38 offen offset:3072 sc1\n"
"buffer_load_dwordx2 a[236:237], v233, s[44:47], s38 offen offset:3584 sc1\n"
"Lskb2%=:\n"
"s_bitcmp1_b32 s93, 1\n"
"s_cbranch_scc0 Lskb1%=\n"
"buffer_load_dwordx2 a[10:11], v233, s[44:47], s37 offen offset:0 sc1\n"
"buffer_load_dwordx2 a[42:43], v233, s[44:47], s37 offen offset:512 sc1\n"
"buffer_load_dwordx2 a[74:75], v233, s[44:47], s37 offen offset:1024 sc1\n"
"buffer_load_dwordx2 a[106:107], v233, s[44:47], s37 offen offset:1536 sc1\n"
"buffer_load_dwordx2 a[138:139], v233, s[44:47], s37 offen offset:2048 sc1\n"
"buffer_load_dwordx2 a[170:171], v233, s[44:47], s37 offen offset:2560 sc1\n"
"buffer_load_dwordx2 a[202:203], v233, s[44:47], s37 offen offset:3072 sc1\n"
"buffer_load_dwordx2 a[234:235], v233, s[44:47], s37 offen offset:3584 sc1\n"
"Lskb1%=:\n"
"s_bitcmp1_b32 s93, 0\n"
"s_cbranch_scc0 Lskb0%=\n"
"buffer_load_dwordx2 a[8:9], v233, s[44:47], s36 offen offset:0 sc1\n"
"buffer_load_dwordx2 a[40:41], v233, s[44:47], s36 offen offset:512 sc1\n"
"buffer_load_dwordx2 a[72:73], v233, s[44:47], s36 offen offset:1024 sc1\n"
"buffer_load_dwordx2 a[104:105], v233, s[44:47], s36 offen offset:1536 sc1\n"
"buffer_load_dwordx2 a[136:137], v233, s[44:47], s36 offen offset:2048 sc1\n"
"buffer_load_dwordx2 a[168:169], v233, s[44:47], s36 offen offset:2560 sc1\n"
"buffer_load_dwordx2 a[200:201], v233, s[44:47], s36 offen offset:3072 sc1\n"
"buffer_load_dwordx2 a[232:233], v233, s[44:47], s36 offen offset:3584 sc1\n"
"Lskb0%=:\n"
"s_or_b32 s90, s90, s93\n"
"s_cmp_eq_u32 s90, 0x7f\n"
"s_cbranch_scc1 Lpolled%=\n"
"buffer_load_dwordx2 v[246:247], v235, s[56:59], 0 offen sc1\n"
"Lpolled%=:\n"
"s_cmp_gt_u32 s94, 0\n"
"s_cbranch_scc1 Lskc0%=\n"
"s_and_b32 s87, s92, 3\n"
"s_cmp_eq_u32 s87, 3\n"
"s_cbranch_scc0 Lpoll%=\n"
"s_mov_b32 s94, 1\n"
"v_mfma_f32_16x16x32_bf16 v[128:131], v[226:229], a[8:11], v[128:131]\n"
"v_mfma_f32_16x16x32_bf16 v[132:135], v[226:229], a[40:43], v[132:135]\n"
"v_mfma_f32_16x16x32_bf16 v[136:139], v[226:229], a[72:75], v[136:139]\n"
"v_mfma_f32_16x16x32_bf16 v[140:143], v[226:229], a[104:107], v[140:143]\n"
"v_mfma_f32_16x16x32_bf16 v[144:147], v[226:229], a[136:139], v[144:147]\n"
"v_mfma_f32_16x16x32_bf16 v[148:151], v[226:229], a[168:171], v[148:151]\n"
"v_mfma_f32_16x16x32_bf16 v[152:155], v[226:229], a[200:203], v[152:155]\n"
"v_mfma_f32_16x16x32_bf16 v[156:159], v[226:229], a[232:235], v[156:159]\n"
"Lskc0%=:\n"
"s_cmp_gt_u32 s94, 1\n"
"s_cbranch_scc1 Lskc1%=\n"
"s_and_b32 s87, s92, 12\n"
"s_cmp_eq_u32 s87, 12\n"
"s_cbranch_scc0 Lpoll%=\n"
"s_mov_b32 s94, 2\n"
"v_mfma_f32_16x16x32_bf16 v[128:131], v[226:229], a[12:15], v[128:131]\n"
"v_mfma_f32_16x16x32_bf16 v[132:135], v[226:229], a[44:47], v[132:135]\n"
"v_mfma_f32_16x16x32_bf16 v[136:139], v[226:229], a[76:79], v[136:139]\n"
"v_mfma_f32_16x16x32_bf16 v[140:143], v[226:229], a[108:111], v[140:143]\n"
"v_mfma_f32_16x16x32_bf16 v[144:147], v[226:229], a[140:143], v[144:147]\n"
"v_mfma_f32_16x16x32_bf16 v[148:151], v[226:229], a[172:175], v[148:151]\n"
"v_mfma_f32_16x16x32_bf16 v[152:155], v[226:229], a[204:207], v[152:155]\n"
"v_mfma_f32_16x16x32_bf16 v[156:159], v[226:229], a[236:239], v[156:159]\n"
"Lskc1%=:\n"
"s_cmp_gt_u32 s94, 2\n"
"s_cbranch_scc1 Lskc2%=\n"
"s_and_b32 s87, s92, 80\n"
"s_cmp_eq_u32 s87, 80\n"
"s_cbranch_scc0 Lpoll%=\n"
"s_mov_b32 s94, 3\n"
"v_mfma_f32_16x16x32_bf16 v[128:131], v[226:229], a[24:27], v[128:131]\n"
"v_mfma_f32_16x16x32_bf16 v[132:135], v[226:229], a[56:59], v[132:135]\n"
"v_mfma_f32_16x16x32_bf16 v[136:139], v[226:229], a[88:91], v[136:139]\n"
"v_mfma_f32_16x16x32_bf16 v[140:143], v[226:229], a[120:123], v[140:143]\n"
"v_mfma_f32_16x16x32_bf16 v[144:147], v[226:229], a[152:155], v[144:147]\n"
"v_mfma_f32_16x16x32_bf16 v[148:151], v[226:229], a[184:187], v[148:151]\n"
"v_mfma_f32_16x16x32_bf16 v[152:155], v[226:229], a[216:219], v[152:155]\n"
"v_mfma_f32_16x16x32_bf16 v[156:159], v[226:229], a[248:251], v[156:159]\n"
"Lskc2%=:\n"
"s_and_b32 s87, s92, 32\n"
"s_cmp_eq_u32 s87, 32\n"
"s_cbranch_scc0 Lpoll%=\n"
"s_mov_b32 s94, 4\n"
"v_mfma_f32_16x16x32_bf16 v[128:131], v[224:227], a[26:29], v[128:131]\n"
"v_mfma_f32_16x16x32_bf16 v[132:135], v[224:227], a[58:61], v[132:135]\n"
"v_mfma_f32_16x16x32_bf16 v[136:139], v[224:227], a[90:93], v[136:139]\n"
"v_mfma_f32_16x16x32_bf16 v[140:143], v[224:227], a[122:125], v[140:143]\n"
"v_mfma_f32_16x16x32_bf16 v[144:147], v[224:227], a[154:157], v[144:147]\n"
"v_mfma_f32_16x16x32_bf16 v[148:151], v[224:227], a[186:189], v[148:151]\n"
"v_mfma_f32_16x16x32_bf16 v[152:155], v[224:227], a[218:221], v[152:155]\n"
"v_mfma_f32_16x16x32_bf16 v[156:159], v[224:227], a[250:253], v[156:159]\n"
"s_lshl_b32 s87, s34, 7\n"
"s_add_u32 s37, s87, 14336\n"
"s_add_u32 s38, s87, 28672\n"
"s_add_u32 s39, s87, 43008\n"
"v_cvt_pk_bf16_f32 v128, v128, v129\n"
"v_cvt_pk_bf16_f32 v129, v130, v131\n"
"ds_write_b64 v248, v[128:129] offset:0\n"
"v_cvt_pk_bf16_f32 v132, v132, v133\n"
"v_cvt_pk_bf16_f32 v133, v134, v135\n"
"ds_write_b64 v248, v[132:133] offset:2304\n"
"v_cvt_pk_bf16_f32 v136, v136, v137\n"
"v_cvt_pk_bf16_f32 v137, v138, v139\n"
"ds_write_b64 v248, v[136:137] offset:32\n"
"v_cvt_pk_bf16_f32 v140, v140, v141\n"
"v_cvt_pk_bf16_f32 v141, v142, v143\n"
"ds_write_b64 v248, v[140:141] offset:2336\n"
"v_cvt_pk_bf16_f32 v144, v144, v145\n"
"v_cvt_pk_bf16_f32 v145, v146, v147\n"
"ds_write_b64 v248, v[144:145] offset:64\n"
"v_cvt_pk_bf16_f32 v148, v148, v149\n"
"v_cvt_pk_bf16_f32 v149, v150, v151\n"
"ds_write_b64 v248, v[148:149] offset:2368\n"
"v_cvt_pk_bf16_f32 v152, v152, v153\n"
"v_cvt_pk_bf16_f32 v153, v154, v155\n"
"ds_write_b64 v248, v[152:153] offset:96\n"
"v_cvt_pk_bf16_f32 v156, v156, v157\n"
"v_cvt_pk_bf16_f32 v157, v158, v159\n"
"ds_write_b64 v248, v[156:157] offset:2400\n"
"ds_read_b128 v[0:3], v249 offset:0\n"
"ds_read_b128 v[4:7], v249 offset:1152\n"
"ds_read_b128 v[8:11], v249 offset:2304\n"
"ds_read_b128 v[12:15], v249 offset:3456\n"
"s_waitcnt lgkmcnt(3)\n"
"buffer_store_dwordx4 v[0:3], v250, s[60:63], s87 offen sc1\n"
"s_waitcnt lgkmcnt(2)\n"
"buffer_store_dwordx4 v[4:7], v250, s[60:63], s37 offen sc1\n"
"s_waitcnt lgkmcnt(1)\n"
"buffer_store_dwordx4 v[8:11], v250, s[60:63], s38 offen sc1\n"
"s_waitcnt lgkmcnt(0)\n"
"buffer_store_dwordx4 v[12:15], v250, s[60:63], s39 offen sc1\n"
"s_or_b32 s87, s34, s80\n"
"s_cmp_eq_u32 s87, 0\n"
"s_cbranch_scc0 Ldone%=\n"
"s_mov_b32 s87, 32768\n"
"buffer_store_dwordx2 v[244:245], v234, s[56:59], s87 offen sc1\n"
"Ldone%=:\n"
"s_waitcnt vmcnt(0)\n"
"s_branch Lcodeend%=\n"
"Lfallback%=:\n"
"s_mov_b32 s94, 1\n"
"s_waitcnt vmcnt(0) lgkmcnt(0)\n"
"s_barrier\n"
"v_mbcnt_lo_u32_b32 v128, -1, 0\n"
"v_mbcnt_hi_u32_b32 v128, -1, v128\n"
"s_mov_b32 s84, 0\n"
"s_mov_b32 s85, 0\n"
"s_mov_b32 s46, 0x80000\n"
"s_mov_b32 s50, 0x80000\n"
"s_add_u32 s83, s82, 135168\n"
"v_lshrrev_b32 v129, 2, v128\n"
"v_mul_u32_u24 v129, 1056, v129\n"
"v_and_b32 v130, 3, v128\n"
"v_lshl_add_u32 v129, v130, 3, v129\n"
"v_add_u32 v253, 101376, v129\n"
"v_add_u32 v254, 118272, v129\n"
"s_mov_b32 s89, 33792\n"
"s_mov_b32 s90, 50688\n"
"v_mov_b32 v232, 0xff800000\n"
"v_mov_b32 v236, 0xff800000\n"
"v_mov_b32 v240, 0xff800000\n"
"v_mov_b32 v244, 0xff800000\n"
"Lfbloop%=:\n"
"s_add_u32 m0, s82, 0\n"
"s_nop 0\n"
"buffer_load_dwordx4 v255, s[44:47], s84 offen offset:0 lds\n"
"s_add_u32 m0, s82, 544\n"
"s_nop 0\n"
"buffer_load_dwordx4 v255, s[44:47], s84 offen offset:512 lds\n"
"s_add_u32 m0, s82, 1088\n"
"s_nop 0\n"
"buffer_load_dwordx4 v255, s[44:47], s84 offen offset:1024 nt lds\n"
"s_add_u32 m0, s82, 1632\n"
"s_nop 0\n"
"buffer_load_dwordx4 v255, s[44:47], s84 offen offset:1536 nt lds\n"
"s_add_u32 s84, s84, 0x4000\n"
"s_cmp_lt_u32 s84, 0x80000\n"
"s_cselect_b32 s46, 0x80000, 0\n"
"s_waitcnt vmcnt(0)\n"
"s_barrier\n"
"ds_read_b128 v[200:203], v252 offset:0\n"
"ds_read_b128 v[204:207], v252 offset:64\n"
"ds_read_b128 v[208:211], v252 offset:128\n"
"ds_read_b128 v[212:215], v252 offset:192\n"
"ds_read_b128 v[216:219], v252 offset:256\n"
"ds_read_b128 v[220:223], v252 offset:320\n"
"ds_read_b128 v[224:227], v252 offset:384\n"
"ds_read_b128 v[228:231], v252 offset:448\n"
"s_waitcnt lgkmcnt(0)\n"
"v_mfma_f32_16x16x32_bf16 v[132:135], v[200:203], v[0:3], 0\n"
"v_mfma_f32_16x16x32_bf16 v[132:135], v[204:207], v[4:7], v[132:135]\n"
"v_mfma_f32_16x16x32_bf16 v[150:153], v[200:203], v[32:35], 0\n"
"v_mfma_f32_16x16x32_bf16 v[150:153], v[204:207], v[36:39], v[150:153]\n"
"v_mfma_f32_16x16x32_bf16 v[168:171], v[200:203], v[64:67], 0\n"
"v_mfma_f32_16x16x32_bf16 v[168:171], v[204:207], v[68:71], v[168:171]\n"
"v_mfma_f32_16x16x32_bf16 v[186:189], v[200:203], v[96:99], 0\n"
"v_mfma_f32_16x16x32_bf16 v[186:189], v[204:207], v[100:103], v[186:189]\n"
"v_mfma_f32_16x16x32_bf16 v[132:135], v[208:211], v[8:11], v[132:135]\n"
"v_mfma_f32_16x16x32_bf16 v[132:135], v[212:215], v[12:15], v[132:135]\n"
"v_mfma_f32_16x16x32_bf16 v[150:153], v[208:211], v[40:43], v[150:153]\n"
"v_mfma_f32_16x16x32_bf16 v[150:153], v[212:215], v[44:47], v[150:153]\n"
"v_mfma_f32_16x16x32_bf16 v[168:171], v[208:211], v[72:75], v[168:171]\n"
"v_mfma_f32_16x16x32_bf16 v[168:171], v[212:215], v[76:79], v[168:171]\n"
"v_mfma_f32_16x16x32_bf16 v[186:189], v[208:211], v[104:107], v[186:189]\n"
"v_mfma_f32_16x16x32_bf16 v[186:189], v[212:215], v[108:111], v[186:189]\n"
"v_mfma_f32_16x16x32_bf16 v[132:135], v[216:219], v[16:19], v[132:135]\n"
"v_mfma_f32_16x16x32_bf16 v[132:135], v[220:223], v[20:23], v[132:135]\n"
"v_mfma_f32_16x16x32_bf16 v[150:153], v[216:219], v[48:51], v[150:153]\n"
"v_mfma_f32_16x16x32_bf16 v[150:153], v[220:223], v[52:55], v[150:153]\n"
"v_mfma_f32_16x16x32_bf16 v[168:171], v[216:219], v[80:83], v[168:171]\n"
"v_mfma_f32_16x16x32_bf16 v[168:171], v[220:223], v[84:87], v[168:171]\n"
"v_mfma_f32_16x16x32_bf16 v[186:189], v[216:219], v[112:115], v[186:189]\n"
"v_mfma_f32_16x16x32_bf16 v[186:189], v[220:223], v[116:119], v[186:189]\n"
"v_mfma_f32_16x16x32_bf16 v[132:135], v[224:227], v[24:27], v[132:135]\n"
"v_mfma_f32_16x16x32_bf16 v[132:135], v[228:231], v[28:31], v[132:135]\n"
"v_mfma_f32_16x16x32_bf16 v[150:153], v[224:227], v[56:59], v[150:153]\n"
"v_mfma_f32_16x16x32_bf16 v[150:153], v[228:231], v[60:63], v[150:153]\n"
"v_mfma_f32_16x16x32_bf16 v[168:171], v[224:227], v[88:91], v[168:171]\n"
"v_mfma_f32_16x16x32_bf16 v[168:171], v[228:231], v[92:95], v[168:171]\n"
"v_mfma_f32_16x16x32_bf16 v[186:189], v[224:227], v[120:123], v[186:189]\n"
"v_mfma_f32_16x16x32_bf16 v[186:189], v[228:231], v[124:127], v[186:189]\n"
"s_nop 7\n"
"s_nop 3\n"
"v_max3_f32 v232, v232, v132, v133\n"
"v_max3_f32 v232, v232, v134, v135\n"
"v_max3_f32 v236, v236, v150, v151\n"
"v_max3_f32 v236, v236, v152, v153\n"
"v_max3_f32 v240, v240, v168, v169\n"
"v_max3_f32 v240, v240, v170, v171\n"
"v_max3_f32 v244, v244, v186, v187\n"
"v_max3_f32 v244, v244, v188, v189\n"
"ds_read_b128 v[200:203], v252 offset:512\n"
"ds_read_b128 v[204:207], v252 offset:576\n"
"ds_read_b128 v[208:211], v252 offset:640\n"
"ds_read_b128 v[212:215], v252 offset:704\n"
"ds_read_b128 v[216:219], v252 offset:768\n"
"ds_read_b128 v[220:223], v252 offset:832\n"
"ds_read_b128 v[224:227], v252 offset:896\n"
"ds_read_b128 v[228:231], v252 offset:960\n"
"s_waitcnt lgkmcnt(0)\n"
"v_mfma_f32_16x16x32_bf16 v[132:135], v[200:203], v[0:3], 0\n"
"v_mfma_f32_16x16x32_bf16 v[132:135], v[204:207], v[4:7], v[132:135]\n"
"v_mfma_f32_16x16x32_bf16 v[150:153], v[200:203], v[32:35], 0\n"
"v_mfma_f32_16x16x32_bf16 v[150:153], v[204:207], v[36:39], v[150:153]\n"
"v_mfma_f32_16x16x32_bf16 v[168:171], v[200:203], v[64:67], 0\n"
"v_mfma_f32_16x16x32_bf16 v[168:171], v[204:207], v[68:71], v[168:171]\n"
"v_mfma_f32_16x16x32_bf16 v[186:189], v[200:203], v[96:99], 0\n"
"v_mfma_f32_16x16x32_bf16 v[186:189], v[204:207], v[100:103], v[186:189]\n"
"v_mfma_f32_16x16x32_bf16 v[132:135], v[208:211], v[8:11], v[132:135]\n"
"v_mfma_f32_16x16x32_bf16 v[132:135], v[212:215], v[12:15], v[132:135]\n"
"v_mfma_f32_16x16x32_bf16 v[150:153], v[208:211], v[40:43], v[150:153]\n"
"v_mfma_f32_16x16x32_bf16 v[150:153], v[212:215], v[44:47], v[150:153]\n"
"v_mfma_f32_16x16x32_bf16 v[168:171], v[208:211], v[72:75], v[168:171]\n"
"v_mfma_f32_16x16x32_bf16 v[168:171], v[212:215], v[76:79], v[168:171]\n"
"v_mfma_f32_16x16x32_bf16 v[186:189], v[208:211], v[104:107], v[186:189]\n"
"v_mfma_f32_16x16x32_bf16 v[186:189], v[212:215], v[108:111], v[186:189]\n"
"v_mfma_f32_16x16x32_bf16 v[132:135], v[216:219], v[16:19], v[132:135]\n"
"v_mfma_f32_16x16x32_bf16 v[132:135], v[220:223], v[20:23], v[132:135]\n"
"v_mfma_f32_16x16x32_bf16 v[150:153], v[216:219], v[48:51], v[150:153]\n"
"v_mfma_f32_16x16x32_bf16 v[150:153], v[220:223], v[52:55], v[150:153]\n"
"v_mfma_f32_16x16x32_bf16 v[168:171], v[216:219], v[80:83], v[168:171]\n"
"v_mfma_f32_16x16x32_bf16 v[168:171], v[220:223], v[84:87], v[168:171]\n"
"v_mfma_f32_16x16x32_bf16 v[186:189], v[216:219], v[112:115], v[186:189]\n"
"v_mfma_f32_16x16x32_bf16 v[186:189], v[220:223], v[116:119], v[186:189]\n"
"v_mfma_f32_16x16x32_bf16 v[132:135], v[224:227], v[24:27], v[132:135]\n"
"v_mfma_f32_16x16x32_bf16 v[132:135], v[228:231], v[28:31], v[132:135]\n"
"v_mfma_f32_16x16x32_bf16 v[150:153], v[224:227], v[56:59], v[150:153]\n"
"v_mfma_f32_16x16x32_bf16 v[150:153], v[228:231], v[60:63], v[150:153]\n"
"v_mfma_f32_16x16x32_bf16 v[168:171], v[224:227], v[88:91], v[168:171]\n"
"v_mfma_f32_16x16x32_bf16 v[168:171], v[228:231], v[92:95], v[168:171]\n"
"v_mfma_f32_16x16x32_bf16 v[186:189], v[224:227], v[120:123], v[186:189]\n"
"v_mfma_f32_16x16x32_bf16 v[186:189], v[228:231], v[124:127], v[186:189]\n"
"s_nop 7\n"
"s_nop 3\n"
"v_max3_f32 v232, v232, v132, v133\n"
"v_max3_f32 v232, v232, v134, v135\n"
"v_max3_f32 v236, v236, v150, v151\n"
"v_max3_f32 v236, v236, v152, v153\n"
"v_max3_f32 v240, v240, v168, v169\n"
"v_max3_f32 v240, v240, v170, v171\n"
"v_max3_f32 v244, v244, v186, v187\n"
"v_max3_f32 v244, v244, v188, v189\n"
"s_barrier\n"
"s_cmp_lt_u32 s84, 0x80000\n"
"s_cbranch_scc1 Lfbloop%=\n"
"v_mov_b32 v233, v232\n"
"v_mov_b32 v237, v236\n"
"v_mov_b32 v241, v240\n"
"v_mov_b32 v245, v244\n"
"s_nop 1\n"
"v_permlane32_swap_b32 v232, v233\n"
"v_permlane32_swap_b32 v236, v237\n"
"v_permlane32_swap_b32 v240, v241\n"
"v_permlane32_swap_b32 v244, v245\n"
"s_nop 1\n"
"v_max_f32 v232, v232, v233\n"
"v_max_f32 v236, v236, v237\n"
"v_max_f32 v240, v240, v241\n"
"v_max_f32 v244, v244, v245\n"
"v_mov_b32 v233, v232\n"
"v_mov_b32 v237, v236\n"
"v_mov_b32 v241, v240\n"
"v_mov_b32 v245, v244\n"
"s_nop 1\n"
"v_permlane16_swap_b32 v232, v233\n"
"v_permlane16_swap_b32 v236, v237\n"
"v_permlane16_swap_b32 v240, v241\n"
"v_permlane16_swap_b32 v244, v245\n"
"s_nop 1\n"
"v_max_f32 v232, v232, v233\n"
"v_max_f32 v236, v236, v237\n"
"v_max_f32 v240, v240, v241\n"
"v_max_f32 v244, v244, v245\n"
"v_xor_b32 v232, 0x80000000, v232\n"
"v_xor_b32 v236, 0x80000000, v236\n"
"v_xor_b32 v240, 0x80000000, v240\n"
"v_xor_b32 v244, 0x80000000, v244\n"
"s_mov_b32 s84, 0\n"
"s_mov_b32 s46, 0x80000\n"
"s_add_u32 m0, s82, 0\n"
"s_nop 0\n"
"buffer_load_dwordx4 v255, s[44:47], s84 offen offset:0 lds\n"
"s_add_u32 m0, s82, 544\n"
"s_nop 0\n"
"buffer_load_dwordx4 v255, s[44:47], s84 offen offset:512 lds\n"
"s_add_u32 m0, s82, 1088\n"
"s_nop 0\n"
"buffer_load_dwordx4 v255, s[44:47], s84 offen offset:1024 nt lds\n"
"s_add_u32 m0, s82, 1632\n"
"s_nop 0\n"
"buffer_load_dwordx4 v255, s[44:47], s84 offen offset:1536 nt lds\n"
"s_add_u32 s84, s84, 0x4000\n"
"s_cmp_lt_u32 s84, 0x80000\n"
"s_cselect_b32 s46, 0x80000, 0\n"
"s_add_u32 m0, s82, 16896\n"
"s_nop 0\n"
"buffer_load_dwordx4 v255, s[44:47], s84 offen offset:0 lds\n"
"s_add_u32 m0, s82, 17440\n"
"s_nop 0\n"
"buffer_load_dwordx4 v255, s[44:47], s84 offen offset:512 lds\n"
"s_add_u32 m0, s82, 17984\n"
"s_nop 0\n"
"buffer_load_dwordx4 v255, s[44:47], s84 offen offset:1024 nt lds\n"
"s_add_u32 m0, s82, 18528\n"
"s_nop 0\n"
"buffer_load_dwordx4 v255, s[44:47], s84 offen offset:1536 nt lds\n"
"s_add_u32 s84, s84, 0x4000\n"
"s_cmp_lt_u32 s84, 0x80000\n"
"s_cselect_b32 s46, 0x80000, 0\n"
"s_add_u32 m0, s83, 0\n"
"s_nop 0\n"
"buffer_load_dwordx4 v255, s[48:51], s85 offen offset:0 nt lds\n"
"s_add_u32 m0, s83, 544\n"
"s_nop 0\n"
"buffer_load_dwordx4 v255, s[48:51], s85 offen offset:512 nt lds\n"
"s_add_u32 m0, s83, 1088\n"
"s_nop 0\n"
"buffer_load_dwordx4 v255, s[48:51], s85 offen offset:1024 nt lds\n"
"s_add_u32 m0, s83, 1632\n"
"s_nop 0\n"
"buffer_load_dwordx4 v255, s[48:51], s85 offen offset:1536 nt lds\n"
"s_add_u32 s85, s85, 0x4000\n"
"s_cmp_lt_u32 s85, 0x80000\n"
"s_cselect_b32 s50, 0x80000, 0\n"
"s_add_u32 s83, s83, 16896\n"
"s_cmp_ge_u32 s83, s91\n"
"s_cselect_b32 s87, 84480, 0\n"
"s_sub_u32 s83, s83, s87\n"
"s_add_u32 m0, s82, 33792\n"
"s_nop 0\n"
"buffer_load_dwordx4 v255, s[44:47], s84 offen offset:0 lds\n"
"s_add_u32 m0, s82, 34336\n"
"s_nop 0\n"
"buffer_load_dwordx4 v255, s[44:47], s84 offen offset:512 lds\n"
"s_add_u32 m0, s82, 34880\n"
"s_nop 0\n"
"buffer_load_dwordx4 v255, s[44:47], s84 offen offset:1024 nt lds\n"
"s_add_u32 m0, s82, 35424\n"
"s_nop 0\n"
"buffer_load_dwordx4 v255, s[44:47], s84 offen offset:1536 nt lds\n"
"s_add_u32 s84, s84, 0x4000\n"
"s_cmp_lt_u32 s84, 0x80000\n"
"s_cselect_b32 s46, 0x80000, 0\n"
"s_waitcnt vmcnt(0)\n"
"s_barrier\n"
"ds_read_b128 v[200:203], v252 offset:0\n"
"ds_read_b128 v[204:207], v252 offset:64\n"
"ds_read_b128 v[208:211], v252 offset:128\n"
"ds_read_b128 v[212:215], v252 offset:192\n"
"ds_read_b128 v[216:219], v252 offset:256\n"
"ds_read_b128 v[220:223], v252 offset:320\n"
"s_waitcnt lgkmcnt(0)\n"
"s_branch Lrerun%=\n"
"Lcodeend%=:\n"
  :: [wg]"s"(wg), [wave]"s"(wave), [ka]"s"(ka), [ldsb]"s"(ldsb)
  : "v0","v1","v2","v3","v4","v5","v6","v7","v8","v9","v10","v11","v12","v13","v14","v15","v16","v17","v18","v19","v20","v21","v22","v23","v24","v25","v26","v27","v28","v29","v30","v31","v32","v33","v34","v35","v36","v37","v38","v39","v40","v41","v42","v43","v44","v45","v46","v47","v48","v49","v50","v51","v52","v53","v54","v55","v56","v57","v58","v59","v60","v61","v62","v63","v64","v65","v66","v67","v68","v69","v70","v71","v72","v73","v74","v75","v76","v77","v78","v79","v80","v81","v82","v83","v84","v85","v86","v87","v88","v89","v90","v91","v92","v93","v94","v95","v96","v97","v98","v99","v100","v101","v102","v103","v104","v105","v106","v107","v108","v109","v110","v111","v112","v113","v114","v115","v116","v117","v118","v119","v120","v121","v122","v123","v124","v125","v126","v127","v128","v129","v130","v131","v132","v133","v134","v135","v136","v137","v138","v139","v140","v141","v142","v143","v144","v145","v146","v147","v148","v149","v150","v151","v152","v153","v154","v155","v156","v157","v158","v159","v160","v161","v162","v163","v164","v165","v166","v167","v168","v169","v170","v171","v172","v173","v174","v175","v176","v177","v178","v179","v180","v181","v182","v183","v184","v185","v186","v187","v188","v189","v190","v191","v192","v193","v194","v195","v196","v197","v198","v199","v200","v201","v202","v203","v204","v205","v206","v207","v208","v209","v210","v211","v212","v213","v214","v215","v216","v217","v218","v219","v220","v221","v222","v223","v224","v225","v226","v227","v228","v229","v230","v231","v232","v233","v234","v235","v236","v237","v238","v239","v240","v241","v242","v243","v244","v245","v246","v247","v248","v249","v250","v251","v252","v253","v254","v255","a0","a1","a2","a3","a4","a5","a6","a7","a8","a9","a10","a11","a12","a13","a14","a15","a16","a17","a18","a19","a20","a21","a22","a23","a24","a25","a26","a27","a28","a29","a30","a31","a32","a33","a34","a35","a36","a37","a38","a39","a40","a41","a42","a43","a44","a45","a46","a47","a48","a49","a50","a51","a52","a53","a54","a55","a56","a57","a58","a59","a60","a61","a62","a63","a64","a65","a66","a67","a68","a69","a70","a71","a72","a73","a74","a75","a76","a77","a78","a79","a80","a81","a82","a83","a84","a85","a86","a87","a88","a89","a90","a91","a92","a93","a94","a95","a96","a97","a98","a99","a100","a101","a102","a103","a104","a105","a106","a107","a108","a109","a110","a111","a112","a113","a114","a115","a116","a117","a118","a119","a120","a121","a122","a123","a124","a125","a126","a127","a128","a129","a130","a131","a132","a133","a134","a135","a136","a137","a138","a139","a140","a141","a142","a143","a144","a145","a146","a147","a148","a149","a150","a151","a152","a153","a154","a155","a156","a157","a158","a159","a160","a161","a162","a163","a164","a165","a166","a167","a168","a169","a170","a171","a172","a173","a174","a175","a176","a177","a178","a179","a180","a181","a182","a183","a184","a185","a186","a187","a188","a189","a190","a191","a192","a193","a194","a195","a196","a197","a198","a199","a200","a201","a202","a203","a204","a205","a206","a207","a208","a209","a210","a211","a212","a213","a214","a215","a216","a217","a218","a219","a220","a221","a222","a223","a224","a225","a226","a227","a228","a229","a230","a231","a232","a233","a234","a235","a236","a237","a238","a239","a240","a241","a242","a243","a244","a245","a246","a247","a248","a249","a250","a251","a252","a253","a254","a255","s14","s15","s16","s17","s18","s19","s20","s21","s22","s23","s24","s25","s26","s27","s28","s29","s30","s31","s33","s34","s35","s36","s37","s38","s39","s40","s41","s42","s43","s44","s45","s46","s47","s48","s49","s50","s51","s52","s53","s54","s55","s56","s57","s58","s59","s60","s61","s62","s63","s64","s65","s66","s67","s68","s69","s70","s71","s72","s73","s74","s75","s76","s77","s78","s79","s80","s81","s82","s83","s84","s85","s86","s87","s88","s89","s90","s91","s92","s93","s94","s95","s96","s97","vcc","scc","memory");
}

extern "C" void kernel_launch(void* const* d_in, const int* in_sizes, int n_in, void* d_out, int out_size, void* d_ws, size_t ws_size,
                              hipStream_t stream) {
  gqa_fused_kernel<<<256, 256, 0, stream>>>((const char*)d_in[0], (const char*)d_in[1], (const char*)d_in[2], (const char*)d_in[3], (const char*)d_in[4],
                                            (const char*)d_in[5], (const char*)d_in[6], (const char*)d_in[7], (const char*)d_in[8], (const char*)d_in[9], (char*)d_ws, (char*)d_out);
}
```
